```python
import torch

def _softmax(x, axis=-1):
    return torch.softmax(x, dim=axis)
def _gelu(x):
    return torch.nn.functional.gelu(x, approximate="tanh")
def _silu(x):
    return torch.nn.functional.silu(x)
def _sigmoid(x):
    return torch.sigmoid(x)
def _layernorm(x, axis=-1, eps=1e-5):
    m = x.mean(dim=axis, keepdim=True)
    v = x.var(dim=axis, keepdim=True, unbiased=False)
    return (x - m) / torch.sqrt(v + eps)
def _scatter_add(tgt, idx, src, axis=0):
    return tgt.clone().index_add_(axis, idx, src)
def _segment_sum(src, seg, n):
    out = torch.zeros((n,) + src.shape[1:], dtype=src.dtype)
    return out.index_add_(0, seg, src)
def _pad(x, pw):
    flat = tuple(v for pair in reversed(pw) for v in pair)
    return torch.nn.functional.pad(x, flat)

kernel_name = 'concat_heads__spectral_gfnet__ssm_conv_s4d__prompt'

def setup_inputs():

    _w = (lambda a: (lambda t: t.to(torch.bfloat16) if t.is_floating_point() else t)(torch.as_tensor(a)))
    def _A_setup():
        B, S, D = 1, 2048, 1024
        Df = D // 2 + 1
        return {
            "x": _w((lambda *s: torch.randn(*s, dtype=torch.bfloat16) * 0.05)(B, S, D)),
            "w_r": _w((lambda *s: torch.randn(*s, dtype=torch.bfloat16) * 0.8)(S, Df) * 0.02),
            "w_i": _w((lambda *s: torch.randn(*s, dtype=torch.bfloat16) * 0.8)(S, Df) * 0.02),
        }
    _A_in = _A_setup()

    def _B_setup():
        Bsz, L, D, N = 1, 2048, 1024, 128
        return {
            "u": _w((lambda *s: torch.randn(*s, dtype=torch.bfloat16) * 0.05)(Bsz, L, D)),
            "log_A_real": _w((lambda *s: torch.randn(*s, dtype=torch.bfloat16) * 6.4)(D, N) * 0.5),
            "A_imag": _w((lambda *s: torch.randn(*s, dtype=torch.bfloat16) * 0.05)(D, N) * 3.14),
            "C_real": _w((lambda *s: torch.randn(*s, dtype=torch.bfloat16) * 0.05)(D, N) / N ** 0.5),
            "C_imag": _w((lambda *s: torch.randn(*s, dtype=torch.bfloat16) * 0.05)(D, N) / N ** 0.5),
            "log_dt": _w((lambda *s: torch.randn(*s, dtype=torch.bfloat16) * 0.05)(D) * 0.5 - 4.0),
            "D_skip": _w((lambda *s: torch.randn(*s, dtype=torch.bfloat16) * 0.05)(D)),
        }
    _B_in = _B_setup()

    return {
        "A_x": _A_in["x"],
        "A_w_r": _A_in["w_r"],
        "A_w_i": _A_in["w_i"],
        "B_u": _B_in["u"],
        "B_log_A_real": _B_in["log_A_real"],
        "B_A_imag": _B_in["A_imag"],
        "B_C_real": _B_in["C_real"],
        "B_C_imag": _B_in["C_imag"],
        "B_log_dt": _B_in["log_dt"],
        "B_D_skip": _B_in["D_skip"],
    }

def reference(A_x, A_w_r, A_w_i, B_u, B_log_A_real, B_A_imag, B_C_real, B_C_imag, B_log_dt, B_D_skip):
    def _A_ref(x, w_r, w_i):
        B, S, D = x.shape
        X = (lambda x, axes=(-2,-1): torch.fft.rfft2(x.float(), dim=axes))(x, axes=(-2, -1))
        W = w_r + 1j * w_i
        X = X * W
        y = (lambda x, s, axes=(-2,-1): torch.fft.irfft2(x, s=s, dim=axes).to(torch.bfloat16))(X, (S, D), axes=(-2, -1))
        return y

    def _B_ref(u, log_A_real, A_imag, C_real, C_imag, log_dt, D_skip):
        L = u.shape[1]
        dt = torch.exp(log_dt)
        A = -torch.exp(log_A_real) + 1j * A_imag
        C = (C_real + 1j * C_imag) * dt[:, None]
        pos = (lambda x, dt: x.to(dt))(torch.arange(L), log_dt.dtype)
        P = torch.exp((A * dt[:, None])[:, :, None] * pos[None, None, :])
        K = 2.0 * torch.einsum("dn,dnl->dl", C, P).real
        L2 = 2 * L
        u_t = (lambda x, *d: x.permute(*d))(u, 0, 2, 1)
        Kf = (lambda x, *a, **k: torch.fft.rfft(x.float(), *a, **k))(K, L2)
        Uf = (lambda x, *a, **k: torch.fft.rfft(x.float(), *a, **k))(u_t, L2)
        y = (lambda x, *a, **k: torch.fft.irfft(x, *a, **k).to(torch.bfloat16))(Uf * Kf[None, :, :], L2)[:, :, :L]
        y = (lambda x, *d: x.permute(*d))(y, 0, 2, 1)
        return y + u * D_skip[None, None, :]

    a = _A_ref(A_x, A_w_r, A_w_i)
    b = _B_ref(B_u, B_log_A_real, B_A_imag, B_C_real, B_C_imag, B_log_dt, B_D_skip)
    return torch.concatenate([a, b], axis=-1)
```

```cpp
#if defined(__HIP_DEVICE_COMPILE__)
#pragma clang attribute push(__attribute__((target("no-packed-fp32-ops,no-fma-mix-insts"))), apply_to = function)
#endif
#include <hip/hip_runtime.h>
#include <hip/hip_bf16.h>
#include <stdint.h>

typedef _Float16 h8 __attribute__((ext_vector_type(8)));
typedef _Float16 h4 __attribute__((ext_vector_type(4)));
typedef _Float16 h2 __attribute__((ext_vector_type(2)));
typedef __bf16 b8 __attribute__((ext_vector_type(8)));
typedef float f4 __attribute__((ext_vector_type(4)));
typedef uint32_t u4 __attribute__((ext_vector_type(4)));
typedef uint32_t u2 __attribute__((ext_vector_type(2)));
typedef unsigned short us2 __attribute__((ext_vector_type(2)));

__device__ const uint32_t __attribute__((aligned(16))) TAB1[3584] = {
0x80003f80,0x80003f80,0x80003f80,0x80003f80,0x80003f80,0x3ec43f6d,0x3f353f35,0x3f6d3ec4,0x80003f80,0x3f353f35,0x3f800000,0x3f35bf35,0x80003f80,0x3f6d3ec4,0x3f35bf35,0xbec4bf6d,
0x80003f80,0x3f800000,0x8000bf80,0xbf800000,0x80003f80,0x3f6dbec4,0xbf35bf35,0xbec43f6d,0x80003f80,0x3f35bf35,0xbf800000,0x3f353f35,0x80003f80,0x3ec4bf6d,0xbf353f35,0x3f6dbec4,
0x80003f80,0x8000bf80,0x80003f80,0x8000bf80,0x80003f80,0xbec4bf6d,0x3f353f35,0xbf6dbec4,0x80003f80,0xbf35bf35,0x3f800000,0xbf353f35,0x80003f80,0xbf6dbec4,0x3f35bf35,0x3ec43f6d,
0x80003f80,0xbf800000,0x8000bf80,0x3f800000,0x80003f80,0xbf6d3ec4,0xbf35bf35,0x3ec4bf6d,0x80003f80,0xbf353f35,0xbf800000,0xbf35bf35,0x80003f80,0xbec43f6d,0xbf353f35,0xbf6d3ec4,
0x80003f80,0x80003f80,0x80003f80,0x80003f80,0x3f800000,0x3f6dbec4,0x3f35bf35,0x3ec4bf6d,0x8000bf80,0xbf35bf35,0xbf800000,0xbf353f35,0xbf800000,0xbec43f6d,0x3f353f35,0x3f6dbec4,
0x80003f80,0x3f800000,0x8000bf80,0xbf800000,0x3f800000,0xbec4bf6d,0xbf353f35,0x3f6d3ec4,0x8000bf80,0xbf353f35,0x3f800000,0xbf35bf35,0xbf800000,0x3f6d3ec4,0xbf35bf35,0x3ec43f6d,
0x80003f80,0x8000bf80,0x80003f80,0x8000bf80,0x3f800000,0xbf6d3ec4,0x3f35bf35,0xbec43f6d,0x8000bf80,0x3f353f35,0xbf800000,0x3f35bf35,0xbf800000,0x3ec4bf6d,0x3f353f35,0xbf6d3ec4,
0x80003f80,0xbf800000,0x8000bf80,0x3f800000,0x3f800000,0x3ec43f6d,0xbf353f35,0xbf6dbec4,0x8000bf80,0x3f35bf35,0x3f800000,0x3f353f35,0xbf800000,0xbf6dbec4,0xbf35bf35,0xbec4bf6d,
0x80003f80,0x80003f80,0x80003f80,0x80003f80,0x8000bf80,0xbec4bf6d,0xbf35bf35,0xbf6dbec4,0x80003f80,0x3f353f35,0x3f800000,0x3f35bf35,0x8000bf80,0xbf6dbec4,0xbf353f35,0x3ec43f6d,
0x80003f80,0x3f800000,0x8000bf80,0xbf800000,0x8000bf80,0xbf6d3ec4,0x3f353f35,0x3ec4bf6d,0x80003f80,0x3f35bf35,0xbf800000,0x3f353f35,0x8000bf80,0xbec43f6d,0x3f35bf35,0xbf6d3ec4,
0x80003f80,0x8000bf80,0x80003f80,0x8000bf80,0x8000bf80,0x3ec43f6d,0xbf35bf35,0x3f6d3ec4,0x80003f80,0xbf35bf35,0x3f800000,0xbf353f35,0x8000bf80,0x3f6d3ec4,0xbf353f35,0xbec4bf6d,
0x80003f80,0xbf800000,0x8000bf80,0x3f800000,0x8000bf80,0x3f6dbec4,0x3f353f35,0xbec43f6d,0x80003f80,0xbf353f35,0xbf800000,0xbf35bf35,0x8000bf80,0x3ec4bf6d,0x3f35bf35,0x3f6dbec4,
0x80003f80,0x80003f80,0x80003f80,0x80003f80,0xbf800000,0xbf6d3ec4,0xbf353f35,0xbec43f6d,0x8000bf80,0xbf35bf35,0xbf800000,0xbf353f35,0x3f800000,0x3ec4bf6d,0xbf35bf35,0xbf6d3ec4,
0x80003f80,0x3f800000,0x8000bf80,0xbf800000,0xbf800000,0x3ec43f6d,0x3f35bf35,0xbf6dbec4,0x8000bf80,0xbf353f35,0x3f800000,0xbf35bf35,0x3f800000,0xbf6dbec4,0x3f353f35,0xbec4bf6d,
0x80003f80,0x8000bf80,0x80003f80,0x8000bf80,0xbf800000,0x3f6dbec4,0xbf353f35,0x3ec4bf6d,0x8000bf80,0x3f353f35,0xbf800000,0x3f35bf35,0x3f800000,0xbec43f6d,0xbf35bf35,0x3f6dbec4,
0x80003f80,0xbf800000,0x8000bf80,0x3f800000,0xbf800000,0xbec4bf6d,0x3f35bf35,0x3f6d3ec4,0x8000bf80,0x3f35bf35,0x3f800000,0x3f353f35,0x3f800000,0x3f6d3ec4,0x3f353f35,0x3ec43f6d,
0x0,0x0,0x0,0x0,0x0,0xb907baf9,0x389e389e,0xbaf9b907,0x0,0x389e389e,0x0,0x389eb89e,0x0,0xbaf9b907,0x389eb89e,0x39073af9,
0x0,0x0,0x0,0x0,0x0,0xbaf93907,0xb89eb89e,0x3907baf9,0x0,0x389eb89e,0x0,0x389e389e,0x0,0xb9073af9,0xb89e389e,0xbaf93907,
0x0,0x0,0x0,0x0,0x0,0x39073af9,0x389e389e,0x3af93907,0x0,0xb89eb89e,0x0,0xb89e389e,0x0,0x3af93907,0x389eb89e,0xb907baf9,
0x0,0x0,0x0,0x0,0x0,0x3af9b907,0xb89eb89e,0xb9073af9,0x0,0xb89e389e,0x0,0xb89eb89e,0x0,0x3907baf9,0xb89e389e,0x3af9b907,
0x0,0x0,0x0,0x0,0x0,0xbaf93907,0x389eb89e,0xb9073af9,0x0,0xb89eb89e,0x0,0xb89e389e,0x0,0x3907baf9,0x389e389e,0xbaf93907,
0x0,0x0,0x0,0x0,0x0,0x39073af9,0xb89e389e,0xbaf9b907,0x0,0xb89e389e,0x0,0xb89eb89e,0x0,0xbaf9b907,0xb89eb89e,0xb907baf9,
0x0,0x0,0x0,0x0,0x0,0x3af9b907,0x389eb89e,0x3907baf9,0x0,0x389e389e,0x0,0x389eb89e,0x0,0xb9073af9,0x389e389e,0x3af9b907,
0x0,0x0,0x0,0x0,0x0,0xb907baf9,0xb89e389e,0x3af93907,0x0,0x389eb89e,0x0,0x389e389e,0x0,0x3af93907,0xb89eb89e,0x39073af9,
0x0,0x0,0x0,0x0,0x0,0x39073af9,0xb89eb89e,0x3af93907,0x0,0x389e389e,0x0,0x389eb89e,0x0,0x3af93907,0xb89e389e,0xb907baf9,
0x0,0x0,0x0,0x0,0x0,0x3af9b907,0x389e389e,0xb9073af9,0x0,0x389eb89e,0x0,0x389e389e,0x0,0x3907baf9,0x389eb89e,0x3af9b907,
0x0,0x0,0x0,0x0,0x0,0xb907baf9,0xb89eb89e,0xbaf9b907,0x0,0xb89eb89e,0x0,0xb89e389e,0x0,0xbaf9b907,0xb89e389e,0x39073af9,
0x0,0x0,0x0,0x0,0x0,0xbaf93907,0x389e389e,0x3907baf9,0x0,0xb89e389e,0x0,0xb89eb89e,0x0,0xb9073af9,0x389eb89e,0xbaf93907,
0x0,0x0,0x0,0x0,0x0,0x3af9b907,0xb89e389e,0x3907baf9,0x0,0xb89eb89e,0x0,0xb89e389e,0x0,0xb9073af9,0xb89eb89e,0x3af9b907,
0x0,0x0,0x0,0x0,0x0,0xb907baf9,0x389eb89e,0x3af93907,0x0,0xb89e389e,0x0,0xb89eb89e,0x0,0x3af93907,0x389e389e,0x39073af9,
0x0,0x0,0x0,0x0,0x0,0xbaf93907,0xb89e389e,0xb9073af9,0x0,0x389e389e,0x0,0x389eb89e,0x0,0x3907baf9,0xb89eb89e,0xbaf93907,
0x0,0x0,0x0,0x0,0x0,0x39073af9,0x389eb89e,0xbaf9b907,0x0,0x389eb89e,0x0,0x389e389e,0x0,0xbaf9b907,0x389e389e,0xb907baf9,
0x3f800000,0x3f800000,0x3f800000,0x3f800000,0x3f800000,0x3f6dbec4,0x3f35bf35,0x3ec4bf6d,0x3f800000,0x3f35bf35,0xbf80,0xbf35bf35,0x3f800000,0x3ec4bf6d,0xbf35bf35,0xbf6d3ec4,
0x3f800000,0xbf80,0xbf800000,0x3f80,0x3f800000,0xbec4bf6d,0xbf353f35,0x3f6d3ec4,0x3f800000,0xbf35bf35,0x3f80,0x3f35bf35,0x3f800000,0xbf6dbec4,0x3f353f35,0xbec4bf6d,
0x3f800000,0xbf800000,0x3f800000,0xbf800000,0x3f800000,0xbf6d3ec4,0x3f35bf35,0xbec43f6d,0x3f800000,0xbf353f35,0xbf80,0x3f353f35,0x3f800000,0xbec43f6d,0xbf35bf35,0x3f6dbec4,
0x3f800000,0x3f80,0xbf800000,0xbf80,0x3f800000,0x3ec43f6d,0xbf353f35,0xbf6dbec4,0x3f800000,0x3f353f35,0x3f80,0xbf353f35,0x3f800000,0x3f6d3ec4,0x3f353f35,0x3ec43f6d,
0x3f800000,0x3f800000,0x3f800000,0x3f800000,0xbf80,0xbec4bf6d,0xbf35bf35,0xbf6dbec4,0xbf800000,0xbf353f35,0x3f80,0x3f353f35,0x3f80,0x3f6d3ec4,0x3f35bf35,0xbec4bf6d,
0x3f800000,0xbf80,0xbf800000,0x3f80,0xbf80,0xbf6d3ec4,0x3f353f35,0x3ec4bf6d,0xbf800000,0x3f353f35,0xbf80,0xbf353f35,0x3f80,0x3ec4bf6d,0xbf353f35,0x3f6dbec4,
0x3f800000,0xbf800000,0x3f800000,0xbf800000,0xbf80,0x3ec43f6d,0xbf35bf35,0x3f6d3ec4,0xbf800000,0x3f35bf35,0x3f80,0xbf35bf35,0x3f80,0xbf6dbec4,0x3f35bf35,0x3ec43f6d,
0x3f800000,0x3f80,0xbf800000,0xbf80,0xbf80,0x3f6dbec4,0x3f353f35,0xbec43f6d,0xbf800000,0xbf35bf35,0xbf80,0x3f35bf35,0x3f80,0xbec43f6d,0xbf353f35,0xbf6d3ec4,
0x3f800000,0x3f800000,0x3f800000,0x3f800000,0xbf800000,0xbf6d3ec4,0xbf353f35,0xbec43f6d,0x3f800000,0x3f35bf35,0xbf80,0xbf35bf35,0xbf800000,0xbec43f6d,0x3f353f35,0x3f6dbec4,
0x3f800000,0xbf80,0xbf800000,0x3f80,0xbf800000,0x3ec43f6d,0x3f35bf35,0xbf6dbec4,0x3f800000,0xbf35bf35,0x3f80,0x3f35bf35,0xbf800000,0x3f6d3ec4,0xbf35bf35,0x3ec43f6d,
0x3f800000,0xbf800000,0x3f800000,0xbf800000,0xbf800000,0x3f6dbec4,0xbf353f35,0x3ec4bf6d,0x3f800000,0xbf353f35,0xbf80,0x3f353f35,0xbf800000,0x3ec4bf6d,0x3f353f35,0xbf6d3ec4,
0x3f800000,0x3f80,0xbf800000,0xbf80,0xbf800000,0xbec4bf6d,0x3f35bf35,0x3f6d3ec4,0x3f800000,0x3f353f35,0x3f80,0xbf353f35,0xbf800000,0xbf6dbec4,0xbf35bf35,0xbec4bf6d,
0x3f800000,0x3f800000,0x3f800000,0x3f800000,0x3f80,0x3ec43f6d,0x3f353f35,0x3f6d3ec4,0xbf800000,0xbf353f35,0x3f80,0x3f353f35,0xbf80,0xbf6dbec4,0xbf353f35,0x3ec43f6d,
0x3f800000,0xbf80,0xbf800000,0x3f80,0x3f80,0x3f6dbec4,0xbf35bf35,0xbec43f6d,0xbf800000,0x3f353f35,0xbf80,0xbf353f35,0xbf80,0xbec43f6d,0x3f35bf35,0xbf6d3ec4,
0x3f800000,0xbf800000,0x3f800000,0xbf800000,0x3f80,0xbec4bf6d,0x3f353f35,0xbf6dbec4,0xbf800000,0x3f35bf35,0x3f80,0xbf35bf35,0xbf80,0x3f6d3ec4,0xbf353f35,0xbec4bf6d,
0x3f800000,0x3f80,0xbf800000,0xbf80,0x3f80,0xbf6d3ec4,0xbf35bf35,0x3ec4bf6d,0xbf800000,0xbf35bf35,0xbf80,0x3f35bf35,0xbf80,0x3ec4bf6d,0x3f35bf35,0x3f6dbec4,
0x0,0x0,0x0,0x0,0x0,0xbaf93907,0x389eb89e,0xb9073af9,0x0,0x389eb89e,0x0,0xb89eb89e,0x0,0xb9073af9,0xb89eb89e,0x3af9b907,
0x0,0x0,0x0,0x0,0x0,0x39073af9,0xb89e389e,0xbaf9b907,0x0,0xb89eb89e,0x0,0x389eb89e,0x0,0x3af93907,0x389e389e,0x39073af9,
0x0,0x0,0x0,0x0,0x0,0x3af9b907,0x389eb89e,0x3907baf9,0x0,0xb89e389e,0x0,0x389e389e,0x0,0x3907baf9,0xb89eb89e,0xbaf93907,
0x0,0x0,0x0,0x0,0x0,0xb907baf9,0xb89e389e,0x3af93907,0x0,0x389e389e,0x0,0xb89e389e,0x0,0xbaf9b907,0x389e389e,0xb907baf9,
0x0,0x0,0x0,0x0,0x0,0x39073af9,0xb89eb89e,0x3af93907,0x0,0xb89e389e,0x0,0x389e389e,0x0,0xbaf9b907,0x389eb89e,0x39073af9,
0x0,0x0,0x0,0x0,0x0,0x3af9b907,0x389e389e,0xb9073af9,0x0,0x389e389e,0x0,0xb89e389e,0x0,0xb9073af9,0xb89e389e,0xbaf93907,
0x0,0x0,0x0,0x0,0x0,0xb907baf9,0xb89eb89e,0xbaf9b907,0x0,0x389eb89e,0x0,0xb89eb89e,0x0,0x3af93907,0x389eb89e,0xb907baf9,
0x0,0x0,0x0,0x0,0x0,0xbaf93907,0x389e389e,0x3907baf9,0x0,0xb89eb89e,0x0,0x389eb89e,0x0,0x3907baf9,0xb89e389e,0x3af9b907,
0x0,0x0,0x0,0x0,0x0,0x3af9b907,0xb89e389e,0x3907baf9,0x0,0x389eb89e,0x0,0xb89eb89e,0x0,0x3907baf9,0x389e389e,0xbaf93907,
0x0,0x0,0x0,0x0,0x0,0xb907baf9,0x389eb89e,0x3af93907,0x0,0xb89eb89e,0x0,0x389eb89e,0x0,0xbaf9b907,0xb89eb89e,0xb907baf9,
0x0,0x0,0x0,0x0,0x0,0xbaf93907,0xb89e389e,0xb9073af9,0x0,0xb89e389e,0x0,0x389e389e,0x0,0xb9073af9,0x389e389e,0x3af9b907,
0x0,0x0,0x0,0x0,0x0,0x39073af9,0x389eb89e,0xbaf9b907,0x0,0x389e389e,0x0,0xb89e389e,0x0,0x3af93907,0xb89eb89e,0x39073af9,
0x0,0x0,0x0,0x0,0x0,0xb907baf9,0x389e389e,0xbaf9b907,0x0,0xb89e389e,0x0,0x389e389e,0x0,0x3af93907,0xb89e389e,0xb907baf9,
0x0,0x0,0x0,0x0,0x0,0xbaf93907,0xb89eb89e,0x3907baf9,0x0,0x389e389e,0x0,0xb89e389e,0x0,0x3907baf9,0x389eb89e,0x3af9b907,
0x0,0x0,0x0,0x0,0x0,0x39073af9,0x389e389e,0x3af93907,0x0,0x389eb89e,0x0,0xb89eb89e,0x0,0xbaf9b907,0xb89e389e,0x39073af9,
0x0,0x0,0x0,0x0,0x0,0x3af9b907,0xb89eb89e,0xb9073af9,0x0,0xb89eb89e,0x0,0x389eb89e,0x0,0xb9073af9,0x389eb89e,0xbaf93907,
0x80002c00,0x80002c00,0x80002c00,0x80002c00,0x80002c00,0x261f2b64,0x29a829a8,0x2b64261f,0x80002c00,0x29a829a8,0x2c000000,0x29a8a9a8,0x80002c00,0x2b64261f,0x29a8a9a8,0xa61fab64,
0x80002c00,0x2c000000,0x8000ac00,0xac000000,0x80002c00,0x2b64a61f,0xa9a8a9a8,0xa61f2b64,0x80002c00,0x29a8a9a8,0xac000000,0x29a829a8,0x80002c00,0x261fab64,0xa9a829a8,0x2b64a61f,
0x80002c00,0x8000ac00,0x80002c00,0x8000ac00,0x80002c00,0xa61fab64,0x29a829a8,0xab64a61f,0x80002c00,0xa9a8a9a8,0x2c000000,0xa9a829a8,0x80002c00,0xab64a61f,0x29a8a9a8,0x261f2b64,
0x80002c00,0xac000000,0x8000ac00,0x2c000000,0x80002c00,0xab64261f,0xa9a8a9a8,0x261fab64,0x80002c00,0xa9a829a8,0xac000000,0xa9a8a9a8,0x80002c00,0xa61f2b64,0xa9a829a8,0xab64261f,
0x80002c00,0x80002c00,0x80002c00,0x80002c00,0x2c000000,0x2b64a61f,0x29a8a9a8,0x261fab64,0x8000ac00,0xa9a8a9a8,0xac000000,0xa9a829a8,0xac000000,0xa61f2b64,0x29a829a8,0x2b64a61f,
0x80002c00,0x2c000000,0x8000ac00,0xac000000,0x2c000000,0xa61fab64,0xa9a829a8,0x2b64261f,0x8000ac00,0xa9a829a8,0x2c000000,0xa9a8a9a8,0xac000000,0x2b64261f,0xa9a8a9a8,0x261f2b64,
0x80002c00,0x8000ac00,0x80002c00,0x8000ac00,0x2c000000,0xab64261f,0x29a8a9a8,0xa61f2b64,0x8000ac00,0x29a829a8,0xac000000,0x29a8a9a8,0xac000000,0x261fab64,0x29a829a8,0xab64261f,
0x80002c00,0xac000000,0x8000ac00,0x2c000000,0x2c000000,0x261f2b64,0xa9a829a8,0xab64a61f,0x8000ac00,0x29a8a9a8,0x2c000000,0x29a829a8,0xac000000,0xab64a61f,0xa9a8a9a8,0xa61fab64,
0x80002c00,0x80002c00,0x80002c00,0x80002c00,0x8000ac00,0xa61fab64,0xa9a8a9a8,0xab64a61f,0x80002c00,0x29a829a8,0x2c000000,0x29a8a9a8,0x8000ac00,0xab64a61f,0xa9a829a8,0x261f2b64,
0x80002c00,0x2c000000,0x8000ac00,0xac000000,0x8000ac00,0xab64261f,0x29a829a8,0x261fab64,0x80002c00,0x29a8a9a8,0xac000000,0x29a829a8,0x8000ac00,0xa61f2b64,0x29a8a9a8,0xab64261f,
0x80002c00,0x8000ac00,0x80002c00,0x8000ac00,0x8000ac00,0x261f2b64,0xa9a8a9a8,0x2b64261f,0x80002c00,0xa9a8a9a8,0x2c000000,0xa9a829a8,0x8000ac00,0x2b64261f,0xa9a829a8,0xa61fab64,
0x80002c00,0xac000000,0x8000ac00,0x2c000000,0x8000ac00,0x2b64a61f,0x29a829a8,0xa61f2b64,0x80002c00,0xa9a829a8,0xac000000,0xa9a8a9a8,0x8000ac00,0x261fab64,0x29a8a9a8,0x2b64a61f,
0x80002c00,0x80002c00,0x80002c00,0x80002c00,0xac000000,0xab64261f,0xa9a829a8,0xa61f2b64,0x8000ac00,0xa9a8a9a8,0xac000000,0xa9a829a8,0x2c000000,0x261fab64,0xa9a8a9a8,0xab64261f,
0x80002c00,0x2c000000,0x8000ac00,0xac000000,0xac000000,0x261f2b64,0x29a8a9a8,0xab64a61f,0x8000ac00,0xa9a829a8,0x2c000000,0xa9a8a9a8,0x2c000000,0xab64a61f,0x29a829a8,0xa61fab64,
0x80002c00,0x8000ac00,0x80002c00,0x8000ac00,0xac000000,0x2b64a61f,0xa9a829a8,0x261fab64,0x8000ac00,0x29a829a8,0xac000000,0x29a8a9a8,0x2c000000,0xa61f2b64,0xa9a8a9a8,0x2b64a61f,
0x80002c00,0xac000000,0x8000ac00,0x2c000000,0xac000000,0xa61fab64,0x29a8a9a8,0x2b64261f,0x8000ac00,0x29a8a9a8,0x2c000000,0x29a829a8,0x2c000000,0x2b64261f,0x29a829a8,0x261f2b64,
0x2c000000,0x2c000000,0x2c000000,0x2c000000,0x2c000000,0x2b64a61f,0x29a8a9a8,0x261fab64,0x2c000000,0x29a8a9a8,0xac00,0xa9a8a9a8,0x2c000000,0x261fab64,0xa9a8a9a8,0xab64261f,
0x2c000000,0xac00,0xac000000,0x2c00,0x2c000000,0xa61fab64,0xa9a829a8,0x2b64261f,0x2c000000,0xa9a8a9a8,0x2c00,0x29a8a9a8,0x2c000000,0xab64a61f,0x29a829a8,0xa61fab64,
0x2c000000,0xac000000,0x2c000000,0xac000000,0x2c000000,0xab64261f,0x29a8a9a8,0xa61f2b64,0x2c000000,0xa9a829a8,0xac00,0x29a829a8,0x2c000000,0xa61f2b64,0xa9a8a9a8,0x2b64a61f,
0x2c000000,0x2c00,0xac000000,0xac00,0x2c000000,0x261f2b64,0xa9a829a8,0xab64a61f,0x2c000000,0x29a829a8,0x2c00,0xa9a829a8,0x2c000000,0x2b64261f,0x29a829a8,0x261f2b64,
0x2c000000,0x2c000000,0x2c000000,0x2c000000,0xac00,0xa61fab64,0xa9a8a9a8,0xab64a61f,0xac000000,0xa9a829a8,0x2c00,0x29a829a8,0x2c00,0x2b64261f,0x29a8a9a8,0xa61fab64,
0x2c000000,0xac00,0xac000000,0x2c00,0xac00,0xab64261f,0x29a829a8,0x261fab64,0xac000000,0x29a829a8,0xac00,0xa9a829a8,0x2c00,0x261fab64,0xa9a829a8,0x2b64a61f,
0x2c000000,0xac000000,0x2c000000,0xac000000,0xac00,0x261f2b64,0xa9a8a9a8,0x2b64261f,0xac000000,0x29a8a9a8,0x2c00,0xa9a8a9a8,0x2c00,0xab64a61f,0x29a8a9a8,0x261f2b64,
0x2c000000,0x2c00,0xac000000,0xac00,0xac00,0x2b64a61f,0x29a829a8,0xa61f2b64,0xac000000,0xa9a8a9a8,0xac00,0x29a8a9a8,0x2c00,0xa61f2b64,0xa9a829a8,0xab64261f,
0x2c000000,0x2c000000,0x2c000000,0x2c000000,0xac000000,0xab64261f,0xa9a829a8,0xa61f2b64,0x2c000000,0x29a8a9a8,0xac00,0xa9a8a9a8,0xac000000,0xa61f2b64,0x29a829a8,0x2b64a61f,
0x2c000000,0xac00,0xac000000,0x2c00,0xac000000,0x261f2b64,0x29a8a9a8,0xab64a61f,0x2c000000,0xa9a8a9a8,0x2c00,0x29a8a9a8,0xac000000,0x2b64261f,0xa9a8a9a8,0x261f2b64,
0x2c000000,0xac000000,0x2c000000,0xac000000,0xac000000,0x2b64a61f,0xa9a829a8,0x261fab64,0x2c000000,0xa9a829a8,0xac00,0x29a829a8,0xac000000,0x261fab64,0x29a829a8,0xab64261f,
0x2c000000,0x2c00,0xac000000,0xac00,0xac000000,0xa61fab64,0x29a8a9a8,0x2b64261f,0x2c000000,0x29a829a8,0x2c00,0xa9a829a8,0xac000000,0xab64a61f,0xa9a8a9a8,0xa61fab64,
0x2c000000,0x2c000000,0x2c000000,0x2c000000,0x2c00,0x261f2b64,0x29a829a8,0x2b64261f,0xac000000,0xa9a829a8,0x2c00,0x29a829a8,0xac00,0xab64a61f,0xa9a829a8,0x261f2b64,
0x2c000000,0xac00,0xac000000,0x2c00,0x2c00,0x2b64a61f,0xa9a8a9a8,0xa61f2b64,0xac000000,0x29a829a8,0xac00,0xa9a829a8,0xac00,0xa61f2b64,0x29a8a9a8,0xab64261f,
0x2c000000,0xac000000,0x2c000000,0xac000000,0x2c00,0xa61fab64,0x29a829a8,0xab64a61f,0xac000000,0x29a8a9a8,0x2c00,0xa9a8a9a8,0xac00,0x2b64261f,0xa9a829a8,0xa61fab64,
0x2c000000,0x2c00,0xac000000,0xac00,0x2c00,0xab64261f,0xa9a8a9a8,0x261fab64,0xac000000,0xa9a8a9a8,0xac00,0x29a8a9a8,0xac00,0x261fab64,0x29a8a9a8,0x2b64a61f,
0x80002c00,0x80002c00,0x80002c00,0x80002c00,0x223e2bd9,0x28722aa7,0x2aa72872,0x2bd9223e,0x261f2b64,0x2b64261f,0x2b64a61f,0x261fab64,0x28722aa7,0x2bd9a23e,0x223eabd9,0xaaa7a872,
0x29a829a8,0x29a8a9a8,0xa9a8a9a8,0xa9a829a8,0x2aa72872,0x223eabd9,0xabd9223e,0x28722aa7,0x2b64261f,0xa61fab64,0xa61f2b64,0x2b64a61f,0x2bd9223e,0xaaa7a872,0x28722aa7,0xa23eabd9,
0x2c000000,0xac000000,0x2c000000,0xac000000,0x2bd9a23e,0xaaa72872,0x2872aaa7,0xa23e2bd9,0x2b64a61f,0xa61f2b64,0xa61fab64,0x2b64261f,0x2aa7a872,0x223e2bd9,0xabd9a23e,0x2872aaa7,
0x29a8a9a8,0x29a829a8,0xa9a829a8,0xa9a8a9a8,0x2872aaa7,0x2bd9223e,0x223e2bd9,0xaaa72872,0x261fab64,0x2b64a61f,0x2b64261f,0x261f2b64,0x223eabd9,0x2872aaa7,0x2aa7a872,0x2bd9a23e,
0x80002c00,0x80002c00,0x80002c00,0x80002c00,0x2bd9a23e,0x2aa7a872,0x2872aaa7,0x223eabd9,0xa61fab64,0xab64a61f,0xab64261f,0xa61f2b64,0xaaa72872,0x223e2bd9,0x2bd9223e,0x2872aaa7,
0x29a829a8,0x29a8a9a8,0xa9a8a9a8,0xa9a829a8,0x2872aaa7,0xabd9a23e,0x223e2bd9,0x2aa7a872,0xab64a61f,0x261f2b64,0x261fab64,0xab64261f,0xa23e2bd9,0x2872aaa7,0xaaa72872,0x2bd9a23e,
0x2c000000,0xac000000,0x2c000000,0xac000000,0xa23eabd9,0x28722aa7,0xaaa7a872,0x2bd9223e,0xab64261f,0x261fab64,0x261f2b64,0xab64a61f,0x28722aa7,0xabd9223e,0x223eabd9,0x2aa72872,
0x29a8a9a8,0x29a829a8,0xa9a829a8,0xa9a8a9a8,0xaaa7a872,0x223eabd9,0x2bd9a23e,0x28722aa7,0xa61f2b64,0xab64261f,0xab64a61f,0xa61fab64,0x2bd9223e,0x2aa72872,0x28722aa7,0x223e2bd9,
0x80002c00,0x80002c00,0x80002c00,0x80002c00,0xa23eabd9,0xa872aaa7,0xaaa7a872,0xabd9a23e,0x261f2b64,0x2b64261f,0x2b64a61f,0x261fab64,0xa872aaa7,0xabd9223e,0xa23e2bd9,0x2aa72872,
0x29a829a8,0x29a8a9a8,0xa9a8a9a8,0xa9a829a8,0xaaa7a872,0xa23e2bd9,0x2bd9a23e,0xa872aaa7,0x2b64261f,0xa61fab64,0xa61f2b64,0x2b64a61f,0xabd9a23e,0x2aa72872,0xa872aaa7,0x223e2bd9,
0x2c000000,0xac000000,0x2c000000,0xac000000,0xabd9223e,0x2aa7a872,0xa8722aa7,0x223eabd9,0x2b64a61f,0xa61f2b64,0xa61fab64,0x2b64261f,0xaaa72872,0xa23eabd9,0x2bd9223e,0xa8722aa7,
0x29a8a9a8,0x29a829a8,0xa9a829a8,0xa9a8a9a8,0xa8722aa7,0xabd9a23e,0xa23eabd9,0x2aa7a872,0x261fab64,0x2b64a61f,0x2b64261f,0x261f2b64,0xa23e2bd9,0xa8722aa7,0xaaa72872,0xabd9223e,
0x80002c00,0x80002c00,0x80002c00,0x80002c00,0xabd9223e,0xaaa72872,0xa8722aa7,0xa23e2bd9,0xa61fab64,0xab64a61f,0xab64261f,0xa61f2b64,0x2aa7a872,0xa23eabd9,0xabd9a23e,0xa8722aa7,
0x29a829a8,0x29a8a9a8,0xa9a8a9a8,0xa9a829a8,0xa8722aa7,0x2bd9223e,0xa23eabd9,0xaaa72872,0xab64a61f,0x261f2b64,0x261fab64,0xab64261f,0x223eabd9,0xa8722aa7,0x2aa7a872,0xabd9223e,
0x2c000000,0xac000000,0x2c000000,0xac000000,0x223e2bd9,0xa872aaa7,0x2aa72872,0xabd9a23e,0xab64261f,0x261fab64,0x261f2b64,0xab64a61f,0xa872aaa7,0x2bd9a23e,0xa23e2bd9,0xaaa7a872,
0x29a8a9a8,0x29a829a8,0xa9a829a8,0xa9a8a9a8,0x2aa72872,0xa23e2bd9,0xabd9223e,0xa872aaa7,0xa61f2b64,0xab64261f,0xab64a61f,0xa61fab64,0xabd9a23e,0xaaa7a872,0xa872aaa7,0xa23eabd9,
0x2c000000,0x2c000000,0x2c000000,0x2c000000,0x2bd9a23e,0x2aa7a872,0x2872aaa7,0x223eabd9,0x2b64a61f,0x261fab64,0xa61fab64,0xab64a61f,0x2aa7a872,0xa23eabd9,0xabd9a23e,0xa8722aa7,
0x29a8a9a8,0xa9a8a9a8,0xa9a829a8,0x29a829a8,0x2872aaa7,0xabd9a23e,0x223e2bd9,0x2aa7a872,0x261fab64,0xab64261f,0x2b64261f,0xa61fab64,0x223eabd9,0xa8722aa7,0x2aa7a872,0xabd9223e,
0xac00,0x2c00,0xac00,0x2c00,0xa23eabd9,0x28722aa7,0xaaa7a872,0x2bd9223e,0xa61fab64,0x2b64261f,0xab64261f,0x261fab64,0xa872aaa7,0x2bd9a23e,0xa23e2bd9,0xaaa7a872,
0xa9a8a9a8,0x29a8a9a8,0x29a829a8,0xa9a829a8,0xaaa7a872,0x223eabd9,0x2bd9a23e,0x28722aa7,0xab64a61f,0xa61fab64,0x261fab64,0x2b64a61f,0xabd9a23e,0xaaa7a872,0xa872aaa7,0xa23eabd9,
0x2c000000,0x2c000000,0x2c000000,0x2c000000,0xa23eabd9,0xa872aaa7,0xaaa7a872,0xabd9a23e,0xab64261f,0xa61f2b64,0x261f2b64,0x2b64261f,0x28722aa7,0x2bd9a23e,0x223eabd9,0xaaa7a872,
0x29a8a9a8,0xa9a8a9a8,0xa9a829a8,0x29a829a8,0xaaa7a872,0xa23e2bd9,0x2bd9a23e,0xa872aaa7,0xa61f2b64,0x2b64a61f,0xab64a61f,0x261f2b64,0x2bd9223e,0xaaa7a872,0x28722aa7,0xa23eabd9,
0xac00,0x2c00,0xac00,0x2c00,0xabd9223e,0x2aa7a872,0xa8722aa7,0x223eabd9,0x261f2b64,0xab64a61f,0x2b64a61f,0xa61f2b64,0x2aa7a872,0x223e2bd9,0xabd9a23e,0x2872aaa7,
0xa9a8a9a8,0x29a8a9a8,0x29a829a8,0xa9a829a8,0xa8722aa7,0xabd9a23e,0xa23eabd9,0x2aa7a872,0x2b64261f,0x261f2b64,0xa61f2b64,0xab64261f,0x223eabd9,0x2872aaa7,0x2aa7a872,0x2bd9a23e,
0x2c000000,0x2c000000,0x2c000000,0x2c000000,0xabd9223e,0xaaa72872,0xa8722aa7,0xa23e2bd9,0x2b64a61f,0x261fab64,0xa61fab64,0xab64a61f,0xaaa72872,0x223e2bd9,0x2bd9223e,0x2872aaa7,
0x29a8a9a8,0xa9a8a9a8,0xa9a829a8,0x29a829a8,0xa8722aa7,0x2bd9223e,0xa23eabd9,0xaaa72872,0x261fab64,0xab64261f,0x2b64261f,0xa61fab64,0xa23e2bd9,0x2872aaa7,0xaaa72872,0x2bd9a23e,
0xac00,0x2c00,0xac00,0x2c00,0x223e2bd9,0xa872aaa7,0x2aa72872,0xabd9a23e,0xa61fab64,0x2b64261f,0xab64261f,0x261fab64,0x28722aa7,0xabd9223e,0x223eabd9,0x2aa72872,
0xa9a8a9a8,0x29a8a9a8,0x29a829a8,0xa9a829a8,0x2aa72872,0xa23e2bd9,0xabd9223e,0xa872aaa7,0xab64a61f,0xa61fab64,0x261fab64,0x2b64a61f,0x2bd9223e,0x2aa72872,0x28722aa7,0x223e2bd9,
0x2c000000,0x2c000000,0x2c000000,0x2c000000,0x223e2bd9,0x28722aa7,0x2aa72872,0x2bd9223e,0xab64261f,0xa61f2b64,0x261f2b64,0x2b64261f,0xa872aaa7,0xabd9223e,0xa23e2bd9,0x2aa72872,
0x29a8a9a8,0xa9a8a9a8,0xa9a829a8,0x29a829a8,0x2aa72872,0x223eabd9,0xabd9223e,0x28722aa7,0xa61f2b64,0x2b64a61f,0xab64a61f,0x261f2b64,0xabd9a23e,0x2aa72872,0xa872aaa7,0x223e2bd9,
0xac00,0x2c00,0xac00,0x2c00,0x2bd9a23e,0xaaa72872,0x2872aaa7,0xa23e2bd9,0x261f2b64,0xab64a61f,0x2b64a61f,0xa61f2b64,0xaaa72872,0xa23eabd9,0x2bd9223e,0xa8722aa7,
0xa9a8a9a8,0x29a8a9a8,0x29a829a8,0xa9a829a8,0x2872aaa7,0x2bd9223e,0x223e2bd9,0xaaa72872,0x2b64261f,0x261f2b64,0xa61f2b64,0xab64261f,0xa23e2bd9,0xa8722aa7,0xaaa72872,0xabd9223e,
0x80003c00,0x80003c00,0x80003c00,0x80003c00,0x3c000000,0x3c000000,0x3c000000,0x3c000000,0x80003c00,0x8000bc00,0x80003c00,0x8000bc00,0x3c000000,0xbc000000,0x3c000000,0xbc000000,
0x80003c00,0x80003c00,0x39a839a8,0x39a839a8,0x3c000000,0x3c000000,0x39a8b9a8,0x39a8b9a8,0x80003c00,0x8000bc00,0x39a839a8,0xb9a8b9a8,0x3c000000,0xbc000000,0x39a8b9a8,0xb9a839a8,
0x80003c00,0x80003c00,0x3c000000,0x3c000000,0x3c000000,0x3c000000,0xbc00,0xbc00,0x80003c00,0x8000bc00,0x3c000000,0xbc000000,0x3c000000,0xbc000000,0xbc00,0x3c00,
0x80003c00,0x80003c00,0x39a8b9a8,0x39a8b9a8,0x3c000000,0x3c000000,0xb9a8b9a8,0xb9a8b9a8,0x80003c00,0x8000bc00,0x39a8b9a8,0xb9a839a8,0x3c000000,0xbc000000,0xb9a8b9a8,0x39a839a8,
0x80003c00,0x80003c00,0x80003c00,0x80003c00,0x3c000000,0x3c000000,0x3c000000,0x3c000000,0x80003c00,0x8000bc00,0x80003c00,0x8000bc00,0x3c000000,0xbc000000,0x3c000000,0xbc000000,
0x3c000000,0x3c000000,0x39a8b9a8,0x39a8b9a8,0xbc00,0xbc00,0xb9a8b9a8,0xb9a8b9a8,0x3c000000,0xbc000000,0x39a8b9a8,0xb9a839a8,0xbc00,0x3c00,0xb9a8b9a8,0x39a839a8,
0x8000bc00,0x8000bc00,0xbc000000,0xbc000000,0xbc000000,0xbc000000,0x3c00,0x3c00,0x8000bc00,0x80003c00,0xbc000000,0x3c000000,0xbc000000,0x3c000000,0x3c00,0xbc00,
0xbc000000,0xbc000000,0x39a839a8,0x39a839a8,0x3c00,0x3c00,0x39a8b9a8,0x39a8b9a8,0xbc000000,0x3c000000,0x39a839a8,0xb9a8b9a8,0x3c00,0xbc00,0x39a8b9a8,0xb9a839a8,
0x80003c00,0x80003c00,0x80003c00,0x80003c00,0x3c000000,0x3c000000,0x3c000000,0x3c000000,0x80003c00,0x8000bc00,0x80003c00,0x8000bc00,0x3c000000,0xbc000000,0x3c000000,0xbc000000,
0x8000bc00,0x8000bc00,0xb9a8b9a8,0xb9a8b9a8,0xbc000000,0xbc000000,0xb9a839a8,0xb9a839a8,0x8000bc00,0x80003c00,0xb9a8b9a8,0x39a839a8,0xbc000000,0x3c000000,0xb9a839a8,0x39a8b9a8,
0x80003c00,0x80003c00,0x3c000000,0x3c000000,0x3c000000,0x3c000000,0xbc00,0xbc00,0x80003c00,0x8000bc00,0x3c000000,0xbc000000,0x3c000000,0xbc000000,0xbc00,0x3c00,
0x8000bc00,0x8000bc00,0xb9a839a8,0xb9a839a8,0xbc000000,0xbc000000,0x39a839a8,0x39a839a8,0x8000bc00,0x80003c00,0xb9a839a8,0x39a8b9a8,0xbc000000,0x3c000000,0x39a839a8,0xb9a8b9a8,
0x80003c00,0x80003c00,0x80003c00,0x80003c00,0x3c000000,0x3c000000,0x3c000000,0x3c000000,0x80003c00,0x8000bc00,0x80003c00,0x8000bc00,0x3c000000,0xbc000000,0x3c000000,0xbc000000,
0xbc000000,0xbc000000,0xb9a839a8,0xb9a839a8,0x3c00,0x3c00,0x39a839a8,0x39a839a8,0xbc000000,0x3c000000,0xb9a839a8,0x39a8b9a8,0x3c00,0xbc00,0x39a839a8,0xb9a8b9a8,
0x8000bc00,0x8000bc00,0xbc000000,0xbc000000,0xbc000000,0xbc000000,0x3c00,0x3c00,0x8000bc00,0x80003c00,0xbc000000,0x3c000000,0xbc000000,0x3c000000,0x3c00,0xbc00,
0x3c000000,0x3c000000,0xb9a8b9a8,0xb9a8b9a8,0xbc00,0xbc00,0xb9a839a8,0xb9a839a8,0x3c000000,0xbc000000,0xb9a8b9a8,0x39a839a8,0xbc00,0x3c00,0xb9a839a8,0x39a8b9a8,
0x80003c00,0x80003c00,0x8000bc00,0x8000bc00,0x3c000000,0x3c000000,0xbc000000,0xbc000000,0x80003c00,0x8000bc00,0x8000bc00,0x80003c00,0x3c000000,0xbc000000,0xbc000000,0x3c000000,
0x80003c00,0x80003c00,0xb9a8b9a8,0xb9a8b9a8,0x3c000000,0x3c000000,0xb9a839a8,0xb9a839a8,0x80003c00,0x8000bc00,0xb9a8b9a8,0x39a839a8,0x3c000000,0xbc000000,0xb9a839a8,0x39a8b9a8,
0x80003c00,0x80003c00,0xbc000000,0xbc000000,0x3c000000,0x3c000000,0x3c00,0x3c00,0x80003c00,0x8000bc00,0xbc000000,0x3c000000,0x3c000000,0xbc000000,0x3c00,0xbc00,
0x80003c00,0x80003c00,0xb9a839a8,0xb9a839a8,0x3c000000,0x3c000000,0x39a839a8,0x39a839a8,0x80003c00,0x8000bc00,0xb9a839a8,0x39a8b9a8,0x3c000000,0xbc000000,0x39a839a8,0xb9a8b9a8,
0x80003c00,0x80003c00,0x8000bc00,0x8000bc00,0x3c000000,0x3c000000,0xbc000000,0xbc000000,0x80003c00,0x8000bc00,0x8000bc00,0x80003c00,0x3c000000,0xbc000000,0xbc000000,0x3c000000,
0x3c000000,0x3c000000,0xb9a839a8,0xb9a839a8,0xbc00,0xbc00,0x39a839a8,0x39a839a8,0x3c000000,0xbc000000,0xb9a839a8,0x39a8b9a8,0xbc00,0x3c00,0x39a839a8,0xb9a8b9a8,
0x8000bc00,0x8000bc00,0x3c000000,0x3c000000,0xbc000000,0xbc000000,0xbc00,0xbc00,0x8000bc00,0x80003c00,0x3c000000,0xbc000000,0xbc000000,0x3c000000,0xbc00,0x3c00,
0xbc000000,0xbc000000,0xb9a8b9a8,0xb9a8b9a8,0x3c00,0x3c00,0xb9a839a8,0xb9a839a8,0xbc000000,0x3c000000,0xb9a8b9a8,0x39a839a8,0x3c00,0xbc00,0xb9a839a8,0x39a8b9a8,
0x80003c00,0x80003c00,0x8000bc00,0x8000bc00,0x3c000000,0x3c000000,0xbc000000,0xbc000000,0x80003c00,0x8000bc00,0x8000bc00,0x80003c00,0x3c000000,0xbc000000,0xbc000000,0x3c000000,
0x8000bc00,0x8000bc00,0x39a839a8,0x39a839a8,0xbc000000,0xbc000000,0x39a8b9a8,0x39a8b9a8,0x8000bc00,0x80003c00,0x39a839a8,0xb9a8b9a8,0xbc000000,0x3c000000,0x39a8b9a8,0xb9a839a8,
0x80003c00,0x80003c00,0xbc000000,0xbc000000,0x3c000000,0x3c000000,0x3c00,0x3c00,0x80003c00,0x8000bc00,0xbc000000,0x3c000000,0x3c000000,0xbc000000,0x3c00,0xbc00,
0x8000bc00,0x8000bc00,0x39a8b9a8,0x39a8b9a8,0xbc000000,0xbc000000,0xb9a8b9a8,0xb9a8b9a8,0x8000bc00,0x80003c00,0x39a8b9a8,0xb9a839a8,0xbc000000,0x3c000000,0xb9a8b9a8,0x39a839a8,
0x80003c00,0x80003c00,0x8000bc00,0x8000bc00,0x3c000000,0x3c000000,0xbc000000,0xbc000000,0x80003c00,0x8000bc00,0x8000bc00,0x80003c00,0x3c000000,0xbc000000,0xbc000000,0x3c000000,
0xbc000000,0xbc000000,0x39a8b9a8,0x39a8b9a8,0x3c00,0x3c00,0xb9a8b9a8,0xb9a8b9a8,0xbc000000,0x3c000000,0x39a8b9a8,0xb9a839a8,0x3c00,0xbc00,0xb9a8b9a8,0x39a839a8,
0x8000bc00,0x8000bc00,0x3c000000,0x3c000000,0xbc000000,0xbc000000,0xbc00,0xbc00,0x8000bc00,0x80003c00,0x3c000000,0xbc000000,0xbc000000,0x3c000000,0xbc00,0x3c00,
0x3c000000,0x3c000000,0x39a839a8,0x39a839a8,0xbc00,0xbc00,0x39a8b9a8,0x39a8b9a8,0x3c000000,0xbc000000,0x39a839a8,0xb9a8b9a8,0xbc00,0x3c00,0x39a8b9a8,0xb9a839a8,
0x3f800000,0x0,0x3f800000,0x0,0x3f800000,0x0,0x3f7fec43,0xbcc90ab0,0x3f800000,0x0,0x3f7fb10f,0xbd48fb30,0x3f800000,0x0,0x3f7f4e6d,0xbd96a905,
0x3f800000,0x0,0x3f7ec46d,0xbdc8bd36,0x3f800000,0x0,0x3f7e1324,0xbdfab273,0x3f800000,0x0,0x3f7d3aac,0xbe164083,0x3f800000,0x0,0x3f7c3b28,0xbe2f10a2,
0x3f800000,0x0,0x3f7b14be,0xbe47c5c2,0x3f800000,0x0,0x3f79c79d,0xbe605c13,0x3f800000,0x0,0x3f7853f8,0xbe78cfcc,0x3f800000,0x0,0x3f76ba07,0xbe888e93,
0x3f800000,0x0,0x3f74fa0b,0xbe94a031,0x3f800000,0x0,0x3f731447,0xbea09ae5,0x3f800000,0x0,0x3f710908,0xbeac7cd4,0x3f800000,0x0,0x3f6ed89e,0xbeb8442a,
0x3f800000,0x0,0x3f800000,0x0,0x3f7ec46d,0xbdc8bd36,0x3f7e1324,0xbdfab273,0x3f7b14be,0xbe47c5c2,0x3f7853f8,0xbe78cfcc,0x3f74fa0b,0xbe94a031,0x3f6ed89e,0xbeb8442a,
0x3f6c835e,0xbec3ef15,0x3f61c598,0xbef15aea,0x3f61c598,0xbef15aea,0x3f514d3d,0xbf13682a,0x3f54db31,0xbf0e39da,0x3f3daef9,0xbf2beb4a,0x3f45e403,0xbf226799,0x3f273656,0xbf41d870,
0x3f3504f3,0xbf3504f3,0x3f0e39da,0xbf54db31,0x3f226799,0xbf45e403,0x3ee63375,0xbf64aa59,0x3f0e39da,0xbf54db31,0x3eac7cd4,0xbf710908,0x3ef15aea,0xbf61c598,0x3e605c13,0xbf79c79d,
0x3ec3ef15,0xbf6c835e,0x3dc8bd36,0xbf7ec46d,0x3e94a031,0xbf74fa0b,0xbcc90ab0,0xbf7fec43,0x3e47c5c2,0xbf7b14be,0xbe164083,0xbf7d3aac,0x3dc8bd36,0xbf7ec46d,0xbe888e93,0xbf76ba07,
0x3f800000,0x0,0x3f800000,0x0,0x3f7b14be,0xbe47c5c2,0x3f79c79d,0xbe605c13,0x3f6c835e,0xbec3ef15,0x3f676bd8,0xbedae880,0x3f54db31,0xbf0e39da,0x3f49d112,0xbf1d7fd1,
0x3f3504f3,0xbf3504f3,0x3f226799,0xbf45e403,0x3f0e39da,0xbf54db31,0x3ee63375,0xbf64aa59,0x3ec3ef15,0xbf6c835e,0x3e78cfcc,0xbf7853f8,0x3e47c5c2,0xbf7b14be,0x3cc90ab0,0xbf7fec43,
0x0,0xbf800000,0xbe47c5c2,0xbf7b14be,0xbe47c5c2,0xbf7b14be,0xbecf7bca,0xbf6a09a7,0xbec3ef15,0xbf6c835e,0xbf187fc0,0xbf4d9f02,0xbf0e39da,0xbf54db31,0xbf41d870,0xbf273656,
0xbf3504f3,0xbf3504f3,0xbf61c598,0xbef15aea,0xbf54db31,0xbf0e39da,0xbf76ba07,0xbe888e93,0xbf6c835e,0xbec3ef15,0xbf7fb10f,0xbd48fb30,0xbf7b14be,0xbe47c5c2,0xbf7c3b28,0x3e2f10a2,
0x3f800000,0x0,0x3f800000,0x0,0x3f74fa0b,0xbe94a031,0x3f731447,0xbea09ae5,0x3f54db31,0xbf0e39da,0x3f4d9f02,0xbf187fc0,0x3f226799,0xbf45e403,0x3f13682a,0xbf514d3d,
0x3ec3ef15,0xbf6c835e,0x3e94a031,0xbf74fa0b,0x3dc8bd36,0xbf7ec46d,0xbcc90ab0,0xbf7fec43,0xbe47c5c2,0xbf7b14be,0xbeac7cd4,0xbf710908,0xbef15aea,0xbf61c598,0xbf1d7fd1,0xbf49d112,
0xbf3504f3,0xbf3504f3,0xbf54db31,0xbf0e39da,0xbf61c598,0xbef15aea,0xbf76ba07,0xbe888e93,0xbf7b14be,0xbe47c5c2,0xbf7fb10f,0x3d48fb30,0xbf7ec46d,0x3dc8bd36,0xbf6ed89e,0x3eb8442a,
0xbf6c835e,0x3ec3ef15,0xbf45e403,0x3f226799,0xbf45e403,0x3f226799,0xbf08f59b,0x3f584853,0xbf0e39da,0x3f54db31,0xbe78cfcc,0x3f7853f8,0xbe94a031,0x3f74fa0b,0x3d96a905,0x3f7f4e6d,
0x3f800000,0x0,0x3f800000,0x0,0x3f7fb10f,0xbd48fb30,0x3f7f4e6d,0xbd96a905,0x3f7ec46d,0xbdc8bd36,0x3f7d3aac,0xbe164083,0x3f7d3aac,0xbe164083,0x3f79c79d,0xbe605c13,
0x3f7b14be,0xbe47c5c2,0x3f74fa0b,0xbe94a031,0x3f7853f8,0xbe78cfcc,0x3f6ed89e,0xbeb8442a,0x3f74fa0b,0xbe94a031,0x3f676bd8,0xbedae880,0x3f710908,0xbeac7cd4,0x3f5ebe05,0xbefc5d27,
0x3f6c835e,0xbec3ef15,0x3f54db31,0xbf0e39da,0x3f676bd8,0xbedae880,0x3f49d112,0xbf1d7fd1,0x3f61c598,0xbef15aea,0x3f3daef9,0xbf2beb4a,0x3f5b941a,0xbf039c3d,0x3f3085bb,0xbf396842,
0x3f54db31,0xbf0e39da,0x3f226799,0xbf45e403,0x3f4d9f02,0xbf187fc0,0x3f13682a,0xbf514d3d,0x3f45e403,0xbf226799,0x3f039c3d,0xbf5b941a,0x3f3daef9,0xbf2beb4a,0x3ee63375,0xbf64aa59,
0x3f800000,0x0,0x3f800000,0x0,0x3f7d3aac,0xbe164083,0x3f7c3b28,0xbe2f10a2,0x3f74fa0b,0xbe94a031,0x3f710908,0xbeac7cd4,0x3f676bd8,0xbedae880,0x3f5ebe05,0xbefc5d27,
0x3f54db31,0xbf0e39da,0x3f45e403,0xbf226799,0x3f3daef9,0xbf2beb4a,0x3f273656,0xbf41d870,0x3f226799,0xbf45e403,0x3f039c3d,0xbf5b941a,0x3f039c3d,0xbf5b941a,0x3eb8442a,0xbf6ed89e,
0x3ec3ef15,0xbf6c835e,0x3e47c5c2,0xbf7b14be,0x3e78cfcc,0xbf7853f8,0x3cc90ab0,0xbf7fec43,0x3dc8bd36,0xbf7ec46d,0xbe164083,0xbf7d3aac,0xbd48fb30,0xbf7fb10f,0xbea09ae5,0xbf731447,
0xbe47c5c2,0xbf7b14be,0xbef15aea,0xbf61c598,0xbeac7cd4,0xbf710908,0xbf1d7fd1,0xbf49d112,0xbef15aea,0xbf61c598,0xbf3daef9,0xbf2beb4a,0xbf187fc0,0xbf4d9f02,0xbf584853,0xbf08f59b,
0x3f800000,0x0,0x3f800000,0x0,0x3f7853f8,0xbe78cfcc,0x3f76ba07,0xbe888e93,0x3f61c598,0xbef15aea,0x3f5b941a,0xbf039c3d,0x3f3daef9,0xbf2beb4a,0x3f3085bb,0xbf396842,
0x3f0e39da,0xbf54db31,0x3ef15aea,0xbf61c598,0x3eac7cd4,0xbf710908,0x3e605c13,0xbf79c79d,0x3dc8bd36,0xbf7ec46d,0xbd48fb30,0xbf7fb10f,0xbe164083,0xbf7d3aac,0xbea09ae5,0xbf731447,
0xbec3ef15,0xbf6c835e,0xbf0e39da,0xbf54db31,0xbf187fc0,0xbf4d9f02,0xbf41d870,0xbf273656,0xbf45e403,0xbf226799,0xbf676bd8,0xbedae880,0xbf676bd8,0xbedae880,0xbf7c3b28,0xbe2f10a2,
0xbf7b14be,0xbe47c5c2,0xbf7ec46d,0x3dc8bd36,0xbf7fb10f,0x3d48fb30,0xbf6ed89e,0x3eb8442a,0xbf74fa0b,0x3e94a031,0xbf4d9f02,0x3f187fc0,0xbf5b941a,0x3f039c3d,0xbf1d7fd1,0x3f49d112,
0x3f800000,0x0,0x3f800000,0x0,0x3f710908,0xbeac7cd4,0x3f6ed89e,0xbeb8442a,0x3f45e403,0xbf226799,0x3f3daef9,0xbf2beb4a,0x3f039c3d,0xbf5b941a,0x3ee63375,0xbf64aa59,
0x3e47c5c2,0xbf7b14be,0x3dc8bd36,0xbf7ec46d,0xbe164083,0xbf7d3aac,0xbe888e93,0xbf76ba07,0xbef15aea,0xbf61c598,0xbf187fc0,0xbf4d9f02,0xbf3daef9,0xbf2beb4a,0xbf584853,0xbf08f59b,
0xbf6c835e,0xbec3ef15,0xbf7b14be,0xbe47c5c2,0xbf7fb10f,0xbd48fb30,0xbf7c3b28,0x3e2f10a2,0xbf74fa0b,0x3e94a031,0xbf5b941a,0x3f039c3d,0xbf4d9f02,0x3f187fc0,0xbf1d7fd1,0x3f49d112,
0xbf0e39da,0x3f54db31,0xbe94a031,0x3f74fa0b,0xbe78cfcc,0x3f7853f8,0x3d96a905,0x3f7f4e6d,0x3dc8bd36,0x3f7ec46d,0x3edae880,0x3f676bd8,0x3edae880,0x3f676bd8,0x3f396842,0x3f3085bb,
0x3f800000,0x0,0x3f800000,0x0,0x3f7ffb11,0xbc490e90,0x3f7fd397,0xbd16c32c,0x3f7fec43,0xbcc90ab0,0x3f7f4e6d,0xbd96a905,0x3f7fd397,0xbd16c32c,0x3f7e70b0,0xbde1bc2e,
0x3f7fb10f,0xbd48fb30,0x3f7d3aac,0xbe164083,0x3f7f84ab,0xbd7b2b74,0x3f7baccd,0xbe3b6ecf,0x3f7f4e6d,0xbd96a905,0x3f79c79d,0xbe605c13,0x3f7f0e58,0xbdafb680,0x3f778bc5,0xbe827dc0,
0x3f7ec46d,0xbdc8bd36,0x3f74fa0b,0xbe94a031,0x3f7e70b0,0xbde1bc2e,0x3f721352,0xbea68f12,0x3f7e1324,0xbdfab273,0x3f6ed89e,0xbeb8442a,0x3f7dabcc,0xbe09cf86,0x3f6b4b0c,0xbec9b953,
0x3f7d3aac,0xbe164083,0x3f676bd8,0xbedae880,0x3f7cbfc9,0xbe22abb6,0x3f633c5a,0xbeebcbbb,0x3f7c3b28,0xbe2f10a2,0x3f5ebe05,0xbefc5d27,0x3f7baccd,0xbe3b6ecf,0x3f59f26a,0xbf064b82,
0x3f800000,0x0,0x3f800000,0x0,0x3f7e70b0,0xbde1bc2e,0x3f7dabcc,0xbe09cf86,0x3f79c79d,0xbe605c13,0x3f76ba07,0xbe888e93,0x3f721352,0xbea68f12,0x3f6b4b0c,0xbec9b953,
0x3f676bd8,0xbedae880,0x3f5b941a,0xbf039c3d,0x3f59f26a,0xbf064b82,0x3f47de65,0xbf1ff6cb,0x3f49d112,0xbf1d7fd1,0x3f3085bb,0xbf396842,0x3f373a23,0xbf32c8c9,0x3f15f6d9,0xbf4f7a1f,
0x3f226799,0xbf45e403,0x3ef15aea,0xbf61c598,0x3f0b9a6b,0xbf5695e5,0x3eb263ef,0xbf6ff573,0x3ee63375,0xbf64aa59,0x3e605c13,0xbf79c79d,0x3eb263ef,0xbf6ff573,0x3dafb680,0xbf7f0e58,
0x3e78cfcc,0xbf7853f8,0xbd48fb30,0xbf7fb10f,0x3e09cf86,0xbf7dabcc,0xbe3b6ecf,0xbf7baccd,0x3cc90ab0,0xbf7fec43,0xbea09ae5,0xbf731447,0xbdafb680,0xbf7f0e58,0xbee0924f,0xbf660f88,
0x3f800000,0x0,0x3f800000,0x0,0x3f7a7302,0xbe541501,0x3f791298,0xbe6c9a7f,0x3f6a09a7,0xbecf7bca,0x3f64aa59,0xbee63375,0x3f4f7a1f,0xbf15f6d9,0x3f43e200,0xbf24d225,
0x3f2beb4a,0xbf3daef9,0x3f187fc0,0xbf4d9f02,0x3f00e7e4,0xbf5d2d53,0x3ec9b953,0xbf6b4b0c,0x3ea09ae5,0xbf731447,0x3e2f10a2,0xbf7c3b28,0x3de1bc2e,0xbf7e70b0,0xbd7b2b74,0xbf7f84ab,
0xbdc8bd36,0xbf7ec46d,0xbe94a031,0xbf74fa0b,0xbe9aa086,0xbf740bdd,0xbf00e7e4,0xbf5d2d53,0xbefc5d27,0xbf5ebe05,0xbf3085bb,0xbf396842,0xbf299415,0xbf3fc767,0xbf5695e5,0xbf0b9a6b,
0xbf4d9f02,0xbf187fc0,0xbf710908,0xbeac7cd4,0xbf68bf3c,0xbed53641,0xbf7e70b0,0xbde1bc2e,0xbf79c79d,0xbe605c13,0xbf7e1324,0x3dfab273,0xbf7ffb11,0xbc490e90,0xbf6ff573,0x3eb263ef,
0x3f800000,0x0,0x3f800000,0x0,0x3f740bdd,0xbe9aa086,0x3f721352,0xbea68f12,0x3f514d3d,0xbf13682a,0x3f49d112,0xbf1d7fd1,0x3f1b02c6,0xbf4bbbf8,0x3f0b9a6b,0xbf5695e5,
0x3eac7cd4,0xbf710908,0x3e78cfcc,0xbf7853f8,0x3d16c32c,0xbf7fd397,0xbdafb680,0xbf7f0e58,0xbe888e93,0xbf76ba07,0xbecf7bca,0xbf6a09a7,0xbf0b9a6b,0xbf5695e5,0xbf2e3bde,0xbf3b8f3b,
0xbf45e403,0xbf226799,0xbf61c598,0xbef15aea,0xbf6db293,0xbebe1d4a,0xbf7cbfc9,0xbe22abb6,0xbf7f4e6d,0xbd96a905,0xbf7c3b28,0x3e2f10a2,0xbf791298,0x3e6c9a7f,0xbf604621,0x3ef6e0cb,
0xbf5b941a,0x3f039c3d,0xbf2beb4a,0x3f3daef9,0xbf299415,0x3f3fc767,0xbec9b953,0x3f6b4b0c,0xbecf7bca,0x3f6a09a7,0xbd96a905,0x3f7f4e6d,0xbde1bc2e,0x3f7e70b0,0x3e827dc0,0x3f778bc5,
0x3f800000,0x0,0x3f800000,0x0,0x3f7f84ab,0xbd7b2b74,0x3f7f0e58,0xbdafb680,0x3f7e1324,0xbdfab273,0x3f7c3b28,0xbe2f10a2,0x3f7baccd,0xbe3b6ecf,0x3f778bc5,0xbe827dc0,
0x3f7853f8,0xbe78cfcc,0x3f710908,0xbeac7cd4,0x3f740bdd,0xbe9aa086,0x3f68bf3c,0xbed53641,0x3f6ed89e,0xbeb8442a,0x3f5ebe05,0xbefc5d27,0x3f68bf3c,0xbed53641,0x3f531849,0xbf10d3cd,
0x3f61c598,0xbef15aea,0x3f45e403,0xbf226799,0x3f59f26a,0xbf064b82,0x3f373a23,0xbf32c8c9,0x3f514d3d,0xbf13682a,0x3f273656,0xbf41d870,0x3f47de65,0xbf1ff6cb,0x3f15f6d9,0xbf4f7a1f,
0x3f3daef9,0xbf2beb4a,0x3f039c3d,0xbf5b941a,0x3f32c8c9,0xbf373a23,0x3ee0924f,0xbf660f88,0x3f273656,0xbf41d870,0x3eb8442a,0xbf6ed89e,0x3f1b02c6,0xbf4bbbf8,0x3e8e9a22,0xbf75dec6,
0x3f800000,0x0,0x3f800000,0x0,0x3f7cbfc9,0xbe22abb6,0x3f7baccd,0xbe3b6ecf,0x3f731447,0xbea09ae5,0x3f6ed89e,0xbeb8442a,0x3f633c5a,0xbeebcbbb,0x3f59f26a,0xbf064b82,
0x3f4d9f02,0xbf187fc0,0x3f3daef9,0xbf2beb4a,0x3f32c8c9,0xbf373a23,0x3f1b02c6,0xbf4bbbf8,0x3f13682a,0xbf514d3d,0x3ee63375,0xbf64aa59,0x3ee0924f,0xbf660f88,0x3e8e9a22,0xbf75dec6,
0x3e94a031,0xbf74fa0b,0x3dc8bd36,0xbf7ec46d,0x3e09cf86,0xbf7dabcc,0xbdafb680,0xbf7f0e58,0xbcc90ab0,0xbf7fec43,0xbe888e93,0xbf76ba07,0xbe3b6ecf,0xbf7baccd,0xbee0924f,0xbf660f88,
0xbeac7cd4,0xbf710908,0xbf187fc0,0xbf4d9f02,0xbef6e0cb,0xbf604621,0xbf3b8f3b,0xbf2e3bde,0xbf1d7fd1,0xbf49d112,0xbf584853,0xbf08f59b,0xbf3b8f3b,0xbf2e3bde,0xbf6db293,0xbebe1d4a,
0x3f800000,0x0,0x3f800000,0x0,0x3f778bc5,0xbe827dc0,0x3f75dec6,0xbe8e9a22,0x3f5ebe05,0xbefc5d27,0x3f584853,0xbf08f59b,0x3f373a23,0xbf32c8c9,0x3f299415,0xbf3fc767,
0x3f039c3d,0xbf5b941a,0x3edae880,0xbf676bd8,0x3e8e9a22,0xbf75dec6,0x3e22abb6,0xbf7cbfc9,0x3cc90ab0,0xbf7fec43,0xbdfab273,0xbf7e1324,0xbe6c9a7f,0xbf791298,0xbec9b953,0xbf6b4b0c,
0xbef15aea,0xbf61c598,0xbf226799,0xbf45e403,0xbf2e3bde,0xbf3b8f3b,0xbf531849,0xbf10d3cd,0xbf584853,0xbf08f59b,0xbf731447,0xbea09ae5,0xbf740bdd,0xbe9aa086,0xbf7fd397,0xbd16c32c,
0xbf7fb10f,0xbd48fb30,0xbf7853f8,0x3e78cfcc,0xbf7a7302,0x3e541501,0xbf5d2d53,0x3f00e7e4,0xbf64aa59,0x3ee63375,0xbf3085bb,0x3f396842,0xbf3fc767,0x3f299415,0xbeebcbbb,0x3f633c5a,
0x3f800000,0x0,0x3f800000,0x0,0x3f6ff573,0xbeb263ef,0x3f6db293,0xbebe1d4a,0x3f41d870,0xbf273656,0x3f396842,0xbf3085bb,0x3ef6e0cb,0xbf604621,0x3ed53641,0xbf68bf3c,
0x3e164083,0xbf7d3aac,0x3d48fb30,0xbf7fb10f,0xbe541501,0xbf7a7302,0xbea68f12,0xbf721352,0xbf08f59b,0xbf584853,0xbf273656,0xbf41d870,0xbf4bbbf8,0xbf1b02c6,0xbf633c5a,0xbeebcbbb,
0xbf74fa0b,0xbe94a031,0xbf7ec46d,0xbdc8bd36,0xbf7f84ab,0x3d7b2b74,0xbf75dec6,0x3e8e9a22,0xbf6a09a7,0x3ecf7bca,0xbf49d112,0x3f1d7fd1,0xbf373a23,0x3f32c8c9,0xbf00e7e4,0x3f5d2d53,
0xbedae880,0x3f676bd8,0xbe164083,0x3f7d3aac,0xbdafb680,0x3f7f0e58,0x3e6c9a7f,0x3f791298,0x3e888e93,0x3f76ba07,0x3f13682a,0x3f514d3d,0x3f15f6d9,0x3f4f7a1f,0x3f5695e5,0x3f0b9a6b,
};
__device__ const uint32_t __attribute__((aligned(16))) TAB2[20480] = {
0x80002c00,0x80002c00,0x80002c00,0x80002c00,0x80002c00,0x261f2b64,0x29a829a8,0x2b64261f,0x80002c00,0x29a829a8,0x2c000000,0x29a8a9a8,0x80002c00,0x2b64261f,0x29a8a9a8,0xa61fab64,
0x80002c00,0x2c000000,0x8000ac00,0xac000000,0x80002c00,0x2b64a61f,0xa9a8a9a8,0xa61f2b64,0x80002c00,0x29a8a9a8,0xac000000,0x29a829a8,0x80002c00,0x261fab64,0xa9a829a8,0x2b64a61f,
0x80002c00,0x8000ac00,0x80002c00,0x8000ac00,0x80002c00,0xa61fab64,0x29a829a8,0xab64a61f,0x80002c00,0xa9a8a9a8,0x2c000000,0xa9a829a8,0x80002c00,0xab64a61f,0x29a8a9a8,0x261f2b64,
0x80002c00,0xac000000,0x8000ac00,0x2c000000,0x80002c00,0xab64261f,0xa9a8a9a8,0x261fab64,0x80002c00,0xa9a829a8,0xac000000,0xa9a8a9a8,0x80002c00,0xa61f2b64,0xa9a829a8,0xab64261f,
0x80002c00,0x80002c00,0x80002c00,0x80002c00,0x2c000000,0x2b64a61f,0x29a8a9a8,0x261fab64,0x8000ac00,0xa9a8a9a8,0xac000000,0xa9a829a8,0xac000000,0xa61f2b64,0x29a829a8,0x2b64a61f,
0x80002c00,0x2c000000,0x8000ac00,0xac000000,0x2c000000,0xa61fab64,0xa9a829a8,0x2b64261f,0x8000ac00,0xa9a829a8,0x2c000000,0xa9a8a9a8,0xac000000,0x2b64261f,0xa9a8a9a8,0x261f2b64,
0x80002c00,0x8000ac00,0x80002c00,0x8000ac00,0x2c000000,0xab64261f,0x29a8a9a8,0xa61f2b64,0x8000ac00,0x29a829a8,0xac000000,0x29a8a9a8,0xac000000,0x261fab64,0x29a829a8,0xab64261f,
0x80002c00,0xac000000,0x8000ac00,0x2c000000,0x2c000000,0x261f2b64,0xa9a829a8,0xab64a61f,0x8000ac00,0x29a8a9a8,0x2c000000,0x29a829a8,0xac000000,0xab64a61f,0xa9a8a9a8,0xa61fab64,
0x80002c00,0x80002c00,0x80002c00,0x80002c00,0x8000ac00,0xa61fab64,0xa9a8a9a8,0xab64a61f,0x80002c00,0x29a829a8,0x2c000000,0x29a8a9a8,0x8000ac00,0xab64a61f,0xa9a829a8,0x261f2b64,
0x80002c00,0x2c000000,0x8000ac00,0xac000000,0x8000ac00,0xab64261f,0x29a829a8,0x261fab64,0x80002c00,0x29a8a9a8,0xac000000,0x29a829a8,0x8000ac00,0xa61f2b64,0x29a8a9a8,0xab64261f,
0x80002c00,0x8000ac00,0x80002c00,0x8000ac00,0x8000ac00,0x261f2b64,0xa9a8a9a8,0x2b64261f,0x80002c00,0xa9a8a9a8,0x2c000000,0xa9a829a8,0x8000ac00,0x2b64261f,0xa9a829a8,0xa61fab64,
0x80002c00,0xac000000,0x8000ac00,0x2c000000,0x8000ac00,0x2b64a61f,0x29a829a8,0xa61f2b64,0x80002c00,0xa9a829a8,0xac000000,0xa9a8a9a8,0x8000ac00,0x261fab64,0x29a8a9a8,0x2b64a61f,
0x80002c00,0x80002c00,0x80002c00,0x80002c00,0xac000000,0xab64261f,0xa9a829a8,0xa61f2b64,0x8000ac00,0xa9a8a9a8,0xac000000,0xa9a829a8,0x2c000000,0x261fab64,0xa9a8a9a8,0xab64261f,
0x80002c00,0x2c000000,0x8000ac00,0xac000000,0xac000000,0x261f2b64,0x29a8a9a8,0xab64a61f,0x8000ac00,0xa9a829a8,0x2c000000,0xa9a8a9a8,0x2c000000,0xab64a61f,0x29a829a8,0xa61fab64,
0x80002c00,0x8000ac00,0x80002c00,0x8000ac00,0xac000000,0x2b64a61f,0xa9a829a8,0x261fab64,0x8000ac00,0x29a829a8,0xac000000,0x29a8a9a8,0x2c000000,0xa61f2b64,0xa9a8a9a8,0x2b64a61f,
0x80002c00,0xac000000,0x8000ac00,0x2c000000,0xac000000,0xa61fab64,0x29a8a9a8,0x2b64261f,0x8000ac00,0x29a8a9a8,0x2c000000,0x29a829a8,0x2c000000,0x2b64261f,0x29a829a8,0x261f2b64,
0x2c000000,0x2c000000,0x2c000000,0x2c000000,0x2c000000,0x2b64a61f,0x29a8a9a8,0x261fab64,0x2c000000,0x29a8a9a8,0xac00,0xa9a8a9a8,0x2c000000,0x261fab64,0xa9a8a9a8,0xab64261f,
0x2c000000,0xac00,0xac000000,0x2c00,0x2c000000,0xa61fab64,0xa9a829a8,0x2b64261f,0x2c000000,0xa9a8a9a8,0x2c00,0x29a8a9a8,0x2c000000,0xab64a61f,0x29a829a8,0xa61fab64,
0x2c000000,0xac000000,0x2c000000,0xac000000,0x2c000000,0xab64261f,0x29a8a9a8,0xa61f2b64,0x2c000000,0xa9a829a8,0xac00,0x29a829a8,0x2c000000,0xa61f2b64,0xa9a8a9a8,0x2b64a61f,
0x2c000000,0x2c00,0xac000000,0xac00,0x2c000000,0x261f2b64,0xa9a829a8,0xab64a61f,0x2c000000,0x29a829a8,0x2c00,0xa9a829a8,0x2c000000,0x2b64261f,0x29a829a8,0x261f2b64,
0x2c000000,0x2c000000,0x2c000000,0x2c000000,0xac00,0xa61fab64,0xa9a8a9a8,0xab64a61f,0xac000000,0xa9a829a8,0x2c00,0x29a829a8,0x2c00,0x2b64261f,0x29a8a9a8,0xa61fab64,
0x2c000000,0xac00,0xac000000,0x2c00,0xac00,0xab64261f,0x29a829a8,0x261fab64,0xac000000,0x29a829a8,0xac00,0xa9a829a8,0x2c00,0x261fab64,0xa9a829a8,0x2b64a61f,
0x2c000000,0xac000000,0x2c000000,0xac000000,0xac00,0x261f2b64,0xa9a8a9a8,0x2b64261f,0xac000000,0x29a8a9a8,0x2c00,0xa9a8a9a8,0x2c00,0xab64a61f,0x29a8a9a8,0x261f2b64,
0x2c000000,0x2c00,0xac000000,0xac00,0xac00,0x2b64a61f,0x29a829a8,0xa61f2b64,0xac000000,0xa9a8a9a8,0xac00,0x29a8a9a8,0x2c00,0xa61f2b64,0xa9a829a8,0xab64261f,
0x2c000000,0x2c000000,0x2c000000,0x2c000000,0xac000000,0xab64261f,0xa9a829a8,0xa61f2b64,0x2c000000,0x29a8a9a8,0xac00,0xa9a8a9a8,0xac000000,0xa61f2b64,0x29a829a8,0x2b64a61f,
0x2c000000,0xac00,0xac000000,0x2c00,0xac000000,0x261f2b64,0x29a8a9a8,0xab64a61f,0x2c000000,0xa9a8a9a8,0x2c00,0x29a8a9a8,0xac000000,0x2b64261f,0xa9a8a9a8,0x261f2b64,
0x2c000000,0xac000000,0x2c000000,0xac000000,0xac000000,0x2b64a61f,0xa9a829a8,0x261fab64,0x2c000000,0xa9a829a8,0xac00,0x29a829a8,0xac000000,0x261fab64,0x29a829a8,0xab64261f,
0x2c000000,0x2c00,0xac000000,0xac00,0xac000000,0xa61fab64,0x29a8a9a8,0x2b64261f,0x2c000000,0x29a829a8,0x2c00,0xa9a829a8,0xac000000,0xab64a61f,0xa9a8a9a8,0xa61fab64,
0x2c000000,0x2c000000,0x2c000000,0x2c000000,0x2c00,0x261f2b64,0x29a829a8,0x2b64261f,0xac000000,0xa9a829a8,0x2c00,0x29a829a8,0xac00,0xab64a61f,0xa9a829a8,0x261f2b64,
0x2c000000,0xac00,0xac000000,0x2c00,0x2c00,0x2b64a61f,0xa9a8a9a8,0xa61f2b64,0xac000000,0x29a829a8,0xac00,0xa9a829a8,0xac00,0xa61f2b64,0x29a8a9a8,0xab64261f,
0x2c000000,0xac000000,0x2c000000,0xac000000,0x2c00,0xa61fab64,0x29a829a8,0xab64a61f,0xac000000,0x29a8a9a8,0x2c00,0xa9a8a9a8,0xac00,0x2b64261f,0xa9a829a8,0xa61fab64,
0x2c000000,0x2c00,0xac000000,0xac00,0x2c00,0xab64261f,0xa9a8a9a8,0x261fab64,0xac000000,0xa9a8a9a8,0xac00,0x29a8a9a8,0xac00,0x261fab64,0x29a8a9a8,0x2b64a61f,
0x80002c00,0x80002c00,0x80002c00,0x80002c00,0x80002c00,0x261f2b64,0x29a829a8,0x2b64261f,0x80002c00,0x29a829a8,0x2c000000,0x29a8a9a8,0x80002c00,0x2b64261f,0x29a8a9a8,0xa61fab64,
0x80002c00,0x2c000000,0x8000ac00,0xac000000,0x80002c00,0x2b64a61f,0xa9a8a9a8,0xa61f2b64,0x80002c00,0x29a8a9a8,0xac000000,0x29a829a8,0x80002c00,0x261fab64,0xa9a829a8,0x2b64a61f,
0x80002c00,0x8000ac00,0x80002c00,0x8000ac00,0x80002c00,0xa61fab64,0x29a829a8,0xab64a61f,0x80002c00,0xa9a8a9a8,0x2c000000,0xa9a829a8,0x80002c00,0xab64a61f,0x29a8a9a8,0x261f2b64,
0x80002c00,0xac000000,0x8000ac00,0x2c000000,0x80002c00,0xab64261f,0xa9a8a9a8,0x261fab64,0x80002c00,0xa9a829a8,0xac000000,0xa9a8a9a8,0x80002c00,0xa61f2b64,0xa9a829a8,0xab64261f,
0x80002c00,0x80002c00,0x80002c00,0x80002c00,0x2c000000,0x2b64a61f,0x29a8a9a8,0x261fab64,0x8000ac00,0xa9a8a9a8,0xac000000,0xa9a829a8,0xac000000,0xa61f2b64,0x29a829a8,0x2b64a61f,
0x80002c00,0x2c000000,0x8000ac00,0xac000000,0x2c000000,0xa61fab64,0xa9a829a8,0x2b64261f,0x8000ac00,0xa9a829a8,0x2c000000,0xa9a8a9a8,0xac000000,0x2b64261f,0xa9a8a9a8,0x261f2b64,
0x80002c00,0x8000ac00,0x80002c00,0x8000ac00,0x2c000000,0xab64261f,0x29a8a9a8,0xa61f2b64,0x8000ac00,0x29a829a8,0xac000000,0x29a8a9a8,0xac000000,0x261fab64,0x29a829a8,0xab64261f,
0x80002c00,0xac000000,0x8000ac00,0x2c000000,0x2c000000,0x261f2b64,0xa9a829a8,0xab64a61f,0x8000ac00,0x29a8a9a8,0x2c000000,0x29a829a8,0xac000000,0xab64a61f,0xa9a8a9a8,0xa61fab64,
0x80002c00,0x80002c00,0x80002c00,0x80002c00,0x8000ac00,0xa61fab64,0xa9a8a9a8,0xab64a61f,0x80002c00,0x29a829a8,0x2c000000,0x29a8a9a8,0x8000ac00,0xab64a61f,0xa9a829a8,0x261f2b64,
0x80002c00,0x2c000000,0x8000ac00,0xac000000,0x8000ac00,0xab64261f,0x29a829a8,0x261fab64,0x80002c00,0x29a8a9a8,0xac000000,0x29a829a8,0x8000ac00,0xa61f2b64,0x29a8a9a8,0xab64261f,
0x80002c00,0x8000ac00,0x80002c00,0x8000ac00,0x8000ac00,0x261f2b64,0xa9a8a9a8,0x2b64261f,0x80002c00,0xa9a8a9a8,0x2c000000,0xa9a829a8,0x8000ac00,0x2b64261f,0xa9a829a8,0xa61fab64,
0x80002c00,0xac000000,0x8000ac00,0x2c000000,0x8000ac00,0x2b64a61f,0x29a829a8,0xa61f2b64,0x80002c00,0xa9a829a8,0xac000000,0xa9a8a9a8,0x8000ac00,0x261fab64,0x29a8a9a8,0x2b64a61f,
0x80002c00,0x80002c00,0x80002c00,0x80002c00,0xac000000,0xab64261f,0xa9a829a8,0xa61f2b64,0x8000ac00,0xa9a8a9a8,0xac000000,0xa9a829a8,0x2c000000,0x261fab64,0xa9a8a9a8,0xab64261f,
0x80002c00,0x2c000000,0x8000ac00,0xac000000,0xac000000,0x261f2b64,0x29a8a9a8,0xab64a61f,0x8000ac00,0xa9a829a8,0x2c000000,0xa9a8a9a8,0x2c000000,0xab64a61f,0x29a829a8,0xa61fab64,
0x80002c00,0x8000ac00,0x80002c00,0x8000ac00,0xac000000,0x2b64a61f,0xa9a829a8,0x261fab64,0x8000ac00,0x29a829a8,0xac000000,0x29a8a9a8,0x2c000000,0xa61f2b64,0xa9a8a9a8,0x2b64a61f,
0x80002c00,0xac000000,0x8000ac00,0x2c000000,0xac000000,0xa61fab64,0x29a8a9a8,0x2b64261f,0x8000ac00,0x29a8a9a8,0x2c000000,0x29a829a8,0x2c000000,0x2b64261f,0x29a829a8,0x261f2b64,
0x2c000000,0x2c000000,0x2c000000,0x2c000000,0x2c000000,0x2b64a61f,0x29a8a9a8,0x261fab64,0x2c000000,0x29a8a9a8,0xac00,0xa9a8a9a8,0x2c000000,0x261fab64,0xa9a8a9a8,0xab64261f,
0x2c000000,0xac00,0xac000000,0x2c00,0x2c000000,0xa61fab64,0xa9a829a8,0x2b64261f,0x2c000000,0xa9a8a9a8,0x2c00,0x29a8a9a8,0x2c000000,0xab64a61f,0x29a829a8,0xa61fab64,
0x2c000000,0xac000000,0x2c000000,0xac000000,0x2c000000,0xab64261f,0x29a8a9a8,0xa61f2b64,0x2c000000,0xa9a829a8,0xac00,0x29a829a8,0x2c000000,0xa61f2b64,0xa9a8a9a8,0x2b64a61f,
0x2c000000,0x2c00,0xac000000,0xac00,0x2c000000,0x261f2b64,0xa9a829a8,0xab64a61f,0x2c000000,0x29a829a8,0x2c00,0xa9a829a8,0x2c000000,0x2b64261f,0x29a829a8,0x261f2b64,
0x2c000000,0x2c000000,0x2c000000,0x2c000000,0xac00,0xa61fab64,0xa9a8a9a8,0xab64a61f,0xac000000,0xa9a829a8,0x2c00,0x29a829a8,0x2c00,0x2b64261f,0x29a8a9a8,0xa61fab64,
0x2c000000,0xac00,0xac000000,0x2c00,0xac00,0xab64261f,0x29a829a8,0x261fab64,0xac000000,0x29a829a8,0xac00,0xa9a829a8,0x2c00,0x261fab64,0xa9a829a8,0x2b64a61f,
0x2c000000,0xac000000,0x2c000000,0xac000000,0xac00,0x261f2b64,0xa9a8a9a8,0x2b64261f,0xac000000,0x29a8a9a8,0x2c00,0xa9a8a9a8,0x2c00,0xab64a61f,0x29a8a9a8,0x261f2b64,
0x2c000000,0x2c00,0xac000000,0xac00,0xac00,0x2b64a61f,0x29a829a8,0xa61f2b64,0xac000000,0xa9a8a9a8,0xac00,0x29a8a9a8,0x2c00,0xa61f2b64,0xa9a829a8,0xab64261f,
0x2c000000,0x2c000000,0x2c000000,0x2c000000,0xac000000,0xab64261f,0xa9a829a8,0xa61f2b64,0x2c000000,0x29a8a9a8,0xac00,0xa9a8a9a8,0xac000000,0xa61f2b64,0x29a829a8,0x2b64a61f,
0x2c000000,0xac00,0xac000000,0x2c00,0xac000000,0x261f2b64,0x29a8a9a8,0xab64a61f,0x2c000000,0xa9a8a9a8,0x2c00,0x29a8a9a8,0xac000000,0x2b64261f,0xa9a8a9a8,0x261f2b64,
0x2c000000,0xac000000,0x2c000000,0xac000000,0xac000000,0x2b64a61f,0xa9a829a8,0x261fab64,0x2c000000,0xa9a829a8,0xac00,0x29a829a8,0xac000000,0x261fab64,0x29a829a8,0xab64261f,
0x2c000000,0x2c00,0xac000000,0xac00,0xac000000,0xa61fab64,0x29a8a9a8,0x2b64261f,0x2c000000,0x29a829a8,0x2c00,0xa9a829a8,0xac000000,0xab64a61f,0xa9a8a9a8,0xa61fab64,
0x2c000000,0x2c000000,0x2c000000,0x2c000000,0x2c00,0x261f2b64,0x29a829a8,0x2b64261f,0xac000000,0xa9a829a8,0x2c00,0x29a829a8,0xac00,0xab64a61f,0xa9a829a8,0x261f2b64,
0x2c000000,0xac00,0xac000000,0x2c00,0x2c00,0x2b64a61f,0xa9a8a9a8,0xa61f2b64,0xac000000,0x29a829a8,0xac00,0xa9a829a8,0xac00,0xa61f2b64,0x29a8a9a8,0xab64261f,
0x2c000000,0xac000000,0x2c000000,0xac000000,0x2c00,0xa61fab64,0x29a829a8,0xab64a61f,0xac000000,0x29a8a9a8,0x2c00,0xa9a8a9a8,0xac00,0x2b64261f,0xa9a829a8,0xa61fab64,
0x2c000000,0x2c00,0xac000000,0xac00,0x2c00,0xab64261f,0xa9a8a9a8,0x261fab64,0xac000000,0xa9a8a9a8,0xac00,0x29a8a9a8,0xac00,0x261fab64,0x29a8a9a8,0x2b64a61f,
0x80002c00,0x80002c00,0x80002c00,0x80002c00,0x80002c00,0x261f2b64,0x29a829a8,0x2b64261f,0x80002c00,0x29a829a8,0x2c000000,0x29a8a9a8,0x80002c00,0x2b64261f,0x29a8a9a8,0xa61fab64,
0x80002c00,0x2c000000,0x8000ac00,0xac000000,0x80002c00,0x2b64a61f,0xa9a8a9a8,0xa61f2b64,0x80002c00,0x29a8a9a8,0xac000000,0x29a829a8,0x80002c00,0x261fab64,0xa9a829a8,0x2b64a61f,
0x80002c00,0x8000ac00,0x80002c00,0x8000ac00,0x80002c00,0xa61fab64,0x29a829a8,0xab64a61f,0x80002c00,0xa9a8a9a8,0x2c000000,0xa9a829a8,0x80002c00,0xab64a61f,0x29a8a9a8,0x261f2b64,
0x80002c00,0xac000000,0x8000ac00,0x2c000000,0x80002c00,0xab64261f,0xa9a8a9a8,0x261fab64,0x80002c00,0xa9a829a8,0xac000000,0xa9a8a9a8,0x80002c00,0xa61f2b64,0xa9a829a8,0xab64261f,
0x80002c00,0x80002c00,0x80002c00,0x80002c00,0x2c000000,0x2b64a61f,0x29a8a9a8,0x261fab64,0x8000ac00,0xa9a8a9a8,0xac000000,0xa9a829a8,0xac000000,0xa61f2b64,0x29a829a8,0x2b64a61f,
0x80002c00,0x2c000000,0x8000ac00,0xac000000,0x2c000000,0xa61fab64,0xa9a829a8,0x2b64261f,0x8000ac00,0xa9a829a8,0x2c000000,0xa9a8a9a8,0xac000000,0x2b64261f,0xa9a8a9a8,0x261f2b64,
0x80002c00,0x8000ac00,0x80002c00,0x8000ac00,0x2c000000,0xab64261f,0x29a8a9a8,0xa61f2b64,0x8000ac00,0x29a829a8,0xac000000,0x29a8a9a8,0xac000000,0x261fab64,0x29a829a8,0xab64261f,
0x80002c00,0xac000000,0x8000ac00,0x2c000000,0x2c000000,0x261f2b64,0xa9a829a8,0xab64a61f,0x8000ac00,0x29a8a9a8,0x2c000000,0x29a829a8,0xac000000,0xab64a61f,0xa9a8a9a8,0xa61fab64,
0x80002c00,0x80002c00,0x80002c00,0x80002c00,0x8000ac00,0xa61fab64,0xa9a8a9a8,0xab64a61f,0x80002c00,0x29a829a8,0x2c000000,0x29a8a9a8,0x8000ac00,0xab64a61f,0xa9a829a8,0x261f2b64,
0x80002c00,0x2c000000,0x8000ac00,0xac000000,0x8000ac00,0xab64261f,0x29a829a8,0x261fab64,0x80002c00,0x29a8a9a8,0xac000000,0x29a829a8,0x8000ac00,0xa61f2b64,0x29a8a9a8,0xab64261f,
0x80002c00,0x8000ac00,0x80002c00,0x8000ac00,0x8000ac00,0x261f2b64,0xa9a8a9a8,0x2b64261f,0x80002c00,0xa9a8a9a8,0x2c000000,0xa9a829a8,0x8000ac00,0x2b64261f,0xa9a829a8,0xa61fab64,
0x80002c00,0xac000000,0x8000ac00,0x2c000000,0x8000ac00,0x2b64a61f,0x29a829a8,0xa61f2b64,0x80002c00,0xa9a829a8,0xac000000,0xa9a8a9a8,0x8000ac00,0x261fab64,0x29a8a9a8,0x2b64a61f,
0x80002c00,0x80002c00,0x80002c00,0x80002c00,0xac000000,0xab64261f,0xa9a829a8,0xa61f2b64,0x8000ac00,0xa9a8a9a8,0xac000000,0xa9a829a8,0x2c000000,0x261fab64,0xa9a8a9a8,0xab64261f,
0x80002c00,0x2c000000,0x8000ac00,0xac000000,0xac000000,0x261f2b64,0x29a8a9a8,0xab64a61f,0x8000ac00,0xa9a829a8,0x2c000000,0xa9a8a9a8,0x2c000000,0xab64a61f,0x29a829a8,0xa61fab64,
0x80002c00,0x8000ac00,0x80002c00,0x8000ac00,0xac000000,0x2b64a61f,0xa9a829a8,0x261fab64,0x8000ac00,0x29a829a8,0xac000000,0x29a8a9a8,0x2c000000,0xa61f2b64,0xa9a8a9a8,0x2b64a61f,
0x80002c00,0xac000000,0x8000ac00,0x2c000000,0xac000000,0xa61fab64,0x29a8a9a8,0x2b64261f,0x8000ac00,0x29a8a9a8,0x2c000000,0x29a829a8,0x2c000000,0x2b64261f,0x29a829a8,0x261f2b64,
0x2c000000,0x2c000000,0x2c000000,0x2c000000,0x2c000000,0x2b64a61f,0x29a8a9a8,0x261fab64,0x2c000000,0x29a8a9a8,0xac00,0xa9a8a9a8,0x2c000000,0x261fab64,0xa9a8a9a8,0xab64261f,
0x2c000000,0xac00,0xac000000,0x2c00,0x2c000000,0xa61fab64,0xa9a829a8,0x2b64261f,0x2c000000,0xa9a8a9a8,0x2c00,0x29a8a9a8,0x2c000000,0xab64a61f,0x29a829a8,0xa61fab64,
0x2c000000,0xac000000,0x2c000000,0xac000000,0x2c000000,0xab64261f,0x29a8a9a8,0xa61f2b64,0x2c000000,0xa9a829a8,0xac00,0x29a829a8,0x2c000000,0xa61f2b64,0xa9a8a9a8,0x2b64a61f,
0x2c000000,0x2c00,0xac000000,0xac00,0x2c000000,0x261f2b64,0xa9a829a8,0xab64a61f,0x2c000000,0x29a829a8,0x2c00,0xa9a829a8,0x2c000000,0x2b64261f,0x29a829a8,0x261f2b64,
0x2c000000,0x2c000000,0x2c000000,0x2c000000,0xac00,0xa61fab64,0xa9a8a9a8,0xab64a61f,0xac000000,0xa9a829a8,0x2c00,0x29a829a8,0x2c00,0x2b64261f,0x29a8a9a8,0xa61fab64,
0x2c000000,0xac00,0xac000000,0x2c00,0xac00,0xab64261f,0x29a829a8,0x261fab64,0xac000000,0x29a829a8,0xac00,0xa9a829a8,0x2c00,0x261fab64,0xa9a829a8,0x2b64a61f,
0x2c000000,0xac000000,0x2c000000,0xac000000,0xac00,0x261f2b64,0xa9a8a9a8,0x2b64261f,0xac000000,0x29a8a9a8,0x2c00,0xa9a8a9a8,0x2c00,0xab64a61f,0x29a8a9a8,0x261f2b64,
0x2c000000,0x2c00,0xac000000,0xac00,0xac00,0x2b64a61f,0x29a829a8,0xa61f2b64,0xac000000,0xa9a8a9a8,0xac00,0x29a8a9a8,0x2c00,0xa61f2b64,0xa9a829a8,0xab64261f,
0x2c000000,0x2c000000,0x2c000000,0x2c000000,0xac000000,0xab64261f,0xa9a829a8,0xa61f2b64,0x2c000000,0x29a8a9a8,0xac00,0xa9a8a9a8,0xac000000,0xa61f2b64,0x29a829a8,0x2b64a61f,
0x2c000000,0xac00,0xac000000,0x2c00,0xac000000,0x261f2b64,0x29a8a9a8,0xab64a61f,0x2c000000,0xa9a8a9a8,0x2c00,0x29a8a9a8,0xac000000,0x2b64261f,0xa9a8a9a8,0x261f2b64,
0x2c000000,0xac000000,0x2c000000,0xac000000,0xac000000,0x2b64a61f,0xa9a829a8,0x261fab64,0x2c000000,0xa9a829a8,0xac00,0x29a829a8,0xac000000,0x261fab64,0x29a829a8,0xab64261f,
0x2c000000,0x2c00,0xac000000,0xac00,0xac000000,0xa61fab64,0x29a8a9a8,0x2b64261f,0x2c000000,0x29a829a8,0x2c00,0xa9a829a8,0xac000000,0xab64a61f,0xa9a8a9a8,0xa61fab64,
0x2c000000,0x2c000000,0x2c000000,0x2c000000,0x2c00,0x261f2b64,0x29a829a8,0x2b64261f,0xac000000,0xa9a829a8,0x2c00,0x29a829a8,0xac00,0xab64a61f,0xa9a829a8,0x261f2b64,
0x2c000000,0xac00,0xac000000,0x2c00,0x2c00,0x2b64a61f,0xa9a8a9a8,0xa61f2b64,0xac000000,0x29a829a8,0xac00,0xa9a829a8,0xac00,0xa61f2b64,0x29a8a9a8,0xab64261f,
0x2c000000,0xac000000,0x2c000000,0xac000000,0x2c00,0xa61fab64,0x29a829a8,0xab64a61f,0xac000000,0x29a8a9a8,0x2c00,0xa9a8a9a8,0xac00,0x2b64261f,0xa9a829a8,0xa61fab64,
0x2c000000,0x2c00,0xac000000,0xac00,0x2c00,0xab64261f,0xa9a8a9a8,0x261fab64,0xac000000,0xa9a8a9a8,0xac00,0x29a8a9a8,0xac00,0x261fab64,0x29a8a9a8,0x2b64a61f,
0x3f800000,0x0,0x3f800000,0x0,0x3f800000,0x0,0x3f7fec43,0xbcc90ab0,0x3f800000,0x0,0x3f7fb10f,0xbd48fb30,0x3f800000,0x0,0x3f7f4e6d,0xbd96a905,
0x3f800000,0x0,0x3f7ec46d,0xbdc8bd36,0x3f800000,0x0,0x3f7e1324,0xbdfab273,0x3f800000,0x0,0x3f7d3aac,0xbe164083,0x3f800000,0x0,0x3f7c3b28,0xbe2f10a2,
0x3f800000,0x0,0x3f7b14be,0xbe47c5c2,0x3f800000,0x0,0x3f79c79d,0xbe605c13,0x3f800000,0x0,0x3f7853f8,0xbe78cfcc,0x3f800000,0x0,0x3f76ba07,0xbe888e93,
0x3f800000,0x0,0x3f74fa0b,0xbe94a031,0x3f800000,0x0,0x3f731447,0xbea09ae5,0x3f800000,0x0,0x3f710908,0xbeac7cd4,0x3f800000,0x0,0x3f6ed89e,0xbeb8442a,
0x3f800000,0x0,0x3f800000,0x0,0x3f7ec46d,0xbdc8bd36,0x3f7e1324,0xbdfab273,0x3f7b14be,0xbe47c5c2,0x3f7853f8,0xbe78cfcc,0x3f74fa0b,0xbe94a031,0x3f6ed89e,0xbeb8442a,
0x3f6c835e,0xbec3ef15,0x3f61c598,0xbef15aea,0x3f61c598,0xbef15aea,0x3f514d3d,0xbf13682a,0x3f54db31,0xbf0e39da,0x3f3daef9,0xbf2beb4a,0x3f45e403,0xbf226799,0x3f273656,0xbf41d870,
0x3f3504f3,0xbf3504f3,0x3f0e39da,0xbf54db31,0x3f226799,0xbf45e403,0x3ee63375,0xbf64aa59,0x3f0e39da,0xbf54db31,0x3eac7cd4,0xbf710908,0x3ef15aea,0xbf61c598,0x3e605c13,0xbf79c79d,
0x3ec3ef15,0xbf6c835e,0x3dc8bd36,0xbf7ec46d,0x3e94a031,0xbf74fa0b,0xbcc90ab0,0xbf7fec43,0x3e47c5c2,0xbf7b14be,0xbe164083,0xbf7d3aac,0x3dc8bd36,0xbf7ec46d,0xbe888e93,0xbf76ba07,
0x3f800000,0x0,0x3f800000,0x0,0x3f7b14be,0xbe47c5c2,0x3f79c79d,0xbe605c13,0x3f6c835e,0xbec3ef15,0x3f676bd8,0xbedae880,0x3f54db31,0xbf0e39da,0x3f49d112,0xbf1d7fd1,
0x3f3504f3,0xbf3504f3,0x3f226799,0xbf45e403,0x3f0e39da,0xbf54db31,0x3ee63375,0xbf64aa59,0x3ec3ef15,0xbf6c835e,0x3e78cfcc,0xbf7853f8,0x3e47c5c2,0xbf7b14be,0x3cc90ab0,0xbf7fec43,
0x0,0xbf800000,0xbe47c5c2,0xbf7b14be,0xbe47c5c2,0xbf7b14be,0xbecf7bca,0xbf6a09a7,0xbec3ef15,0xbf6c835e,0xbf187fc0,0xbf4d9f02,0xbf0e39da,0xbf54db31,0xbf41d870,0xbf273656,
0xbf3504f3,0xbf3504f3,0xbf61c598,0xbef15aea,0xbf54db31,0xbf0e39da,0xbf76ba07,0xbe888e93,0xbf6c835e,0xbec3ef15,0xbf7fb10f,0xbd48fb30,0xbf7b14be,0xbe47c5c2,0xbf7c3b28,0x3e2f10a2,
0x3f800000,0x0,0x3f800000,0x0,0x3f74fa0b,0xbe94a031,0x3f731447,0xbea09ae5,0x3f54db31,0xbf0e39da,0x3f4d9f02,0xbf187fc0,0x3f226799,0xbf45e403,0x3f13682a,0xbf514d3d,
0x3ec3ef15,0xbf6c835e,0x3e94a031,0xbf74fa0b,0x3dc8bd36,0xbf7ec46d,0xbcc90ab0,0xbf7fec43,0xbe47c5c2,0xbf7b14be,0xbeac7cd4,0xbf710908,0xbef15aea,0xbf61c598,0xbf1d7fd1,0xbf49d112,
0xbf3504f3,0xbf3504f3,0xbf54db31,0xbf0e39da,0xbf61c598,0xbef15aea,0xbf76ba07,0xbe888e93,0xbf7b14be,0xbe47c5c2,0xbf7fb10f,0x3d48fb30,0xbf7ec46d,0x3dc8bd36,0xbf6ed89e,0x3eb8442a,
0xbf6c835e,0x3ec3ef15,0xbf45e403,0x3f226799,0xbf45e403,0x3f226799,0xbf08f59b,0x3f584853,0xbf0e39da,0x3f54db31,0xbe78cfcc,0x3f7853f8,0xbe94a031,0x3f74fa0b,0x3d96a905,0x3f7f4e6d,
0x3f800000,0x0,0x3f800000,0x0,0x3f7fb10f,0xbd48fb30,0x3f7f4e6d,0xbd96a905,0x3f7ec46d,0xbdc8bd36,0x3f7d3aac,0xbe164083,0x3f7d3aac,0xbe164083,0x3f79c79d,0xbe605c13,
0x3f7b14be,0xbe47c5c2,0x3f74fa0b,0xbe94a031,0x3f7853f8,0xbe78cfcc,0x3f6ed89e,0xbeb8442a,0x3f74fa0b,0xbe94a031,0x3f676bd8,0xbedae880,0x3f710908,0xbeac7cd4,0x3f5ebe05,0xbefc5d27,
0x3f6c835e,0xbec3ef15,0x3f54db31,0xbf0e39da,0x3f676bd8,0xbedae880,0x3f49d112,0xbf1d7fd1,0x3f61c598,0xbef15aea,0x3f3daef9,0xbf2beb4a,0x3f5b941a,0xbf039c3d,0x3f3085bb,0xbf396842,
0x3f54db31,0xbf0e39da,0x3f226799,0xbf45e403,0x3f4d9f02,0xbf187fc0,0x3f13682a,0xbf514d3d,0x3f45e403,0xbf226799,0x3f039c3d,0xbf5b941a,0x3f3daef9,0xbf2beb4a,0x3ee63375,0xbf64aa59,
0x3f800000,0x0,0x3f800000,0x0,0x3f7d3aac,0xbe164083,0x3f7c3b28,0xbe2f10a2,0x3f74fa0b,0xbe94a031,0x3f710908,0xbeac7cd4,0x3f676bd8,0xbedae880,0x3f5ebe05,0xbefc5d27,
0x3f54db31,0xbf0e39da,0x3f45e403,0xbf226799,0x3f3daef9,0xbf2beb4a,0x3f273656,0xbf41d870,0x3f226799,0xbf45e403,0x3f039c3d,0xbf5b941a,0x3f039c3d,0xbf5b941a,0x3eb8442a,0xbf6ed89e,
0x3ec3ef15,0xbf6c835e,0x3e47c5c2,0xbf7b14be,0x3e78cfcc,0xbf7853f8,0x3cc90ab0,0xbf7fec43,0x3dc8bd36,0xbf7ec46d,0xbe164083,0xbf7d3aac,0xbd48fb30,0xbf7fb10f,0xbea09ae5,0xbf731447,
0xbe47c5c2,0xbf7b14be,0xbef15aea,0xbf61c598,0xbeac7cd4,0xbf710908,0xbf1d7fd1,0xbf49d112,0xbef15aea,0xbf61c598,0xbf3daef9,0xbf2beb4a,0xbf187fc0,0xbf4d9f02,0xbf584853,0xbf08f59b,
0x3f800000,0x0,0x3f800000,0x0,0x3f7853f8,0xbe78cfcc,0x3f76ba07,0xbe888e93,0x3f61c598,0xbef15aea,0x3f5b941a,0xbf039c3d,0x3f3daef9,0xbf2beb4a,0x3f3085bb,0xbf396842,
0x3f0e39da,0xbf54db31,0x3ef15aea,0xbf61c598,0x3eac7cd4,0xbf710908,0x3e605c13,0xbf79c79d,0x3dc8bd36,0xbf7ec46d,0xbd48fb30,0xbf7fb10f,0xbe164083,0xbf7d3aac,0xbea09ae5,0xbf731447,
0xbec3ef15,0xbf6c835e,0xbf0e39da,0xbf54db31,0xbf187fc0,0xbf4d9f02,0xbf41d870,0xbf273656,0xbf45e403,0xbf226799,0xbf676bd8,0xbedae880,0xbf676bd8,0xbedae880,0xbf7c3b28,0xbe2f10a2,
0xbf7b14be,0xbe47c5c2,0xbf7ec46d,0x3dc8bd36,0xbf7fb10f,0x3d48fb30,0xbf6ed89e,0x3eb8442a,0xbf74fa0b,0x3e94a031,0xbf4d9f02,0x3f187fc0,0xbf5b941a,0x3f039c3d,0xbf1d7fd1,0x3f49d112,
0x3f800000,0x0,0x3f800000,0x0,0x3f710908,0xbeac7cd4,0x3f6ed89e,0xbeb8442a,0x3f45e403,0xbf226799,0x3f3daef9,0xbf2beb4a,0x3f039c3d,0xbf5b941a,0x3ee63375,0xbf64aa59,
0x3e47c5c2,0xbf7b14be,0x3dc8bd36,0xbf7ec46d,0xbe164083,0xbf7d3aac,0xbe888e93,0xbf76ba07,0xbef15aea,0xbf61c598,0xbf187fc0,0xbf4d9f02,0xbf3daef9,0xbf2beb4a,0xbf584853,0xbf08f59b,
0xbf6c835e,0xbec3ef15,0xbf7b14be,0xbe47c5c2,0xbf7fb10f,0xbd48fb30,0xbf7c3b28,0x3e2f10a2,0xbf74fa0b,0x3e94a031,0xbf5b941a,0x3f039c3d,0xbf4d9f02,0x3f187fc0,0xbf1d7fd1,0x3f49d112,
0xbf0e39da,0x3f54db31,0xbe94a031,0x3f74fa0b,0xbe78cfcc,0x3f7853f8,0x3d96a905,0x3f7f4e6d,0x3dc8bd36,0x3f7ec46d,0x3edae880,0x3f676bd8,0x3edae880,0x3f676bd8,0x3f396842,0x3f3085bb,
0x3f800000,0x0,0x3f800000,0x0,0x3f800000,0x0,0x3f7fec43,0xbcc90ab0,0x3f800000,0x0,0x3f7fb10f,0xbd48fb30,0x3f800000,0x0,0x3f7f4e6d,0xbd96a905,
0x3f800000,0x0,0x3f7ec46d,0xbdc8bd36,0x3f800000,0x0,0x3f7e1324,0xbdfab273,0x3f800000,0x0,0x3f7d3aac,0xbe164083,0x3f800000,0x0,0x3f7c3b28,0xbe2f10a2,
0x3f800000,0x0,0x3f7b14be,0xbe47c5c2,0x3f800000,0x0,0x3f79c79d,0xbe605c13,0x3f800000,0x0,0x3f7853f8,0xbe78cfcc,0x3f800000,0x0,0x3f76ba07,0xbe888e93,
0x3f800000,0x0,0x3f74fa0b,0xbe94a031,0x3f800000,0x0,0x3f731447,0xbea09ae5,0x3f800000,0x0,0x3f710908,0xbeac7cd4,0x3f800000,0x0,0x3f6ed89e,0xbeb8442a,
0x3f800000,0x0,0x3f800000,0x0,0x3f7ec46d,0xbdc8bd36,0x3f7e1324,0xbdfab273,0x3f7b14be,0xbe47c5c2,0x3f7853f8,0xbe78cfcc,0x3f74fa0b,0xbe94a031,0x3f6ed89e,0xbeb8442a,
0x3f6c835e,0xbec3ef15,0x3f61c598,0xbef15aea,0x3f61c598,0xbef15aea,0x3f514d3d,0xbf13682a,0x3f54db31,0xbf0e39da,0x3f3daef9,0xbf2beb4a,0x3f45e403,0xbf226799,0x3f273656,0xbf41d870,
0x3f3504f3,0xbf3504f3,0x3f0e39da,0xbf54db31,0x3f226799,0xbf45e403,0x3ee63375,0xbf64aa59,0x3f0e39da,0xbf54db31,0x3eac7cd4,0xbf710908,0x3ef15aea,0xbf61c598,0x3e605c13,0xbf79c79d,
0x3ec3ef15,0xbf6c835e,0x3dc8bd36,0xbf7ec46d,0x3e94a031,0xbf74fa0b,0xbcc90ab0,0xbf7fec43,0x3e47c5c2,0xbf7b14be,0xbe164083,0xbf7d3aac,0x3dc8bd36,0xbf7ec46d,0xbe888e93,0xbf76ba07,
0x3f800000,0x0,0x3f800000,0x0,0x3f7b14be,0xbe47c5c2,0x3f79c79d,0xbe605c13,0x3f6c835e,0xbec3ef15,0x3f676bd8,0xbedae880,0x3f54db31,0xbf0e39da,0x3f49d112,0xbf1d7fd1,
0x3f3504f3,0xbf3504f3,0x3f226799,0xbf45e403,0x3f0e39da,0xbf54db31,0x3ee63375,0xbf64aa59,0x3ec3ef15,0xbf6c835e,0x3e78cfcc,0xbf7853f8,0x3e47c5c2,0xbf7b14be,0x3cc90ab0,0xbf7fec43,
0x0,0xbf800000,0xbe47c5c2,0xbf7b14be,0xbe47c5c2,0xbf7b14be,0xbecf7bca,0xbf6a09a7,0xbec3ef15,0xbf6c835e,0xbf187fc0,0xbf4d9f02,0xbf0e39da,0xbf54db31,0xbf41d870,0xbf273656,
0xbf3504f3,0xbf3504f3,0xbf61c598,0xbef15aea,0xbf54db31,0xbf0e39da,0xbf76ba07,0xbe888e93,0xbf6c835e,0xbec3ef15,0xbf7fb10f,0xbd48fb30,0xbf7b14be,0xbe47c5c2,0xbf7c3b28,0x3e2f10a2,
0x3f800000,0x0,0x3f800000,0x0,0x3f74fa0b,0xbe94a031,0x3f731447,0xbea09ae5,0x3f54db31,0xbf0e39da,0x3f4d9f02,0xbf187fc0,0x3f226799,0xbf45e403,0x3f13682a,0xbf514d3d,
0x3ec3ef15,0xbf6c835e,0x3e94a031,0xbf74fa0b,0x3dc8bd36,0xbf7ec46d,0xbcc90ab0,0xbf7fec43,0xbe47c5c2,0xbf7b14be,0xbeac7cd4,0xbf710908,0xbef15aea,0xbf61c598,0xbf1d7fd1,0xbf49d112,
0xbf3504f3,0xbf3504f3,0xbf54db31,0xbf0e39da,0xbf61c598,0xbef15aea,0xbf76ba07,0xbe888e93,0xbf7b14be,0xbe47c5c2,0xbf7fb10f,0x3d48fb30,0xbf7ec46d,0x3dc8bd36,0xbf6ed89e,0x3eb8442a,
0xbf6c835e,0x3ec3ef15,0xbf45e403,0x3f226799,0xbf45e403,0x3f226799,0xbf08f59b,0x3f584853,0xbf0e39da,0x3f54db31,0xbe78cfcc,0x3f7853f8,0xbe94a031,0x3f74fa0b,0x3d96a905,0x3f7f4e6d,
0x3f800000,0x0,0x3f800000,0x0,0x3f7fb10f,0xbd48fb30,0x3f7f4e6d,0xbd96a905,0x3f7ec46d,0xbdc8bd36,0x3f7d3aac,0xbe164083,0x3f7d3aac,0xbe164083,0x3f79c79d,0xbe605c13,
0x3f7b14be,0xbe47c5c2,0x3f74fa0b,0xbe94a031,0x3f7853f8,0xbe78cfcc,0x3f6ed89e,0xbeb8442a,0x3f74fa0b,0xbe94a031,0x3f676bd8,0xbedae880,0x3f710908,0xbeac7cd4,0x3f5ebe05,0xbefc5d27,
0x3f6c835e,0xbec3ef15,0x3f54db31,0xbf0e39da,0x3f676bd8,0xbedae880,0x3f49d112,0xbf1d7fd1,0x3f61c598,0xbef15aea,0x3f3daef9,0xbf2beb4a,0x3f5b941a,0xbf039c3d,0x3f3085bb,0xbf396842,
0x3f54db31,0xbf0e39da,0x3f226799,0xbf45e403,0x3f4d9f02,0xbf187fc0,0x3f13682a,0xbf514d3d,0x3f45e403,0xbf226799,0x3f039c3d,0xbf5b941a,0x3f3daef9,0xbf2beb4a,0x3ee63375,0xbf64aa59,
0x3f800000,0x0,0x3f800000,0x0,0x3f7d3aac,0xbe164083,0x3f7c3b28,0xbe2f10a2,0x3f74fa0b,0xbe94a031,0x3f710908,0xbeac7cd4,0x3f676bd8,0xbedae880,0x3f5ebe05,0xbefc5d27,
0x3f54db31,0xbf0e39da,0x3f45e403,0xbf226799,0x3f3daef9,0xbf2beb4a,0x3f273656,0xbf41d870,0x3f226799,0xbf45e403,0x3f039c3d,0xbf5b941a,0x3f039c3d,0xbf5b941a,0x3eb8442a,0xbf6ed89e,
0x3ec3ef15,0xbf6c835e,0x3e47c5c2,0xbf7b14be,0x3e78cfcc,0xbf7853f8,0x3cc90ab0,0xbf7fec43,0x3dc8bd36,0xbf7ec46d,0xbe164083,0xbf7d3aac,0xbd48fb30,0xbf7fb10f,0xbea09ae5,0xbf731447,
0xbe47c5c2,0xbf7b14be,0xbef15aea,0xbf61c598,0xbeac7cd4,0xbf710908,0xbf1d7fd1,0xbf49d112,0xbef15aea,0xbf61c598,0xbf3daef9,0xbf2beb4a,0xbf187fc0,0xbf4d9f02,0xbf584853,0xbf08f59b,
0x3f800000,0x0,0x3f800000,0x0,0x3f7853f8,0xbe78cfcc,0x3f76ba07,0xbe888e93,0x3f61c598,0xbef15aea,0x3f5b941a,0xbf039c3d,0x3f3daef9,0xbf2beb4a,0x3f3085bb,0xbf396842,
0x3f0e39da,0xbf54db31,0x3ef15aea,0xbf61c598,0x3eac7cd4,0xbf710908,0x3e605c13,0xbf79c79d,0x3dc8bd36,0xbf7ec46d,0xbd48fb30,0xbf7fb10f,0xbe164083,0xbf7d3aac,0xbea09ae5,0xbf731447,
0xbec3ef15,0xbf6c835e,0xbf0e39da,0xbf54db31,0xbf187fc0,0xbf4d9f02,0xbf41d870,0xbf273656,0xbf45e403,0xbf226799,0xbf676bd8,0xbedae880,0xbf676bd8,0xbedae880,0xbf7c3b28,0xbe2f10a2,
0xbf7b14be,0xbe47c5c2,0xbf7ec46d,0x3dc8bd36,0xbf7fb10f,0x3d48fb30,0xbf6ed89e,0x3eb8442a,0xbf74fa0b,0x3e94a031,0xbf4d9f02,0x3f187fc0,0xbf5b941a,0x3f039c3d,0xbf1d7fd1,0x3f49d112,
0x3f800000,0x0,0x3f800000,0x0,0x3f710908,0xbeac7cd4,0x3f6ed89e,0xbeb8442a,0x3f45e403,0xbf226799,0x3f3daef9,0xbf2beb4a,0x3f039c3d,0xbf5b941a,0x3ee63375,0xbf64aa59,
0x3e47c5c2,0xbf7b14be,0x3dc8bd36,0xbf7ec46d,0xbe164083,0xbf7d3aac,0xbe888e93,0xbf76ba07,0xbef15aea,0xbf61c598,0xbf187fc0,0xbf4d9f02,0xbf3daef9,0xbf2beb4a,0xbf584853,0xbf08f59b,
0xbf6c835e,0xbec3ef15,0xbf7b14be,0xbe47c5c2,0xbf7fb10f,0xbd48fb30,0xbf7c3b28,0x3e2f10a2,0xbf74fa0b,0x3e94a031,0xbf5b941a,0x3f039c3d,0xbf4d9f02,0x3f187fc0,0xbf1d7fd1,0x3f49d112,
0xbf0e39da,0x3f54db31,0xbe94a031,0x3f74fa0b,0xbe78cfcc,0x3f7853f8,0x3d96a905,0x3f7f4e6d,0x3dc8bd36,0x3f7ec46d,0x3edae880,0x3f676bd8,0x3edae880,0x3f676bd8,0x3f396842,0x3f3085bb,
0x80002c00,0x1a482bfe,0x1e462bf6,0x20b22bea,0x80002c00,0x26d72b3b,0x2a2f2913,0x2bc323c6,0x80002c00,0x29ed295f,0x2bf69e46,0x28c4aa6d,0x80002c00,0x2b882564,0x2913aa2f,0xa81daadd,
0x80002c00,0x2bfe9a48,0x9e46abf6,0xabea20b2,0x80002c00,0x2b3ba6d7,0xaa2fa913,0xa3c62bc3,0x80002c00,0x295fa9ed,0xabf61e46,0x2a6d28c4,0x80002c00,0x2564ab88,0xa9132a2f,0x2adda81d,
0x80002c00,0x9a48abfe,0x1e462bf6,0xa0b2abea,0x80002c00,0xa6d7ab3b,0x2a2f2913,0xabc3a3c6,0x80002c00,0xa9eda95f,0x2bf69e46,0xa8c42a6d,0x80002c00,0xab88a564,0x2913aa2f,0x281d2add,
0x80002c00,0xabfe1a48,0x9e46abf6,0x2beaa0b2,0x80002c00,0xab3b26d7,0xaa2fa913,0x23c6abc3,0x80002c00,0xa95f29ed,0xabf61e46,0xaa6da8c4,0x80002c00,0xa5642b88,0xa9132a2f,0xaadd281d,
0x223e2bd9,0x23c62bc3,0x24a52ba8,0x25642b88,0x2bd9a23e,0x2a6da8c4,0x278bab0e,0x1a48abfe,0xa23eabd9,0xaadda81d,0xaba824a5,0xa6d72b3b,0xabd9223e,0xa0b22bea,0x2b0e278b,0x29eda95f,
0x223e2bd9,0x2bc3a3c6,0xa4a5aba8,0xab882564,0x2bd9a23e,0xa8c4aa6d,0xa78b2b0e,0x2bfe1a48,0xa23eabd9,0xa81d2add,0x2ba8a4a5,0xab3ba6d7,0xabd9223e,0x2bea20b2,0xab0ea78b,0x295f29ed,
0x223e2bd9,0xa3c6abc3,0x24a52ba8,0xa564ab88,0x2bd9a23e,0xaa6d28c4,0x278bab0e,0x9a482bfe,0xa23eabd9,0x2add281d,0xaba824a5,0x26d7ab3b,0xabd9223e,0x20b2abea,0x2b0e278b,0xa9ed295f,
0x223e2bd9,0xabc323c6,0xa4a5aba8,0x2b88a564,0x2bd9a23e,0x28c42a6d,0xa78b2b0e,0xabfe9a48,0xa23eabd9,0x281daadd,0x2ba8a4a5,0x2b3b26d7,0xabd9223e,0xabeaa0b2,0xab0ea78b,0xa95fa9ed,
0x261f2b64,0x26d72b3b,0x278b2b0e,0x281d2add,0xa61fab64,0xa9eda95f,0xaba8a4a5,0xabea20b2,0x261f2b64,0x2b882564,0x2b0ea78b,0x23c6abc3,0xa61fab64,0xabfe1a48,0xa4a52ba8,0x2a6d28c4,
0x261f2b64,0x2b3ba6d7,0xa78bab0e,0xaadd281d,0xa61fab64,0xa95f29ed,0x2ba824a5,0xa0b2abea,0x261f2b64,0x2564ab88,0xab0e278b,0x2bc323c6,0xa61fab64,0x1a482bfe,0x24a5aba8,0xa8c42a6d,
0x261f2b64,0xa6d7ab3b,0x278b2b0e,0xa81daadd,0xa61fab64,0x29ed295f,0xaba8a4a5,0x2beaa0b2,0x261f2b64,0xab88a564,0x2b0ea78b,0xa3c62bc3,0xa61fab64,0x2bfe9a48,0xa4a52ba8,0xaa6da8c4,
0x261f2b64,0xab3b26d7,0xa78bab0e,0x2adda81d,0xa61fab64,0x295fa9ed,0x2ba824a5,0x20b22bea,0x261f2b64,0xa5642b88,0xab0e278b,0xabc3a3c6,0xa61fab64,0x9a48abfe,0x24a5aba8,0x28c4aa6d,
0x28722aa7,0x28c42a6d,0x29132a2f,0x295f29ed,0xaaa72872,0xa81d2add,0x9e462bf6,0x25642b88,0xa872aaa7,0xabeaa0b2,0xaa2f2913,0x9a482bfe,0x2aa7a872,0xa3c6abc3,0xabf69e46,0xa6d72b3b,
0x28722aa7,0x2a6da8c4,0xa913aa2f,0xa9ed295f,0xaaa72872,0x2add281d,0x1e46abf6,0xab882564,0xa872aaa7,0xa0b22bea,0x2a2fa913,0xabfe9a48,0x2aa7a872,0xabc323c6,0x2bf61e46,0xab3ba6d7,
0x28722aa7,0xa8c4aa6d,0x29132a2f,0xa95fa9ed,0xaaa72872,0x281daadd,0x9e462bf6,0xa564ab88,0xa872aaa7,0x2bea20b2,0xaa2f2913,0x1a48abfe,0x2aa7a872,0x23c62bc3,0xabf69e46,0x26d7ab3b,
0x28722aa7,0xaa6d28c4,0xa913aa2f,0x29eda95f,0xaaa72872,0xaadda81d,0x1e46abf6,0x2b88a564,0xa872aaa7,0x20b2abea,0x2a2fa913,0x2bfe1a48,0x2aa7a872,0x2bc3a3c6,0x2bf61e46,0x2b3b26d7,
0x2c000000,0x2bfe9a48,0x2bf69e46,0x2beaa0b2,0x2c000000,0x2b3ba6d7,0x2913aa2f,0x23c6abc3,0x2c000000,0x295fa9ed,0x9e46abf6,0xaa6da8c4,0x2c000000,0x2564ab88,0xaa2fa913,0xaadd281d,
0x2c000000,0x9a48abfe,0xabf61e46,0x20b22bea,0x2c000000,0xa6d7ab3b,0xa9132a2f,0x2bc323c6,0x2c000000,0xa9eda95f,0x1e462bf6,0x28c4aa6d,0x2c000000,0xab88a564,0x2a2f2913,0xa81daadd,
0x2c000000,0xabfe1a48,0x2bf69e46,0xabea20b2,0x2c000000,0xab3b26d7,0x2913aa2f,0xa3c62bc3,0x2c000000,0xa95f29ed,0x9e46abf6,0x2a6d28c4,0x2c000000,0xa5642b88,0xaa2fa913,0x2adda81d,
0x2c000000,0x1a482bfe,0xabf61e46,0xa0b2abea,0x2c000000,0x26d72b3b,0xa9132a2f,0xabc3a3c6,0x2c000000,0x29ed295f,0x1e462bf6,0xa8c42a6d,0x2c000000,0x2b882564,0x2a2f2913,0x281d2add,
0x2bd9a23e,0x2bc3a3c6,0x2ba8a4a5,0x2b88a564,0xa23eabd9,0xa8c4aa6d,0xab0ea78b,0xabfe9a48,0xabd9223e,0xa81d2add,0x24a52ba8,0x2b3b26d7,0x223e2bd9,0x2bea20b2,0x278bab0e,0xa95fa9ed,
0x2bd9a23e,0xa3c6abc3,0xaba824a5,0x25642b88,0xa23eabd9,0xaa6d28c4,0x2b0e278b,0x1a48abfe,0xabd9223e,0x2add281d,0xa4a5aba8,0xa6d72b3b,0x223e2bd9,0x20b2abea,0xa78b2b0e,0x29eda95f,
0x2bd9a23e,0xabc323c6,0x2ba8a4a5,0xab882564,0xa23eabd9,0x28c42a6d,0xab0ea78b,0x2bfe1a48,0xabd9223e,0x281daadd,0x24a52ba8,0xab3ba6d7,0x223e2bd9,0xabeaa0b2,0x278bab0e,0x295f29ed,
0x2bd9a23e,0x23c62bc3,0xaba824a5,0xa564ab88,0xa23eabd9,0x2a6da8c4,0x2b0e278b,0x9a482bfe,0xabd9223e,0xaadda81d,0xa4a5aba8,0x26d7ab3b,0x223e2bd9,0xa0b22bea,0xa78b2b0e,0xa9ed295f,
0x2b64a61f,0x2b3ba6d7,0x2b0ea78b,0x2adda81d,0xab64261f,0xa95f29ed,0xa4a52ba8,0x20b22bea,0x2b64a61f,0x2564ab88,0xa78bab0e,0xabc3a3c6,0xab64261f,0x1a482bfe,0x2ba824a5,0x28c4aa6d,
0x2b64a61f,0xa6d7ab3b,0xab0e278b,0x281d2add,0xab64261f,0x29ed295f,0x24a5aba8,0xabea20b2,0x2b64a61f,0xab88a564,0x278b2b0e,0x23c6abc3,0xab64261f,0x2bfe9a48,0xaba8a4a5,0x2a6d28c4,
0x2b64a61f,0xab3b26d7,0x2b0ea78b,0xaadd281d,0xab64261f,0x295fa9ed,0xa4a52ba8,0xa0b2abea,0x2b64a61f,0xa5642b88,0xa78bab0e,0x2bc323c6,0xab64261f,0x9a48abfe,0x2ba824a5,0xa8c42a6d,
0x2b64a61f,0x26d72b3b,0xab0e278b,0xa81daadd,0xab64261f,0xa9eda95f,0x24a5aba8,0x2beaa0b2,0x2b64a61f,0x2b882564,0x278b2b0e,0xa3c62bc3,0xab64261f,0xabfe1a48,0xaba8a4a5,0xaa6da8c4,
0x2aa7a872,0x2a6da8c4,0x2a2fa913,0x29eda95f,0x28722aa7,0x2add281d,0x2bf61e46,0x2b88a564,0xaaa72872,0xa0b22bea,0x29132a2f,0x2bfe1a48,0xa872aaa7,0xabc323c6,0x9e462bf6,0x2b3b26d7,
0x2aa7a872,0xa8c4aa6d,0xaa2f2913,0x295f29ed,0x28722aa7,0x281daadd,0xabf69e46,0x25642b88,0xaaa72872,0x2bea20b2,0xa913aa2f,0x9a482bfe,0xa872aaa7,0x23c62bc3,0x1e46abf6,0xa6d72b3b,
0x2aa7a872,0xaa6d28c4,0x2a2fa913,0xa9ed295f,0x28722aa7,0xaadda81d,0x2bf61e46,0xab882564,0xaaa72872,0x20b2abea,0x29132a2f,0xabfe9a48,0xa872aaa7,0x2bc3a3c6,0x9e462bf6,0xab3ba6d7,
0x2aa7a872,0x28c42a6d,0xaa2f2913,0xa95fa9ed,0x28722aa7,0xa81d2add,0xabf69e46,0xa564ab88,0xaaa72872,0xabeaa0b2,0xa913aa2f,0x1a48abfe,0xa872aaa7,0xa3c6abc3,0x1e46abf6,0x26d7ab3b,
0x80002c00,0x80002c00,0x80002c00,0x80002c00,0x1a482bfe,0x26d72b3b,0x29ed295f,0x2b882564,0x1e462bf6,0x2a2f2913,0x2bf69e46,0x2913aa2f,0x20b22bea,0x2bc323c6,0x28c4aa6d,0xa81daadd,
0x223e2bd9,0x2bd9a23e,0xa23eabd9,0xabd9223e,0x23c62bc3,0x2a6da8c4,0xaadda81d,0xa0b22bea,0x24a52ba8,0x278bab0e,0xaba824a5,0x2b0e278b,0x25642b88,0x1a48abfe,0xa6d72b3b,0x29eda95f,
0x261f2b64,0xa61fab64,0x261f2b64,0xa61fab64,0x26d72b3b,0xa9eda95f,0x2b882564,0xabfe1a48,0x278b2b0e,0xaba8a4a5,0x2b0ea78b,0xa4a52ba8,0x281d2add,0xabea20b2,0x23c6abc3,0x2a6d28c4,
0x28722aa7,0xaaa72872,0xa872aaa7,0x2aa7a872,0x28c42a6d,0xa81d2add,0xabeaa0b2,0xa3c6abc3,0x29132a2f,0x9e462bf6,0xaa2f2913,0xabf69e46,0x295f29ed,0x25642b88,0x9a482bfe,0xa6d72b3b,
0x80002c00,0x80002c00,0x80002c00,0x80002c00,0x2bfe9a48,0x2b3ba6d7,0x295fa9ed,0x2564ab88,0x9e46abf6,0xaa2fa913,0xabf61e46,0xa9132a2f,0xabea20b2,0xa3c62bc3,0x2a6d28c4,0x2adda81d,
0x223e2bd9,0x2bd9a23e,0xa23eabd9,0xabd9223e,0x2bc3a3c6,0xa8c4aa6d,0xa81d2add,0x2bea20b2,0xa4a5aba8,0xa78b2b0e,0x2ba8a4a5,0xab0ea78b,0xab882564,0x2bfe1a48,0xab3ba6d7,0x295f29ed,
0x261f2b64,0xa61fab64,0x261f2b64,0xa61fab64,0x2b3ba6d7,0xa95f29ed,0x2564ab88,0x1a482bfe,0xa78bab0e,0x2ba824a5,0xab0e278b,0x24a5aba8,0xaadd281d,0xa0b2abea,0x2bc323c6,0xa8c42a6d,
0x28722aa7,0xaaa72872,0xa872aaa7,0x2aa7a872,0x2a6da8c4,0x2add281d,0xa0b22bea,0xabc323c6,0xa913aa2f,0x1e46abf6,0x2a2fa913,0x2bf61e46,0xa9ed295f,0xab882564,0xabfe9a48,0xab3ba6d7,
0x80002c00,0x80002c00,0x80002c00,0x80002c00,0x9a48abfe,0xa6d7ab3b,0xa9eda95f,0xab88a564,0x1e462bf6,0x2a2f2913,0x2bf69e46,0x2913aa2f,0xa0b2abea,0xabc3a3c6,0xa8c42a6d,0x281d2add,
0x223e2bd9,0x2bd9a23e,0xa23eabd9,0xabd9223e,0xa3c6abc3,0xaa6d28c4,0x2add281d,0x20b2abea,0x24a52ba8,0x278bab0e,0xaba824a5,0x2b0e278b,0xa564ab88,0x9a482bfe,0x26d7ab3b,0xa9ed295f,
0x261f2b64,0xa61fab64,0x261f2b64,0xa61fab64,0xa6d7ab3b,0x29ed295f,0xab88a564,0x2bfe9a48,0x278b2b0e,0xaba8a4a5,0x2b0ea78b,0xa4a52ba8,0xa81daadd,0x2beaa0b2,0xa3c62bc3,0xaa6da8c4,
0x28722aa7,0xaaa72872,0xa872aaa7,0x2aa7a872,0xa8c4aa6d,0x281daadd,0x2bea20b2,0x23c62bc3,0x29132a2f,0x9e462bf6,0xaa2f2913,0xabf69e46,0xa95fa9ed,0xa564ab88,0x1a48abfe,0x26d7ab3b,
0x80002c00,0x80002c00,0x80002c00,0x80002c00,0xabfe1a48,0xab3b26d7,0xa95f29ed,0xa5642b88,0x9e46abf6,0xaa2fa913,0xabf61e46,0xa9132a2f,0x2beaa0b2,0x23c6abc3,0xaa6da8c4,0xaadd281d,
0x223e2bd9,0x2bd9a23e,0xa23eabd9,0xabd9223e,0xabc323c6,0x28c42a6d,0x281daadd,0xabeaa0b2,0xa4a5aba8,0xa78b2b0e,0x2ba8a4a5,0xab0ea78b,0x2b88a564,0xabfe9a48,0x2b3b26d7,0xa95fa9ed,
0x261f2b64,0xa61fab64,0x261f2b64,0xa61fab64,0xab3b26d7,0x295fa9ed,0xa5642b88,0x9a48abfe,0xa78bab0e,0x2ba824a5,0xab0e278b,0x24a5aba8,0x2adda81d,0x20b22bea,0xabc3a3c6,0x28c4aa6d,
0x28722aa7,0xaaa72872,0xa872aaa7,0x2aa7a872,0xaa6d28c4,0xaadda81d,0x20b2abea,0x2bc3a3c6,0xa913aa2f,0x1e46abf6,0x2a2fa913,0x2bf61e46,0x29eda95f,0x2b88a564,0x2bfe1a48,0x2b3b26d7,
0x2c000000,0x2c000000,0x2c000000,0x2c000000,0x2bfe9a48,0x2b3ba6d7,0x295fa9ed,0x2564ab88,0x2bf69e46,0x2913aa2f,0x9e46abf6,0xaa2fa913,0x2beaa0b2,0x23c6abc3,0xaa6da8c4,0xaadd281d,
0x2bd9a23e,0xa23eabd9,0xabd9223e,0x223e2bd9,0x2bc3a3c6,0xa8c4aa6d,0xa81d2add,0x2bea20b2,0x2ba8a4a5,0xab0ea78b,0x24a52ba8,0x278bab0e,0x2b88a564,0xabfe9a48,0x2b3b26d7,0xa95fa9ed,
0x2b64a61f,0xab64261f,0x2b64a61f,0xab64261f,0x2b3ba6d7,0xa95f29ed,0x2564ab88,0x1a482bfe,0x2b0ea78b,0xa4a52ba8,0xa78bab0e,0x2ba824a5,0x2adda81d,0x20b22bea,0xabc3a3c6,0x28c4aa6d,
0x2aa7a872,0x28722aa7,0xaaa72872,0xa872aaa7,0x2a6da8c4,0x2add281d,0xa0b22bea,0xabc323c6,0x2a2fa913,0x2bf61e46,0x29132a2f,0x9e462bf6,0x29eda95f,0x2b88a564,0x2bfe1a48,0x2b3b26d7,
0x2c000000,0x2c000000,0x2c000000,0x2c000000,0x9a48abfe,0xa6d7ab3b,0xa9eda95f,0xab88a564,0xabf61e46,0xa9132a2f,0x1e462bf6,0x2a2f2913,0x20b22bea,0x2bc323c6,0x28c4aa6d,0xa81daadd,
0x2bd9a23e,0xa23eabd9,0xabd9223e,0x223e2bd9,0xa3c6abc3,0xaa6d28c4,0x2add281d,0x20b2abea,0xaba824a5,0x2b0e278b,0xa4a5aba8,0xa78b2b0e,0x25642b88,0x1a48abfe,0xa6d72b3b,0x29eda95f,
0x2b64a61f,0xab64261f,0x2b64a61f,0xab64261f,0xa6d7ab3b,0x29ed295f,0xab88a564,0x2bfe9a48,0xab0e278b,0x24a5aba8,0x278b2b0e,0xaba8a4a5,0x281d2add,0xabea20b2,0x23c6abc3,0x2a6d28c4,
0x2aa7a872,0x28722aa7,0xaaa72872,0xa872aaa7,0xa8c4aa6d,0x281daadd,0x2bea20b2,0x23c62bc3,0xaa2f2913,0xabf69e46,0xa913aa2f,0x1e46abf6,0x295f29ed,0x25642b88,0x9a482bfe,0xa6d72b3b,
0x2c000000,0x2c000000,0x2c000000,0x2c000000,0xabfe1a48,0xab3b26d7,0xa95f29ed,0xa5642b88,0x2bf69e46,0x2913aa2f,0x9e46abf6,0xaa2fa913,0xabea20b2,0xa3c62bc3,0x2a6d28c4,0x2adda81d,
0x2bd9a23e,0xa23eabd9,0xabd9223e,0x223e2bd9,0xabc323c6,0x28c42a6d,0x281daadd,0xabeaa0b2,0x2ba8a4a5,0xab0ea78b,0x24a52ba8,0x278bab0e,0xab882564,0x2bfe1a48,0xab3ba6d7,0x295f29ed,
0x2b64a61f,0xab64261f,0x2b64a61f,0xab64261f,0xab3b26d7,0x295fa9ed,0xa5642b88,0x9a48abfe,0x2b0ea78b,0xa4a52ba8,0xa78bab0e,0x2ba824a5,0xaadd281d,0xa0b2abea,0x2bc323c6,0xa8c42a6d,
0x2aa7a872,0x28722aa7,0xaaa72872,0xa872aaa7,0xaa6d28c4,0xaadda81d,0x20b2abea,0x2bc3a3c6,0x2a2fa913,0x2bf61e46,0x29132a2f,0x9e462bf6,0xa9ed295f,0xab882564,0xabfe9a48,0xab3ba6d7,
0x2c000000,0x2c000000,0x2c000000,0x2c000000,0x1a482bfe,0x26d72b3b,0x29ed295f,0x2b882564,0xabf61e46,0xa9132a2f,0x1e462bf6,0x2a2f2913,0xa0b2abea,0xabc3a3c6,0xa8c42a6d,0x281d2add,
0x2bd9a23e,0xa23eabd9,0xabd9223e,0x223e2bd9,0x23c62bc3,0x2a6da8c4,0xaadda81d,0xa0b22bea,0xaba824a5,0x2b0e278b,0xa4a5aba8,0xa78b2b0e,0xa564ab88,0x9a482bfe,0x26d7ab3b,0xa9ed295f,
0x2b64a61f,0xab64261f,0x2b64a61f,0xab64261f,0x26d72b3b,0xa9eda95f,0x2b882564,0xabfe1a48,0xab0e278b,0x24a5aba8,0x278b2b0e,0xaba8a4a5,0xa81daadd,0x2beaa0b2,0xa3c62bc3,0xaa6da8c4,
0x2aa7a872,0x28722aa7,0xaaa72872,0xa872aaa7,0x28c42a6d,0xa81d2add,0xabeaa0b2,0xa3c6abc3,0xaa2f2913,0xabf69e46,0xa913aa2f,0x1e46abf6,0xa95fa9ed,0xa564ab88,0x1a48abfe,0x26d7ab3b,
0x80002c00,0x80002c00,0x80002c00,0x80002c00,0x80002c00,0x261f2b64,0x29a829a8,0x2b64261f,0x80002c00,0x29a829a8,0x2c000000,0x29a8a9a8,0x80002c00,0x2b64261f,0x29a8a9a8,0xa61fab64,
0x80002c00,0x2c000000,0x8000ac00,0xac000000,0x80002c00,0x2b64a61f,0xa9a8a9a8,0xa61f2b64,0x80002c00,0x29a8a9a8,0xac000000,0x29a829a8,0x80002c00,0x261fab64,0xa9a829a8,0x2b64a61f,
0x80002c00,0x8000ac00,0x80002c00,0x8000ac00,0x80002c00,0xa61fab64,0x29a829a8,0xab64a61f,0x80002c00,0xa9a8a9a8,0x2c000000,0xa9a829a8,0x80002c00,0xab64a61f,0x29a8a9a8,0x261f2b64,
0x80002c00,0xac000000,0x8000ac00,0x2c000000,0x80002c00,0xab64261f,0xa9a8a9a8,0x261fab64,0x80002c00,0xa9a829a8,0xac000000,0xa9a8a9a8,0x80002c00,0xa61f2b64,0xa9a829a8,0xab64261f,
0x80002c00,0x80002c00,0x80002c00,0x80002c00,0x2c000000,0x2b64a61f,0x29a8a9a8,0x261fab64,0x8000ac00,0xa9a8a9a8,0xac000000,0xa9a829a8,0xac000000,0xa61f2b64,0x29a829a8,0x2b64a61f,
0x80002c00,0x2c000000,0x8000ac00,0xac000000,0x2c000000,0xa61fab64,0xa9a829a8,0x2b64261f,0x8000ac00,0xa9a829a8,0x2c000000,0xa9a8a9a8,0xac000000,0x2b64261f,0xa9a8a9a8,0x261f2b64,
0x80002c00,0x8000ac00,0x80002c00,0x8000ac00,0x2c000000,0xab64261f,0x29a8a9a8,0xa61f2b64,0x8000ac00,0x29a829a8,0xac000000,0x29a8a9a8,0xac000000,0x261fab64,0x29a829a8,0xab64261f,
0x80002c00,0xac000000,0x8000ac00,0x2c000000,0x2c000000,0x261f2b64,0xa9a829a8,0xab64a61f,0x8000ac00,0x29a8a9a8,0x2c000000,0x29a829a8,0xac000000,0xab64a61f,0xa9a8a9a8,0xa61fab64,
0x80002c00,0x80002c00,0x80002c00,0x80002c00,0x8000ac00,0xa61fab64,0xa9a8a9a8,0xab64a61f,0x80002c00,0x29a829a8,0x2c000000,0x29a8a9a8,0x8000ac00,0xab64a61f,0xa9a829a8,0x261f2b64,
0x80002c00,0x2c000000,0x8000ac00,0xac000000,0x8000ac00,0xab64261f,0x29a829a8,0x261fab64,0x80002c00,0x29a8a9a8,0xac000000,0x29a829a8,0x8000ac00,0xa61f2b64,0x29a8a9a8,0xab64261f,
0x80002c00,0x8000ac00,0x80002c00,0x8000ac00,0x8000ac00,0x261f2b64,0xa9a8a9a8,0x2b64261f,0x80002c00,0xa9a8a9a8,0x2c000000,0xa9a829a8,0x8000ac00,0x2b64261f,0xa9a829a8,0xa61fab64,
0x80002c00,0xac000000,0x8000ac00,0x2c000000,0x8000ac00,0x2b64a61f,0x29a829a8,0xa61f2b64,0x80002c00,0xa9a829a8,0xac000000,0xa9a8a9a8,0x8000ac00,0x261fab64,0x29a8a9a8,0x2b64a61f,
0x80002c00,0x80002c00,0x80002c00,0x80002c00,0xac000000,0xab64261f,0xa9a829a8,0xa61f2b64,0x8000ac00,0xa9a8a9a8,0xac000000,0xa9a829a8,0x2c000000,0x261fab64,0xa9a8a9a8,0xab64261f,
0x80002c00,0x2c000000,0x8000ac00,0xac000000,0xac000000,0x261f2b64,0x29a8a9a8,0xab64a61f,0x8000ac00,0xa9a829a8,0x2c000000,0xa9a8a9a8,0x2c000000,0xab64a61f,0x29a829a8,0xa61fab64,
0x80002c00,0x8000ac00,0x80002c00,0x8000ac00,0xac000000,0x2b64a61f,0xa9a829a8,0x261fab64,0x8000ac00,0x29a829a8,0xac000000,0x29a8a9a8,0x2c000000,0xa61f2b64,0xa9a8a9a8,0x2b64a61f,
0x80002c00,0xac000000,0x8000ac00,0x2c000000,0xac000000,0xa61fab64,0x29a8a9a8,0x2b64261f,0x8000ac00,0x29a8a9a8,0x2c000000,0x29a829a8,0x2c000000,0x2b64261f,0x29a829a8,0x261f2b64,
0x2c000000,0x2c000000,0x2c000000,0x2c000000,0x2c000000,0x2b64a61f,0x29a8a9a8,0x261fab64,0x2c000000,0x29a8a9a8,0xac00,0xa9a8a9a8,0x2c000000,0x261fab64,0xa9a8a9a8,0xab64261f,
0x2c000000,0xac00,0xac000000,0x2c00,0x2c000000,0xa61fab64,0xa9a829a8,0x2b64261f,0x2c000000,0xa9a8a9a8,0x2c00,0x29a8a9a8,0x2c000000,0xab64a61f,0x29a829a8,0xa61fab64,
0x2c000000,0xac000000,0x2c000000,0xac000000,0x2c000000,0xab64261f,0x29a8a9a8,0xa61f2b64,0x2c000000,0xa9a829a8,0xac00,0x29a829a8,0x2c000000,0xa61f2b64,0xa9a8a9a8,0x2b64a61f,
0x2c000000,0x2c00,0xac000000,0xac00,0x2c000000,0x261f2b64,0xa9a829a8,0xab64a61f,0x2c000000,0x29a829a8,0x2c00,0xa9a829a8,0x2c000000,0x2b64261f,0x29a829a8,0x261f2b64,
0x2c000000,0x2c000000,0x2c000000,0x2c000000,0xac00,0xa61fab64,0xa9a8a9a8,0xab64a61f,0xac000000,0xa9a829a8,0x2c00,0x29a829a8,0x2c00,0x2b64261f,0x29a8a9a8,0xa61fab64,
0x2c000000,0xac00,0xac000000,0x2c00,0xac00,0xab64261f,0x29a829a8,0x261fab64,0xac000000,0x29a829a8,0xac00,0xa9a829a8,0x2c00,0x261fab64,0xa9a829a8,0x2b64a61f,
0x2c000000,0xac000000,0x2c000000,0xac000000,0xac00,0x261f2b64,0xa9a8a9a8,0x2b64261f,0xac000000,0x29a8a9a8,0x2c00,0xa9a8a9a8,0x2c00,0xab64a61f,0x29a8a9a8,0x261f2b64,
0x2c000000,0x2c00,0xac000000,0xac00,0xac00,0x2b64a61f,0x29a829a8,0xa61f2b64,0xac000000,0xa9a8a9a8,0xac00,0x29a8a9a8,0x2c00,0xa61f2b64,0xa9a829a8,0xab64261f,
0x2c000000,0x2c000000,0x2c000000,0x2c000000,0xac000000,0xab64261f,0xa9a829a8,0xa61f2b64,0x2c000000,0x29a8a9a8,0xac00,0xa9a8a9a8,0xac000000,0xa61f2b64,0x29a829a8,0x2b64a61f,
0x2c000000,0xac00,0xac000000,0x2c00,0xac000000,0x261f2b64,0x29a8a9a8,0xab64a61f,0x2c000000,0xa9a8a9a8,0x2c00,0x29a8a9a8,0xac000000,0x2b64261f,0xa9a8a9a8,0x261f2b64,
0x2c000000,0xac000000,0x2c000000,0xac000000,0xac000000,0x2b64a61f,0xa9a829a8,0x261fab64,0x2c000000,0xa9a829a8,0xac00,0x29a829a8,0xac000000,0x261fab64,0x29a829a8,0xab64261f,
0x2c000000,0x2c00,0xac000000,0xac00,0xac000000,0xa61fab64,0x29a8a9a8,0x2b64261f,0x2c000000,0x29a829a8,0x2c00,0xa9a829a8,0xac000000,0xab64a61f,0xa9a8a9a8,0xa61fab64,
0x2c000000,0x2c000000,0x2c000000,0x2c000000,0x2c00,0x261f2b64,0x29a829a8,0x2b64261f,0xac000000,0xa9a829a8,0x2c00,0x29a829a8,0xac00,0xab64a61f,0xa9a829a8,0x261f2b64,
0x2c000000,0xac00,0xac000000,0x2c00,0x2c00,0x2b64a61f,0xa9a8a9a8,0xa61f2b64,0xac000000,0x29a829a8,0xac00,0xa9a829a8,0xac00,0xa61f2b64,0x29a8a9a8,0xab64261f,
0x2c000000,0xac000000,0x2c000000,0xac000000,0x2c00,0xa61fab64,0x29a829a8,0xab64a61f,0xac000000,0x29a8a9a8,0x2c00,0xa9a8a9a8,0xac00,0x2b64261f,0xa9a829a8,0xa61fab64,
0x2c000000,0x2c00,0xac000000,0xac00,0x2c00,0xab64261f,0xa9a8a9a8,0x261fab64,0xac000000,0xa9a8a9a8,0xac00,0x29a8a9a8,0xac00,0x261fab64,0x29a8a9a8,0x2b64a61f,
0x3f800000,0x0,0x3f7fffb1,0xbb490fc6,0x3f800000,0x0,0x3f7fe705,0xbce22a7a,0x3f800000,0x0,0x3f7fa6e3,0xbd55880e,0x3f800000,0x0,0x3f7f3f54,0xbd9cecf9,
0x3f800000,0x0,0x3f7eb069,0xbdcefdb7,0x3f800000,0x0,0x3f7dfa38,0xbe007745,0x3f800000,0x0,0x3f7d1cdd,0xbe195be0,0x3f800000,0x0,0x3f7c187a,0xbe3228d4,
0x3f800000,0x0,0x3f7aed37,0xbe4ada4f,0x3f800000,0x0,0x3f799b43,0xbe636c83,0x3f800000,0x0,0x3f7822d1,0xbe7bdba4,0x3f800000,0x0,0x3f76841b,0xbe8a11f7,
0x3f800000,0x0,0x3f74bf62,0xbe9620d2,0x3f800000,0x0,0x3f72d4eb,0xbea21887,0x3f800000,0x0,0x3f70c501,0xbeadf73c,0x3f800000,0x0,0x3f6e8ff8,0xbeb9bb1e,
0x3f7ffb11,0xbc490e90,0x3f7ff84a,0xbc7b514b,0x3f7e70b0,0xbde1bc2e,0x3f7d906e,0xbe0cec4a,0x3f7a7302,0xbe541501,0x3f77583a,0xbe840270,0x3f740bdd,0xbe9aa086,0x3f6d67a1,0xbebf926f,
0x3f6b4b0c,0xbec9b953,0x3f5fe4e9,0xbef840c8,0x3f604621,0xbef6e0cb,0x3f4f0417,0xbf16999f,0x3f531849,0xbf10d3cd,0x3f3b0629,0xbf2ecef7,0x3f43e200,0xbf24d225,0x3f24381a,0xbf446337,
0x3f32c8c9,0xbf373a23,0x3f0af1b7,0xbf570348,0x3f1ff6cb,0xbf47de65,0x3edf28a9,0xbf666771,0x3f0b9a6b,0xbf5695e5,0x3ea5129f,0xbf725470,0x3eebcbbb,0xbf633c5a,0x3e5101f1,0xbf7a9c59,
0x3ebe1d4a,0xbf6db293,0x3da973ba,0xbf7f1f49,0x3e8e9a22,0xbf75dec6,0xbd2351cb,0xbf7fcbe2,0x3e3b6ecf,0xbf7baccd,0xbe25c58c,0xbf7c9f8a,0x3dafb680,0xbf7f0e58,0xbe901c2c,0xbf75a67b,
0x3f7fec43,0xbcc90ab0,0x3f7fe705,0xbce22a7a,0x3f79c79d,0xbe605c13,0x3f7822d1,0xbe7bdba4,0x3f6a09a7,0xbecf7bca,0x3f644fac,0xbee79a5d,0x3f514d3d,0xbf13682a,0x3f456439,0xbf2302d3,
0x3f3085bb,0xbf396842,0x3f1ce11f,0xbf4a4c87,0x3f08f59b,0xbf584853,0x3ed97cb9,0xbf67c187,0x3eb8442a,0xbf6ed89e,0x3e5d4b1a,0xbf79f35e,0x3e2f10a2,0xbf7c3b28,0xbb490fc6,0xbf7fffb1,
0xbcc90ab0,0xbf7fec43,0xbe636c83,0xbf799b43,0xbe605c13,0xbf79c79d,0xbedc53c1,0xbf671599,0xbecf7bca,0xbf6a09a7,0xbf1e1e22,0xbf495521,0xbf13682a,0xbf514d3d,0xbf466354,0xbf21cbfb,
0xbf396842,0xbf3085bb,0xbf650479,0xbee4cbfe,0xbf584853,0xbf08f59b,0xbf788486,0xbe75c35a,0xbf6ed89e,0xbeb8442a,0xbf7ff0e3,0xbcafea69,0xbf7c3b28,0xbe2f10a2,0xbf7aed37,0x3e4ada4f,
0x3f7fd397,0xbd16c32c,0x3f7fcbe2,0xbd2351cb,0x3f721352,0xbea68f12,0x3f6faf1b,0xbeb3dca5,0x3f4f7a1f,0xbf15f6d9,0x3f476085,0xbf209393,0x3f1b02c6,0xbf4bbbf8,0x3f0af1b7,0xbf570348,
0x3eb263ef,0xbf6ff573,0x3e80f8c0,0xbf77beb7,0x3d7b2b74,0xbf7f84ab,0xbd83db0a,0xbf7f7808,0xbe6c9a7f,0xbf791298,0xbebf926f,0xbf6d67a1,0xbf00e7e4,0xbf5d2d53,0xbf256bcb,0xbf436051,
0xbf3b8f3b,0xbf2e3bde,0xbf5a5ba0,0xbf05a02c,0xbf660f88,0xbee0924f,0xbf7940c0,0xbe698bba,0xbf7cbfc9,0xbe22abb6,0xbf7efcc9,0x3db5f8da,0xbf7dabcc,0x3e09cf86,0xbf6afb8c,0x3ecb2aae,
0xbf68bf3c,0x3ed53641,0xbf3f41fc,0x3f2a2a80,0xbf3fc767,0x3f299415,0xbf003a06,0x3f5d924d,0xbf064b82,0x3f59f26a,0xbe5101f1,0x3f7a9c59,0xbe827dc0,0x3f778bc5,0x3de7fa9a,0x3f7e5a38,
0x3f7ffec4,0xbbc90f88,0x3f7ffd39,0xbc16cb58,0x3f7f9c18,0xbd621469,0x3f7f1f49,0xbda973ba,0x3f7e9bc9,0xbdd53db9,0x3f7cdf6c,0xbe1f917b,0x3f7cfe73,0xbe1c76de,0x3f7940c0,0xbe698bba,
0x3f7ac516,0xbe4dee60,0x3f74484b,0xbe9920fe,0x3f77f110,0xbe7ee6e1,0x3f6dfcf2,0xbebca7af,0x3f748422,0xbe97a117,0x3f666771,0xbedf28a9,0x3f708066,0xbeaf713a,0x3f5d924d,0xbf003a06,
0x3f6be858,0xbec6d529,0x3f5389c7,0xbf102dd5,0x3f66becc,0xbeddbe79,0x3f485bca,0xbf1f599f,0x3f6106f2,0xbef41f07,0x3f3c17d9,0xbf2da859,0x3f5ac450,0xbf04f484,0x3f2ecef7,0xbf3b0629,
0x3f53fac3,0xbf0f8784,0x3f209393,0xbf476085,0x3f4cae79,0xbf19c200,0x3f11796b,0xbf52a649,0x3f44e3f5,0xbf239da9,0x3f019573,0xbf5cc7d1,0x3f3ca003,0xbf2d1469,0x3ee1fb6a,0xbf65b710,
0x3f7ff4e6,0xbc96c9b6,0x3f7ff0e3,0xbcafea69,0x3f7c7eb0,0xbe28defc,0x3f7b3bab,0xbe44b0b9,0x3f73913f,0xbe9d9e78,0x3f6f20b0,0xbeb6ccc3,0x3f655e0b,0xbee363fa,0x3f5bfb34,0xbf02ef9f,
0x3f5233c6,0xbf121eb0,0x3f425b89,0xbf269de3,0x3f3a7ca4,0xbf2f61a5,0x3f2302d3,0xbf456439,0x3f1ebc12,0xbf48d8b3,0x3efdbabb,0xbf5e5aa6,0x3eff17b2,0xbf5df6be,0x3eadf73c,0xbf70c501,
0x3ebb31a0,0xbf6e46be,0x3e3228d4,0xbf7c187a,0x3e667c66,0xbf796e4e,0x3b490fc6,0xbf7fffb1,0x3da3308c,0xbf7f2f9d,0xbe2bf804,0xbf7c5d3a,0xbd8a200a,0xbf7f6ac7,0xbeab0201,0xbf714c7a,
0xbe5a3997,0xbf7a1e84,0xbefafef7,0xbf5f20db,0xbeb554ec,0xbf6f6830,0xbf21cbfb,0xbf466354,0xbef9a02d,0xbf5f8327,0xbf4154e1,0xbf27ce61,0xbf1c420c,0xbf4ac77f,0xbf5b2c79,0xbf044889,
0x3f7fe129,0xbcfb49ba,0x3f7fdaaf,0xbd0a342f,0x3f764d97,0xbe8b9507,0x3f74484b,0xbe9920fe,0x3f5df6be,0xbeff17b2,0x3f570348,0xbf0af1b7,0x3f385216,0xbf31a81d,0x3f2a2a80,0xbf3f41fc,
0x3f07a136,0xbf591e6a,0x3ee1fb6a,0xbf65b710,0x3e9d9e78,0xbf73913f,0x3e3e853e,0xbf7b87b2,0x3d8a200a,0xbf7f6ac7,0xbda973ba,0xbf7f1f49,0xbe354098,0xbf7bf531,0xbeb0eacb,0xbf703b37,
0xbed25a09,0xbf696591,0xbf1553b7,0xbf4fefa8,0xbf1ebc12,0xbf48d8b3,0xbf476085,0xbf209393,0xbf4ac77f,0xbf1c420c,0xbf6afb8c,0xbecb2aae,0xbf6aab7b,0xbecc9b8b,0xbf7d906e,0xbe0cec4a,
0xbf7c7eb0,0xbe28defc,0xbf7dc68c,0x3e06b26e,0xbf7f2f9d,0x3da3308c,0xbf6b99fb,0x3ec8477c,0xbf7294f8,0x3ea395c5,0xbf485bca,0x3f1f599f,0xbf577026,0x3f0a48ad,0xbf16999f,0x3f4f0417,
0x3f7fc38f,0xbd2fe007,0x3f7fba9e,0xbd3c6dd5,0x3f6d1c1d,0xbec1071e,0x3f6a5ad9,0xbece0bea,0x3f3ebc1b,0xbf2ac082,0x3f3592e7,0xbf34768f,0x3ef41f07,0xbf6106f2,0x3ed0eb2a,0xbf69b7e4,
0x3e1c76de,0xbf7cfe73,0x3d55880e,0xbf7fa6e3,0xbe419b37,0xbf7b61fc,0xbe9f1cdf,0xbf73530e,0xbf0242b1,0xbf5c61c7,0xbf21cbfb,0xbf466354,0xbf44e3f5,0xbf239da9,0xbf5e5aa6,0xbefdbabb,
0xbf708066,0xbeaf713a,0xbf7d1cdd,0xbe195be0,0xbf7ffec4,0xbbc90f88,0xbf79f35e,0x3e5d4b1a,0xbf718f57,0x3ea986c4,0xbf554aa4,0x3f0d9281,0xbf46e22a,0x3f212ff9,0xbf140c5f,0x3f50d937,
0xbf04f484,0x3f5ac450,0xbe7bdba4,0x3f7822d1,0xbe4dee60,0x3f7ac516,0x3df475c0,0x3f7e2b72,0x3e1008b7,0x3f7d7474,0x3eeff7fb,0x3f62241a,0x3eee9479,0x3f628210,0x3f4154e1,0x3f27ce61,
0x3f800000,0x0,0x3f800000,0x0,0x3f7fffb1,0xbb490fc6,0x3f7fe705,0xbce22a7a,0x3f7ffec4,0xbbc90f88,0x3f7f9c18,0xbd621469,0x3f7ffd39,0xbc16cb58,0x3f7f1f49,0xbda973ba,
0x3f7ffb11,0xbc490e90,0x3f7e70b0,0xbde1bc2e,0x3f7ff84a,0xbc7b514b,0x3f7d906e,0xbe0cec4a,0x3f7ff4e6,0xbc96c9b6,0x3f7c7eb0,0xbe28defc,0x3f7ff0e3,0xbcafea69,0x3f7b3bab,0xbe44b0b9,
0x3f7fec43,0xbcc90ab0,0x3f79c79d,0xbe605c13,0x3f7fe705,0xbce22a7a,0x3f7822d1,0xbe7bdba4,0x3f7fe129,0xbcfb49ba,0x3f764d97,0xbe8b9507,0x3f7fdaaf,0xbd0a342f,0x3f74484b,0xbe9920fe,
0x3f7fd397,0xbd16c32c,0x3f721352,0xbea68f12,0x3f7fcbe2,0xbd2351cb,0x3f6faf1b,0xbeb3dca5,0x3f7fc38f,0xbd2fe007,0x3f6d1c1d,0xbec1071e,0x3f7fba9e,0xbd3c6dd5,0x3f6a5ad9,0xbece0bea,
0x3f800000,0x0,0x3f800000,0x0,0x3f7eb069,0xbdcefdb7,0x3f7dfa38,0xbe007745,0x3f7ac516,0xbe4dee60,0x3f77f110,0xbe7ee6e1,0x3f74484b,0xbe9920fe,0x3f6dfcf2,0xbebca7af,
0x3f6b4b0c,0xbec9b953,0x3f604621,0xbef6e0cb,0x3f5fe4e9,0xbef840c8,0x3f4f0417,0xbf16999f,0x3f5233c6,0xbf121eb0,0x3f3a7ca4,0xbf2f61a5,0x3f425b89,0xbf269de3,0x3f2302d3,0xbf456439,
0x3f3085bb,0xbf396842,0x3f08f59b,0xbf584853,0x3f1ce11f,0xbf4a4c87,0x3ed97cb9,0xbf67c187,0x3f07a136,0xbf591e6a,0x3e9d9e78,0xbf73913f,0x3ee1fb6a,0xbf65b710,0x3e3e853e,0xbf7b87b2,
0x3eb263ef,0xbf6ff573,0x3d7b2b74,0xbf7f84ab,0x3e80f8c0,0xbf77beb7,0xbd83db0a,0xbf7f7808,0x3e1c76de,0xbf7cfe73,0xbe419b37,0xbf7b61fc,0x3d55880e,0xbf7fa6e3,0xbe9f1cdf,0xbf73530e,
0x3f800000,0x0,0x3f800000,0x0,0x3f7aed37,0xbe4ada4f,0x3f799b43,0xbe636c83,0x3f6be858,0xbec6d529,0x3f66becc,0xbeddbe79,0x3f5389c7,0xbf102dd5,0x3f485bca,0xbf1f599f,
0x3f32c8c9,0xbf373a23,0x3f1ff6cb,0xbf47de65,0x3f0af1b7,0xbf570348,0x3edf28a9,0xbf666771,0x3ebb31a0,0xbf6e46be,0x3e667c66,0xbf796e4e,0x3e3228d4,0xbf7c187a,0x3b490fc6,0xbf7fffb1,
0xbcc90ab0,0xbf7fec43,0xbe605c13,0xbf79c79d,0xbe636c83,0xbf799b43,0xbedc53c1,0xbf671599,0xbed25a09,0xbf696591,0xbf1ebc12,0xbf48d8b3,0xbf1553b7,0xbf4fefa8,0xbf476085,0xbf209393,
0xbf3b8f3b,0xbf2e3bde,0xbf660f88,0xbee0924f,0xbf5a5ba0,0xbf05a02c,0xbf7940c0,0xbe698bba,0xbf708066,0xbeaf713a,0xbf7ffec4,0xbbc90f88,0xbf7d1cdd,0xbe195be0,0xbf79f35e,0x3e5d4b1a,
0x3f800000,0x0,0x3f800000,0x0,0x3f74bf62,0xbe9620d2,0x3f72d4eb,0xbea21887,0x3f53fac3,0xbf0f8784,0x3f4cae79,0xbf19c200,0x3f209393,0xbf476085,0x3f11796b,0xbf52a649,
0x3ebe1d4a,0xbf6db293,0x3e8e9a22,0xbf75dec6,0x3da973ba,0xbf7f1f49,0xbd2351cb,0xbf7fcbe2,0xbe5a3997,0xbf7a1e84,0xbeb554ec,0xbf6f6830,0xbefafef7,0xbf5f20db,0xbf21cbfb,0xbf466354,
0xbf396842,0xbf3085bb,0xbf584853,0xbf08f59b,0xbf650479,0xbee4cbfe,0xbf788486,0xbe75c35a,0xbf7c7eb0,0xbe28defc,0xbf7f2f9d,0x3da3308c,0xbf7dc68c,0x3e06b26e,0xbf6b99fb,0x3ec8477c,
0xbf68bf3c,0x3ed53641,0xbf3fc767,0x3f299415,0xbf3f41fc,0x3f2a2a80,0xbf003a06,0x3f5d924d,0xbf04f484,0x3f5ac450,0xbe4dee60,0x3f7ac516,0xbe7bdba4,0x3f7822d1,0x3df475c0,0x3f7e2b72,
0x3f800000,0x0,0x3f800000,0x0,0x3f7fa6e3,0xbd55880e,0x3f7f3f54,0xbd9cecf9,0x3f7e9bc9,0xbdd53db9,0x3f7cfe73,0xbe1c76de,0x3f7cdf6c,0xbe1f917b,0x3f7940c0,0xbe698bba,
0x3f7a7302,0xbe541501,0x3f740bdd,0xbe9aa086,0x3f77583a,0xbe840270,0x3f6d67a1,0xbebf926f,0x3f73913f,0xbe9d9e78,0x3f655e0b,0xbee363fa,0x3f6f20b0,0xbeb6ccc3,0x3f5bfb34,0xbf02ef9f,
0x3f6a09a7,0xbecf7bca,0x3f514d3d,0xbf13682a,0x3f644fac,0xbee79a5d,0x3f456439,0xbf2302d3,0x3f5df6be,0xbeff17b2,0x3f385216,0xbf31a81d,0x3f570348,0xbf0af1b7,0x3f2a2a80,0xbf3f41fc,
0x3f4f7a1f,0xbf15f6d9,0x3f1b02c6,0xbf4bbbf8,0x3f476085,0xbf209393,0x3f0af1b7,0xbf570348,0x3f3ebc1b,0xbf2ac082,0x3ef41f07,0xbf6106f2,0x3f3592e7,0xbf34768f,0x3ed0eb2a,0xbf69b7e4,
0x3f800000,0x0,0x3f800000,0x0,0x3f7d1cdd,0xbe195be0,0x3f7c187a,0xbe3228d4,0x3f748422,0xbe97a117,0x3f708066,0xbeaf713a,0x3f666771,0xbedf28a9,0x3f5d924d,0xbf003a06,
0x3f531849,0xbf10d3cd,0x3f43e200,0xbf24d225,0x3f3b0629,0xbf2ecef7,0x3f24381a,0xbf446337,0x3f1ebc12,0xbf48d8b3,0x3eff17b2,0xbf5df6be,0x3efdbabb,0xbf5e5aa6,0x3eadf73c,0xbf70c501,
0x3eb8442a,0xbf6ed89e,0x3e2f10a2,0xbf7c3b28,0x3e5d4b1a,0xbf79f35e,0xbb490fc6,0xbf7fffb1,0x3d8a200a,0xbf7f6ac7,0xbe354098,0xbf7bf531,0xbda973ba,0xbf7f1f49,0xbeb0eacb,0xbf703b37,
0xbe6c9a7f,0xbf791298,0xbf00e7e4,0xbf5d2d53,0xbebf926f,0xbf6d67a1,0xbf256bcb,0xbf436051,0xbf0242b1,0xbf5c61c7,0xbf44e3f5,0xbf239da9,0xbf21cbfb,0xbf466354,0xbf5e5aa6,0xbefdbabb,
0x3f800000,0x0,0x3f800000,0x0,0x3f7822d1,0xbe7bdba4,0x3f76841b,0xbe8a11f7,0x3f6106f2,0xbef41f07,0x3f5ac450,0xbf04f484,0x3f3c17d9,0xbf2da859,0x3f2ecef7,0xbf3b0629,
0x3f0b9a6b,0xbf5695e5,0x3eebcbbb,0xbf633c5a,0x3ea5129f,0xbf725470,0x3e5101f1,0xbf7a9c59,0x3da3308c,0xbf7f2f9d,0xbd8a200a,0xbf7f6ac7,0xbe2bf804,0xbf7c5d3a,0xbeab0201,0xbf714c7a,
0xbecf7bca,0xbf6a09a7,0xbf13682a,0xbf514d3d,0xbf1e1e22,0xbf495521,0xbf466354,0xbf21cbfb,0xbf4ac77f,0xbf1c420c,0xbf6aab7b,0xbecc9b8b,0xbf6afb8c,0xbecb2aae,0xbf7d906e,0xbe0cec4a,
0xbf7cbfc9,0xbe22abb6,0xbf7dabcc,0x3e09cf86,0xbf7efcc9,0x3db5f8da,0xbf6afb8c,0x3ecb2aae,0xbf718f57,0x3ea986c4,0xbf46e22a,0x3f212ff9,0xbf554aa4,0x3f0d9281,0xbf140c5f,0x3f50d937,
0x3f800000,0x0,0x3f800000,0x0,0x3f70c501,0xbeadf73c,0x3f6e8ff8,0xbeb9bb1e,0x3f44e3f5,0xbf239da9,0x3f3ca003,0xbf2d1469,0x3f019573,0xbf5cc7d1,0x3ee1fb6a,0xbf65b710,
0x3e3b6ecf,0xbf7baccd,0x3dafb680,0xbf7f0e58,0xbe25c58c,0xbf7c9f8a,0xbe901c2c,0xbf75a67b,0xbef9a02d,0xbf5f8327,0xbf1c420c,0xbf4ac77f,0xbf4154e1,0xbf27ce61,0xbf5b2c79,0xbf044889,
0xbf6ed89e,0xbeb8442a,0xbf7c3b28,0xbe2f10a2,0xbf7ff0e3,0xbcafea69,0xbf7aed37,0x3e4ada4f,0xbf7294f8,0x3ea395c5,0xbf577026,0x3f0a48ad,0xbf485bca,0x3f1f599f,0xbf16999f,0x3f4f0417,
0xbf064b82,0x3f59f26a,0xbe827dc0,0x3f778bc5,0xbe5101f1,0x3f7a9c59,0x3de7fa9a,0x3f7e5a38,0x3e1008b7,0x3f7d7474,0x3eee9479,0x3f628210,0x3eeff7fb,0x3f62241a,0x3f4154e1,0x3f27ce61,
0x80002c00,0x1e462bf6,0x223e2bd9,0x24a52ba8,0x80002c00,0x278b2b0e,0x2aa72872,0x2bf61e46,0x80002c00,0x2a2f2913,0x2bd9a23e,0x278bab0e,0x80002c00,0x2ba824a5,0x2872aaa7,0xa913aa2f,
0x80002c00,0x2bf69e46,0xa23eabd9,0xaba824a5,0x80002c00,0x2b0ea78b,0xaaa7a872,0x9e462bf6,0x80002c00,0x2913aa2f,0xabd9223e,0x2b0e278b,0x80002c00,0x24a5aba8,0xa8722aa7,0x2a2fa913,
0x80002c00,0x9e46abf6,0x223e2bd9,0xa4a5aba8,0x80002c00,0xa78bab0e,0x2aa72872,0xabf69e46,0x80002c00,0xaa2fa913,0x2bd9a23e,0xa78b2b0e,0x80002c00,0xaba8a4a5,0x2872aaa7,0x29132a2f,
0x80002c00,0xabf61e46,0xa23eabd9,0x2ba8a4a5,0x80002c00,0xab0e278b,0xaaa7a872,0x1e46abf6,0x80002c00,0xa9132a2f,0xabd9223e,0xab0ea78b,0x80002c00,0xa4a52ba8,0xa8722aa7,0xaa2f2913,
0x261f2b64,0x278b2b0e,0x28722aa7,0x29132a2f,0x2b64a61f,0x2913aa2f,0x223eabd9,0xa4a5aba8,0xa61fab64,0xaba8a4a5,0xaaa72872,0x9e462bf6,0xab64261f,0x1e462bf6,0x2bd9223e,0x278bab0e,
0x261f2b64,0x2b0ea78b,0xa872aaa7,0xaa2f2913,0x2b64a61f,0xaa2fa913,0xa23e2bd9,0x2ba8a4a5,0xa61fab64,0xa4a52ba8,0x2aa7a872,0xabf69e46,0xab64261f,0x2bf69e46,0xabd9a23e,0x2b0e278b,
0x261f2b64,0xa78bab0e,0x28722aa7,0xa913aa2f,0x2b64a61f,0xa9132a2f,0x223eabd9,0x24a52ba8,0xa61fab64,0x2ba824a5,0xaaa72872,0x1e46abf6,0xab64261f,0x9e46abf6,0x2bd9223e,0xa78b2b0e,
0x261f2b64,0xab0e278b,0xa872aaa7,0x2a2fa913,0x2b64a61f,0x2a2f2913,0xa23e2bd9,0xaba824a5,0xa61fab64,0x24a5aba8,0x2aa7a872,0x2bf61e46,0xab64261f,0xabf61e46,0xabd9a23e,0xab0ea78b,
0x29a829a8,0x2a2f2913,0x2aa72872,0x2b0e278b,0xa9a8a9a8,0xaba8a4a5,0xabd9223e,0xaa2f2913,0x29a829a8,0x2bf69e46,0x2872aaa7,0xa4a5aba8,0xa9a8a9a8,0xab0e278b,0x223e2bd9,0x2bf61e46,
0x29a829a8,0x2913aa2f,0xaaa7a872,0xa78b2b0e,0xa9a8a9a8,0xa4a52ba8,0x2bd9a23e,0xa913aa2f,0x29a829a8,0x9e46abf6,0xa8722aa7,0x2ba8a4a5,0xa9a8a9a8,0x278b2b0e,0xa23eabd9,0x9e462bf6,
0x29a829a8,0xaa2fa913,0x2aa72872,0xab0ea78b,0xa9a8a9a8,0x2ba824a5,0xabd9223e,0x2a2fa913,0x29a829a8,0xabf61e46,0x2872aaa7,0x24a52ba8,0xa9a8a9a8,0x2b0ea78b,0x223e2bd9,0xabf69e46,
0x29a829a8,0xa9132a2f,0xaaa7a872,0x278bab0e,0xa9a8a9a8,0x24a5aba8,0x2bd9a23e,0x29132a2f,0x29a829a8,0x1e462bf6,0xa8722aa7,0xaba824a5,0xa9a8a9a8,0xa78bab0e,0xa23eabd9,0x1e46abf6,
0x2b64261f,0x2ba824a5,0x2bd9223e,0x2bf61e46,0xa61f2b64,0x1e462bf6,0x28722aa7,0x2b0e278b,0xab64a61f,0xab0e278b,0xa23e2bd9,0x29132a2f,0x261fab64,0xaa2fa913,0xaaa72872,0x24a52ba8,
0x2b64261f,0x24a5aba8,0xabd9a23e,0x9e462bf6,0xa61f2b64,0x2bf69e46,0xa872aaa7,0xa78b2b0e,0xab64a61f,0x278b2b0e,0x223eabd9,0xaa2f2913,0x261fab64,0xa9132a2f,0x2aa7a872,0xaba824a5,
0x2b64261f,0xaba8a4a5,0x2bd9223e,0xabf69e46,0xa61f2b64,0x9e46abf6,0x28722aa7,0xab0ea78b,0xab64a61f,0x2b0ea78b,0xa23e2bd9,0xa913aa2f,0x261fab64,0x2a2f2913,0xaaa72872,0xa4a5aba8,
0x2b64261f,0xa4a52ba8,0xabd9a23e,0x1e46abf6,0xa61f2b64,0xabf61e46,0xa872aaa7,0x278bab0e,0xab64a61f,0xa78bab0e,0x223eabd9,0x2a2fa913,0x261fab64,0x2913aa2f,0x2aa7a872,0x2ba8a4a5,
0x2c000000,0x2bf69e46,0x2bd9a23e,0x2ba8a4a5,0x2c000000,0x2b0ea78b,0x2872aaa7,0x1e46abf6,0x2c000000,0x2913aa2f,0xa23eabd9,0xab0ea78b,0x2c000000,0x24a5aba8,0xaaa7a872,0xaa2f2913,
0x2c000000,0x9e46abf6,0xabd9223e,0x24a52ba8,0x2c000000,0xa78bab0e,0xa8722aa7,0x2bf61e46,0x2c000000,0xaa2fa913,0x223e2bd9,0x278bab0e,0x2c000000,0xaba8a4a5,0x2aa72872,0xa913aa2f,
0x2c000000,0xabf61e46,0x2bd9a23e,0xaba824a5,0x2c000000,0xab0e278b,0x2872aaa7,0x9e462bf6,0x2c000000,0xa9132a2f,0xa23eabd9,0x2b0e278b,0x2c000000,0xa4a52ba8,0xaaa7a872,0x2a2fa913,
0x2c000000,0x1e462bf6,0xabd9223e,0xa4a5aba8,0x2c000000,0x278b2b0e,0xa8722aa7,0xabf69e46,0x2c000000,0x2a2f2913,0x223e2bd9,0xa78b2b0e,0x2c000000,0x2ba824a5,0x2aa72872,0x29132a2f,
0x2b64a61f,0x2b0ea78b,0x2aa7a872,0x2a2fa913,0xa61fab64,0xaa2fa913,0xabd9a23e,0xaba824a5,0xab64261f,0xa4a52ba8,0x28722aa7,0x2bf61e46,0x261f2b64,0x2bf69e46,0x223eabd9,0xab0ea78b,
0x2b64a61f,0xa78bab0e,0xaaa72872,0x29132a2f,0xa61fab64,0xa9132a2f,0x2bd9223e,0xa4a5aba8,0xab64261f,0x2ba824a5,0xa872aaa7,0x9e462bf6,0x261f2b64,0x9e46abf6,0xa23e2bd9,0x278bab0e,
0x2b64a61f,0xab0e278b,0x2aa7a872,0xaa2f2913,0xa61fab64,0x2a2f2913,0xabd9a23e,0x2ba8a4a5,0xab64261f,0x24a5aba8,0x28722aa7,0xabf69e46,0x261f2b64,0xabf61e46,0x223eabd9,0x2b0e278b,
0x2b64a61f,0x278b2b0e,0xaaa72872,0xa913aa2f,0xa61fab64,0x2913aa2f,0x2bd9223e,0x24a52ba8,0xab64261f,0xaba8a4a5,0xa872aaa7,0x1e46abf6,0x261f2b64,0x1e462bf6,0xa23e2bd9,0xa78b2b0e,
0x29a8a9a8,0x2913aa2f,0x2872aaa7,0x278bab0e,0xa9a829a8,0xa4a52ba8,0x223e2bd9,0x29132a2f,0x29a8a9a8,0x9e46abf6,0xaaa7a872,0xaba824a5,0xa9a829a8,0x278b2b0e,0x2bd9a23e,0x1e46abf6,
0x29a8a9a8,0xaa2fa913,0xa8722aa7,0x2b0e278b,0xa9a829a8,0x2ba824a5,0xa23eabd9,0xaa2f2913,0x29a8a9a8,0xabf61e46,0x2aa72872,0xa4a5aba8,0xa9a829a8,0x2b0ea78b,0xabd9223e,0x2bf61e46,
0x29a8a9a8,0xa9132a2f,0x2872aaa7,0xa78b2b0e,0xa9a829a8,0x24a5aba8,0x223e2bd9,0xa913aa2f,0x29a8a9a8,0x1e462bf6,0xaaa7a872,0x2ba8a4a5,0xa9a829a8,0xa78bab0e,0x2bd9a23e,0x9e462bf6,
0x29a8a9a8,0x2a2f2913,0xa8722aa7,0xab0ea78b,0xa9a829a8,0xaba8a4a5,0xa23eabd9,0x2a2fa913,0x29a8a9a8,0x2bf69e46,0x2aa72872,0x24a52ba8,0xa9a829a8,0xab0e278b,0xabd9223e,0xabf69e46,
0x261fab64,0x24a5aba8,0x223eabd9,0x1e46abf6,0x2b64261f,0x2bf69e46,0x2aa7a872,0x278bab0e,0xa61f2b64,0x278b2b0e,0x2bd9223e,0x2a2fa913,0xab64a61f,0xa9132a2f,0x28722aa7,0x2ba8a4a5,
0x261fab64,0xaba8a4a5,0xa23e2bd9,0x2bf61e46,0x2b64261f,0x9e46abf6,0xaaa72872,0x2b0e278b,0xa61f2b64,0x2b0ea78b,0xabd9a23e,0x29132a2f,0xab64a61f,0x2a2f2913,0xa872aaa7,0x24a52ba8,
0x261fab64,0xa4a52ba8,0x223eabd9,0x9e462bf6,0x2b64261f,0xabf61e46,0x2aa7a872,0xa78b2b0e,0xa61f2b64,0xa78bab0e,0x2bd9223e,0xaa2f2913,0xab64a61f,0x2913aa2f,0x28722aa7,0xaba824a5,
0x261fab64,0x2ba824a5,0xa23e2bd9,0xabf69e46,0x2b64261f,0x1e462bf6,0xaaa72872,0xab0ea78b,0xa61f2b64,0xab0e278b,0xabd9a23e,0xa913aa2f,0xab64a61f,0xaa2fa913,0xa872aaa7,0xa4a5aba8,
0x80002c00,0x80002c00,0x80002c00,0x80002c00,0x1e462bf6,0x278b2b0e,0x2a2f2913,0x2ba824a5,0x223e2bd9,0x2aa72872,0x2bd9a23e,0x2872aaa7,0x24a52ba8,0x2bf61e46,0x278bab0e,0xa913aa2f,
0x261f2b64,0x2b64a61f,0xa61fab64,0xab64261f,0x278b2b0e,0x2913aa2f,0xaba8a4a5,0x1e462bf6,0x28722aa7,0x223eabd9,0xaaa72872,0x2bd9223e,0x29132a2f,0xa4a5aba8,0x9e462bf6,0x278bab0e,
0x29a829a8,0xa9a8a9a8,0x29a829a8,0xa9a8a9a8,0x2a2f2913,0xaba8a4a5,0x2bf69e46,0xab0e278b,0x2aa72872,0xabd9223e,0x2872aaa7,0x223e2bd9,0x2b0e278b,0xaa2f2913,0xa4a5aba8,0x2bf61e46,
0x2b64261f,0xa61f2b64,0xab64a61f,0x261fab64,0x2ba824a5,0x1e462bf6,0xab0e278b,0xaa2fa913,0x2bd9223e,0x28722aa7,0xa23e2bd9,0xaaa72872,0x2bf61e46,0x2b0e278b,0x29132a2f,0x24a52ba8,
0x80002c00,0x80002c00,0x80002c00,0x80002c00,0x2bf69e46,0x2b0ea78b,0x2913aa2f,0x24a5aba8,0xa23eabd9,0xaaa7a872,0xabd9223e,0xa8722aa7,0xaba824a5,0x9e462bf6,0x2b0e278b,0x2a2fa913,
0x261f2b64,0x2b64a61f,0xa61fab64,0xab64261f,0x2b0ea78b,0xaa2fa913,0xa4a52ba8,0x2bf69e46,0xa872aaa7,0xa23e2bd9,0x2aa7a872,0xabd9a23e,0xaa2f2913,0x2ba8a4a5,0xabf69e46,0x2b0e278b,
0x29a829a8,0xa9a8a9a8,0x29a829a8,0xa9a8a9a8,0x2913aa2f,0xa4a52ba8,0x9e46abf6,0x278b2b0e,0xaaa7a872,0x2bd9a23e,0xa8722aa7,0xa23eabd9,0xa78b2b0e,0xa913aa2f,0x2ba8a4a5,0x9e462bf6,
0x2b64261f,0xa61f2b64,0xab64a61f,0x261fab64,0x24a5aba8,0x2bf69e46,0x278b2b0e,0xa9132a2f,0xabd9a23e,0xa872aaa7,0x223eabd9,0x2aa7a872,0x9e462bf6,0xa78b2b0e,0xaa2f2913,0xaba824a5,
0x80002c00,0x80002c00,0x80002c00,0x80002c00,0x9e46abf6,0xa78bab0e,0xaa2fa913,0xaba8a4a5,0x223e2bd9,0x2aa72872,0x2bd9a23e,0x2872aaa7,0xa4a5aba8,0xabf69e46,0xa78b2b0e,0x29132a2f,
0x261f2b64,0x2b64a61f,0xa61fab64,0xab64261f,0xa78bab0e,0xa9132a2f,0x2ba824a5,0x9e46abf6,0x28722aa7,0x223eabd9,0xaaa72872,0x2bd9223e,0xa913aa2f,0x24a52ba8,0x1e46abf6,0xa78b2b0e,
0x29a829a8,0xa9a8a9a8,0x29a829a8,0xa9a8a9a8,0xaa2fa913,0x2ba824a5,0xabf61e46,0x2b0ea78b,0x2aa72872,0xabd9223e,0x2872aaa7,0x223e2bd9,0xab0ea78b,0x2a2fa913,0x24a52ba8,0xabf69e46,
0x2b64261f,0xa61f2b64,0xab64a61f,0x261fab64,0xaba8a4a5,0x9e46abf6,0x2b0ea78b,0x2a2f2913,0x2bd9223e,0x28722aa7,0xa23e2bd9,0xaaa72872,0xabf69e46,0xab0ea78b,0xa913aa2f,0xa4a5aba8,
0x80002c00,0x80002c00,0x80002c00,0x80002c00,0xabf61e46,0xab0e278b,0xa9132a2f,0xa4a52ba8,0xa23eabd9,0xaaa7a872,0xabd9223e,0xa8722aa7,0x2ba8a4a5,0x1e46abf6,0xab0ea78b,0xaa2f2913,
0x261f2b64,0x2b64a61f,0xa61fab64,0xab64261f,0xab0e278b,0x2a2f2913,0x24a5aba8,0xabf61e46,0xa872aaa7,0xa23e2bd9,0x2aa7a872,0xabd9a23e,0x2a2fa913,0xaba824a5,0x2bf61e46,0xab0ea78b,
0x29a829a8,0xa9a8a9a8,0x29a829a8,0xa9a8a9a8,0xa9132a2f,0x24a5aba8,0x1e462bf6,0xa78bab0e,0xaaa7a872,0x2bd9a23e,0xa8722aa7,0xa23eabd9,0x278bab0e,0x29132a2f,0xaba824a5,0x1e46abf6,
0x2b64261f,0xa61f2b64,0xab64a61f,0x261fab64,0xa4a52ba8,0xabf61e46,0xa78bab0e,0x2913aa2f,0xabd9a23e,0xa872aaa7,0x223eabd9,0x2aa7a872,0x1e46abf6,0x278bab0e,0x2a2fa913,0x2ba8a4a5,
0x2c000000,0x2c000000,0x2c000000,0x2c000000,0x2bf69e46,0x2b0ea78b,0x2913aa2f,0x24a5aba8,0x2bd9a23e,0x2872aaa7,0xa23eabd9,0xaaa7a872,0x2ba8a4a5,0x1e46abf6,0xab0ea78b,0xaa2f2913,
0x2b64a61f,0xa61fab64,0xab64261f,0x261f2b64,0x2b0ea78b,0xaa2fa913,0xa4a52ba8,0x2bf69e46,0x2aa7a872,0xabd9a23e,0x28722aa7,0x223eabd9,0x2a2fa913,0xaba824a5,0x2bf61e46,0xab0ea78b,
0x29a8a9a8,0xa9a829a8,0x29a8a9a8,0xa9a829a8,0x2913aa2f,0xa4a52ba8,0x9e46abf6,0x278b2b0e,0x2872aaa7,0x223e2bd9,0xaaa7a872,0x2bd9a23e,0x278bab0e,0x29132a2f,0xaba824a5,0x1e46abf6,
0x261fab64,0x2b64261f,0xa61f2b64,0xab64a61f,0x24a5aba8,0x2bf69e46,0x278b2b0e,0xa9132a2f,0x223eabd9,0x2aa7a872,0x2bd9223e,0x28722aa7,0x1e46abf6,0x278bab0e,0x2a2fa913,0x2ba8a4a5,
0x2c000000,0x2c000000,0x2c000000,0x2c000000,0x9e46abf6,0xa78bab0e,0xaa2fa913,0xaba8a4a5,0xabd9223e,0xa8722aa7,0x223e2bd9,0x2aa72872,0x24a52ba8,0x2bf61e46,0x278bab0e,0xa913aa2f,
0x2b64a61f,0xa61fab64,0xab64261f,0x261f2b64,0xa78bab0e,0xa9132a2f,0x2ba824a5,0x9e46abf6,0xaaa72872,0x2bd9223e,0xa872aaa7,0xa23e2bd9,0x29132a2f,0xa4a5aba8,0x9e462bf6,0x278bab0e,
0x29a8a9a8,0xa9a829a8,0x29a8a9a8,0xa9a829a8,0xaa2fa913,0x2ba824a5,0xabf61e46,0x2b0ea78b,0xa8722aa7,0xa23eabd9,0x2aa72872,0xabd9223e,0x2b0e278b,0xaa2f2913,0xa4a5aba8,0x2bf61e46,
0x261fab64,0x2b64261f,0xa61f2b64,0xab64a61f,0xaba8a4a5,0x9e46abf6,0x2b0ea78b,0x2a2f2913,0xa23e2bd9,0xaaa72872,0xabd9a23e,0xa872aaa7,0x2bf61e46,0x2b0e278b,0x29132a2f,0x24a52ba8,
0x2c000000,0x2c000000,0x2c000000,0x2c000000,0xabf61e46,0xab0e278b,0xa9132a2f,0xa4a52ba8,0x2bd9a23e,0x2872aaa7,0xa23eabd9,0xaaa7a872,0xaba824a5,0x9e462bf6,0x2b0e278b,0x2a2fa913,
0x2b64a61f,0xa61fab64,0xab64261f,0x261f2b64,0xab0e278b,0x2a2f2913,0x24a5aba8,0xabf61e46,0x2aa7a872,0xabd9a23e,0x28722aa7,0x223eabd9,0xaa2f2913,0x2ba8a4a5,0xabf69e46,0x2b0e278b,
0x29a8a9a8,0xa9a829a8,0x29a8a9a8,0xa9a829a8,0xa9132a2f,0x24a5aba8,0x1e462bf6,0xa78bab0e,0x2872aaa7,0x223e2bd9,0xaaa7a872,0x2bd9a23e,0xa78b2b0e,0xa913aa2f,0x2ba8a4a5,0x9e462bf6,
0x261fab64,0x2b64261f,0xa61f2b64,0xab64a61f,0xa4a52ba8,0xabf61e46,0xa78bab0e,0x2913aa2f,0x223eabd9,0x2aa7a872,0x2bd9223e,0x28722aa7,0x9e462bf6,0xa78b2b0e,0xaa2f2913,0xaba824a5,
0x2c000000,0x2c000000,0x2c000000,0x2c000000,0x1e462bf6,0x278b2b0e,0x2a2f2913,0x2ba824a5,0xabd9223e,0xa8722aa7,0x223e2bd9,0x2aa72872,0xa4a5aba8,0xabf69e46,0xa78b2b0e,0x29132a2f,
0x2b64a61f,0xa61fab64,0xab64261f,0x261f2b64,0x278b2b0e,0x2913aa2f,0xaba8a4a5,0x1e462bf6,0xaaa72872,0x2bd9223e,0xa872aaa7,0xa23e2bd9,0xa913aa2f,0x24a52ba8,0x1e46abf6,0xa78b2b0e,
0x29a8a9a8,0xa9a829a8,0x29a8a9a8,0xa9a829a8,0x2a2f2913,0xaba8a4a5,0x2bf69e46,0xab0e278b,0xa8722aa7,0xa23eabd9,0x2aa72872,0xabd9223e,0xab0ea78b,0x2a2fa913,0x24a52ba8,0xabf69e46,
0x261fab64,0x2b64261f,0xa61f2b64,0xab64a61f,0x2ba824a5,0x1e462bf6,0xab0e278b,0xaa2fa913,0xa23e2bd9,0xaaa72872,0xabd9a23e,0xa872aaa7,0xabf69e46,0xab0ea78b,0xa913aa2f,0xa4a5aba8,
0x80002c00,0x80002c00,0x80002c00,0x80002c00,0x80002c00,0x261f2b64,0x29a829a8,0x2b64261f,0x80002c00,0x29a829a8,0x2c000000,0x29a8a9a8,0x80002c00,0x2b64261f,0x29a8a9a8,0xa61fab64,
0x80002c00,0x2c000000,0x8000ac00,0xac000000,0x80002c00,0x2b64a61f,0xa9a8a9a8,0xa61f2b64,0x80002c00,0x29a8a9a8,0xac000000,0x29a829a8,0x80002c00,0x261fab64,0xa9a829a8,0x2b64a61f,
0x80002c00,0x8000ac00,0x80002c00,0x8000ac00,0x80002c00,0xa61fab64,0x29a829a8,0xab64a61f,0x80002c00,0xa9a8a9a8,0x2c000000,0xa9a829a8,0x80002c00,0xab64a61f,0x29a8a9a8,0x261f2b64,
0x80002c00,0xac000000,0x8000ac00,0x2c000000,0x80002c00,0xab64261f,0xa9a8a9a8,0x261fab64,0x80002c00,0xa9a829a8,0xac000000,0xa9a8a9a8,0x80002c00,0xa61f2b64,0xa9a829a8,0xab64261f,
0x80002c00,0x80002c00,0x80002c00,0x80002c00,0x2c000000,0x2b64a61f,0x29a8a9a8,0x261fab64,0x8000ac00,0xa9a8a9a8,0xac000000,0xa9a829a8,0xac000000,0xa61f2b64,0x29a829a8,0x2b64a61f,
0x80002c00,0x2c000000,0x8000ac00,0xac000000,0x2c000000,0xa61fab64,0xa9a829a8,0x2b64261f,0x8000ac00,0xa9a829a8,0x2c000000,0xa9a8a9a8,0xac000000,0x2b64261f,0xa9a8a9a8,0x261f2b64,
0x80002c00,0x8000ac00,0x80002c00,0x8000ac00,0x2c000000,0xab64261f,0x29a8a9a8,0xa61f2b64,0x8000ac00,0x29a829a8,0xac000000,0x29a8a9a8,0xac000000,0x261fab64,0x29a829a8,0xab64261f,
0x80002c00,0xac000000,0x8000ac00,0x2c000000,0x2c000000,0x261f2b64,0xa9a829a8,0xab64a61f,0x8000ac00,0x29a8a9a8,0x2c000000,0x29a829a8,0xac000000,0xab64a61f,0xa9a8a9a8,0xa61fab64,
0x80002c00,0x80002c00,0x80002c00,0x80002c00,0x8000ac00,0xa61fab64,0xa9a8a9a8,0xab64a61f,0x80002c00,0x29a829a8,0x2c000000,0x29a8a9a8,0x8000ac00,0xab64a61f,0xa9a829a8,0x261f2b64,
0x80002c00,0x2c000000,0x8000ac00,0xac000000,0x8000ac00,0xab64261f,0x29a829a8,0x261fab64,0x80002c00,0x29a8a9a8,0xac000000,0x29a829a8,0x8000ac00,0xa61f2b64,0x29a8a9a8,0xab64261f,
0x80002c00,0x8000ac00,0x80002c00,0x8000ac00,0x8000ac00,0x261f2b64,0xa9a8a9a8,0x2b64261f,0x80002c00,0xa9a8a9a8,0x2c000000,0xa9a829a8,0x8000ac00,0x2b64261f,0xa9a829a8,0xa61fab64,
0x80002c00,0xac000000,0x8000ac00,0x2c000000,0x8000ac00,0x2b64a61f,0x29a829a8,0xa61f2b64,0x80002c00,0xa9a829a8,0xac000000,0xa9a8a9a8,0x8000ac00,0x261fab64,0x29a8a9a8,0x2b64a61f,
0x80002c00,0x80002c00,0x80002c00,0x80002c00,0xac000000,0xab64261f,0xa9a829a8,0xa61f2b64,0x8000ac00,0xa9a8a9a8,0xac000000,0xa9a829a8,0x2c000000,0x261fab64,0xa9a8a9a8,0xab64261f,
0x80002c00,0x2c000000,0x8000ac00,0xac000000,0xac000000,0x261f2b64,0x29a8a9a8,0xab64a61f,0x8000ac00,0xa9a829a8,0x2c000000,0xa9a8a9a8,0x2c000000,0xab64a61f,0x29a829a8,0xa61fab64,
0x80002c00,0x8000ac00,0x80002c00,0x8000ac00,0xac000000,0x2b64a61f,0xa9a829a8,0x261fab64,0x8000ac00,0x29a829a8,0xac000000,0x29a8a9a8,0x2c000000,0xa61f2b64,0xa9a8a9a8,0x2b64a61f,
0x80002c00,0xac000000,0x8000ac00,0x2c000000,0xac000000,0xa61fab64,0x29a8a9a8,0x2b64261f,0x8000ac00,0x29a8a9a8,0x2c000000,0x29a829a8,0x2c000000,0x2b64261f,0x29a829a8,0x261f2b64,
0x2c000000,0x2c000000,0x2c000000,0x2c000000,0x2c000000,0x2b64a61f,0x29a8a9a8,0x261fab64,0x2c000000,0x29a8a9a8,0xac00,0xa9a8a9a8,0x2c000000,0x261fab64,0xa9a8a9a8,0xab64261f,
0x2c000000,0xac00,0xac000000,0x2c00,0x2c000000,0xa61fab64,0xa9a829a8,0x2b64261f,0x2c000000,0xa9a8a9a8,0x2c00,0x29a8a9a8,0x2c000000,0xab64a61f,0x29a829a8,0xa61fab64,
0x2c000000,0xac000000,0x2c000000,0xac000000,0x2c000000,0xab64261f,0x29a8a9a8,0xa61f2b64,0x2c000000,0xa9a829a8,0xac00,0x29a829a8,0x2c000000,0xa61f2b64,0xa9a8a9a8,0x2b64a61f,
0x2c000000,0x2c00,0xac000000,0xac00,0x2c000000,0x261f2b64,0xa9a829a8,0xab64a61f,0x2c000000,0x29a829a8,0x2c00,0xa9a829a8,0x2c000000,0x2b64261f,0x29a829a8,0x261f2b64,
0x2c000000,0x2c000000,0x2c000000,0x2c000000,0xac00,0xa61fab64,0xa9a8a9a8,0xab64a61f,0xac000000,0xa9a829a8,0x2c00,0x29a829a8,0x2c00,0x2b64261f,0x29a8a9a8,0xa61fab64,
0x2c000000,0xac00,0xac000000,0x2c00,0xac00,0xab64261f,0x29a829a8,0x261fab64,0xac000000,0x29a829a8,0xac00,0xa9a829a8,0x2c00,0x261fab64,0xa9a829a8,0x2b64a61f,
0x2c000000,0xac000000,0x2c000000,0xac000000,0xac00,0x261f2b64,0xa9a8a9a8,0x2b64261f,0xac000000,0x29a8a9a8,0x2c00,0xa9a8a9a8,0x2c00,0xab64a61f,0x29a8a9a8,0x261f2b64,
0x2c000000,0x2c00,0xac000000,0xac00,0xac00,0x2b64a61f,0x29a829a8,0xa61f2b64,0xac000000,0xa9a8a9a8,0xac00,0x29a8a9a8,0x2c00,0xa61f2b64,0xa9a829a8,0xab64261f,
0x2c000000,0x2c000000,0x2c000000,0x2c000000,0xac000000,0xab64261f,0xa9a829a8,0xa61f2b64,0x2c000000,0x29a8a9a8,0xac00,0xa9a8a9a8,0xac000000,0xa61f2b64,0x29a829a8,0x2b64a61f,
0x2c000000,0xac00,0xac000000,0x2c00,0xac000000,0x261f2b64,0x29a8a9a8,0xab64a61f,0x2c000000,0xa9a8a9a8,0x2c00,0x29a8a9a8,0xac000000,0x2b64261f,0xa9a8a9a8,0x261f2b64,
0x2c000000,0xac000000,0x2c000000,0xac000000,0xac000000,0x2b64a61f,0xa9a829a8,0x261fab64,0x2c000000,0xa9a829a8,0xac00,0x29a829a8,0xac000000,0x261fab64,0x29a829a8,0xab64261f,
0x2c000000,0x2c00,0xac000000,0xac00,0xac000000,0xa61fab64,0x29a8a9a8,0x2b64261f,0x2c000000,0x29a829a8,0x2c00,0xa9a829a8,0xac000000,0xab64a61f,0xa9a8a9a8,0xa61fab64,
0x2c000000,0x2c000000,0x2c000000,0x2c000000,0x2c00,0x261f2b64,0x29a829a8,0x2b64261f,0xac000000,0xa9a829a8,0x2c00,0x29a829a8,0xac00,0xab64a61f,0xa9a829a8,0x261f2b64,
0x2c000000,0xac00,0xac000000,0x2c00,0x2c00,0x2b64a61f,0xa9a8a9a8,0xa61f2b64,0xac000000,0x29a829a8,0xac00,0xa9a829a8,0xac00,0xa61f2b64,0x29a8a9a8,0xab64261f,
0x2c000000,0xac000000,0x2c000000,0xac000000,0x2c00,0xa61fab64,0x29a829a8,0xab64a61f,0xac000000,0x29a8a9a8,0x2c00,0xa9a8a9a8,0xac00,0x2b64261f,0xa9a829a8,0xa61fab64,
0x2c000000,0x2c00,0xac000000,0xac00,0x2c00,0xab64261f,0xa9a8a9a8,0x261fab64,0xac000000,0xa9a8a9a8,0xac00,0x29a8a9a8,0xac00,0x261fab64,0x29a8a9a8,0x2b64a61f,
0x3f800000,0x0,0x3f7ffec4,0xbbc90f88,0x3f800000,0x0,0x3f7fe129,0xbcfb49ba,0x3f800000,0x0,0x3f7f9c18,0xbd621469,0x3f800000,0x0,0x3f7f2f9d,0xbda3308c,
0x3f800000,0x0,0x3f7e9bc9,0xbdd53db9,0x3f800000,0x0,0x3f7de0b1,0xbe039502,0x3f800000,0x0,0x3f7cfe73,0xbe1c76de,0x3f800000,0x0,0x3f7bf531,0xbe354098,
0x3f800000,0x0,0x3f7ac516,0xbe4dee60,0x3f800000,0x0,0x3f796e4e,0xbe667c66,0x3f800000,0x0,0x3f77f110,0xbe7ee6e1,0x3f800000,0x0,0x3f764d97,0xbe8b9507,
0x3f800000,0x0,0x3f748422,0xbe97a117,0x3f800000,0x0,0x3f7294f8,0xbea395c5,0x3f800000,0x0,0x3f708066,0xbeaf713a,0x3f800000,0x0,0x3f6e46be,0xbebb31a0,
0x3f7fec43,0xbcc90ab0,0x3f7fe129,0xbcfb49ba,0x3f7e1324,0xbdfab273,0x3f7cfe73,0xbe1c76de,0x3f79c79d,0xbe605c13,0x3f764d97,0xbe8b9507,0x3f731447,0xbea09ae5,0x3f6be858,0xbec6d529,
0x3f6a09a7,0xbecf7bca,0x3f5df6be,0xbeff17b2,0x3f5ebe05,0xbefc5d27,0x3f4cae79,0xbf19c200,0x3f514d3d,0xbf13682a,0x3f385216,0xbf31a81d,0x3f41d870,0xbf273656,0x3f212ff9,0xbf46e22a,
0x3f3085bb,0xbf396842,0x3f07a136,0xbf591e6a,0x3f1d7fd1,0xbf49d112,0x3ed8106b,0xbf6816a8,0x3f08f59b,0xbf584853,0x3e9d9e78,0xbf73913f,0x3ee63375,0xbf64aa59,0x3e419b37,0xbf7b61fc,
0x3eb8442a,0xbf6ed89e,0x3d8a200a,0xbf7f6ac7,0x3e888e93,0xbf76ba07,0xbd621469,0xbf7f9c18,0x3e2f10a2,0xbf7c3b28,0xbe354098,0xbf7bf531,0x3d96a905,0xbf7f4e6d,0xbe97a117,0xbf748422,
0x3f7fb10f,0xbd48fb30,0x3f7f9c18,0xbd621469,0x3f7853f8,0xbe78cfcc,0x3f764d97,0xbe8b9507,0x3f676bd8,0xbedae880,0x3f6106f2,0xbef41f07,0x3f4d9f02,0xbf187fc0,0x3f40d0da,0xbf286605,
0x3f2beb4a,0xbf3daef9,0x3f173c07,0xbf4e8d90,0x3f039c3d,0xbf5b941a,0x3ecc9b8b,0xbf6aab7b,0x3eac7cd4,0xbf710908,0x3e419b37,0xbf7b61fc,0x3e164083,0xbf7d3aac,0xbcfb49ba,0xbf7fe129,
0xbd48fb30,0xbf7fb10f,0xbe7ee6e1,0xbf77f110,0xbe78cfcc,0xbf7853f8,0xbee900b7,0xbf63f473,0xbedae880,0xbf676bd8,0xbf239da9,0xbf44e3f5,0xbf187fc0,0xbf4d9f02,0xbf4ac77f,0xbf1c420c,
0xbf3daef9,0xbf2beb4a,0xbf6816a8,0xbed8106b,0xbf5b941a,0xbf039c3d,0xbf7a1e84,0xbe5a3997,0xbf710908,0xbeac7cd4,0xbf7ffec4,0x3bc90f88,0xbf7d3aac,0xbe164083,0xbf796e4e,0x3e667c66,
0x3f7f4e6d,0xbd96a905,0x3f7f2f9d,0xbda3308c,0x3f6ed89e,0xbeb8442a,0x3f6be858,0xbec6d529,0x3f49d112,0xbf1d7fd1,0x3f40d0da,0xbf286605,0x3f13682a,0xbf514d3d,0x3f0242b1,0xbf5c61c7,
0x3ea09ae5,0xbf731447,0x3e5a3997,0xbf7a1e84,0x3cc90ab0,0xbf7fec43,0xbdd53db9,0xbf7e9bc9,0xbe888e93,0xbf76ba07,0xbed25a09,0xbf696591,0xbf08f59b,0xbf584853,0xbf2d1469,0xbf3ca003,
0xbf41d870,0xbf273656,0xbf5f8327,0xbef9a02d,0xbf6a09a7,0xbecf7bca,0xbf7b61fc,0xbe419b37,0xbf7e1324,0xbdfab273,0xbf7de0b1,0x3e039502,0xbf7c3b28,0x3e2f10a2,0xbf66becc,0x3eddbe79,
0xbf64aa59,0x3ee63375,0xbf385216,0x3f31a81d,0xbf396842,0x3f3085bb,0xbeee9479,0x3f628210,0xbefc5d27,0x3f5ebe05,0xbe28defc,0x3f7c7eb0,0xbe605c13,0x3f79c79d,0x3e1c76de,0x3f7cfe73,
0x3f7ffb11,0xbc490e90,0x3f7ff4e6,0xbc96c9b6,0x3f7f84ab,0xbd7b2b74,0x3f7eea9d,0xbdbc3ac3,0x3f7e70b0,0xbde1bc2e,0x3f7c7eb0,0xbe28defc,0x3f7cbfc9,0xbe22abb6,0x3f78b47b,0xbe72b651,
0x3f7a7302,0xbe541501,0x3f73913f,0xbe9d9e78,0x3f778bc5,0xbe827dc0,0x3f6d1c1d,0xbec1071e,0x3f740bdd,0xbe9aa086,0x3f655e0b,0xbee363fa,0x3f6ff573,0xbeb263ef,0x3f5c61c7,0xbf0242b1,
0x3f6b4b0c,0xbec9b953,0x3f5233c6,0xbf121eb0,0x3f660f88,0xbee0924f,0x3f46e22a,0xbf212ff9,0x3f604621,0xbef6e0cb,0x3f3a7ca4,0xbf2f61a5,0x3f59f26a,0xbf064b82,0x3f2d1469,0xbf3ca003,
0x3f531849,0xbf10d3cd,0x3f1ebc12,0xbf48d8b3,0x3f4bbbf8,0xbf1b02c6,0x3f0f8784,0xbf53fac3,0x3f43e200,0xbf24d225,0x3eff17b2,0xbf5df6be,0x3f3b8f3b,0xbf2e3bde,0x3eddbe79,0xbf66becc,
0x3f7fd397,0xbd16c32c,0x3f7fc38f,0xbd2fe007,0x3f7baccd,0xbe3b6ecf,0x3f7a1e84,0xbe5a3997,0x3f721352,0xbea68f12,0x3f6d1c1d,0xbec1071e,0x3f633c5a,0xbeebcbbb,0x3f591e6a,0xbf07a136,
0x3f4f7a1f,0xbf15f6d9,0x3f3ebc1b,0xbf2ac082,0x3f373a23,0xbf32c8c9,0x3f1ebc12,0xbf48d8b3,0x3f1b02c6,0xbf4bbbf8,0x3ef41f07,0xbf6106f2,0x3ef6e0cb,0xbf604621,0x3ea395c5,0xbf7294f8,
0x3eb263ef,0xbf6ff573,0x3e1c76de,0xbf7cfe73,0x3e541501,0xbf7a7302,0xbc96c9b6,0xbf7ff4e6,0x3d7b2b74,0xbf7f84ab,0xbe419b37,0xbf7b61fc,0xbdafb680,0xbf7f0e58,0xbeb554ec,0xbf6f6830,
0xbe6c9a7f,0xbf791298,0xbf0242b1,0xbf5c61c7,0xbebe1d4a,0xbf6db293,0xbf26050a,0xbf42de29,0xbf00e7e4,0xbf5d2d53,0xbf44e3f5,0xbf239da9,0xbf1ff6cb,0xbf47de65,0xbf5df6be,0xbeff17b2,
0x3f7f84ab,0xbd7b2b74,0x3f7f6ac7,0xbd8a200a,0x3f740bdd,0xbe9aa086,0x3f718f57,0xbea986c4,0x3f59f26a,0xbf064b82,0x3f5233c6,0xbf121eb0,0x3f32c8c9,0xbf373a23,0x3f239da9,0xbf44e3f5,
0x3f00e7e4,0xbf5d2d53,0x3ed25a09,0xbf696591,0x3e8e9a22,0xbf75dec6,0x3e1c76de,0xbf7cfe73,0x3d16c32c,0xbf7fd397,0xbdee3876,0xbf7e4323,0xbe541501,0xbf7a7302,0xbec1071e,0xbf6d1c1d,
0xbee0924f,0xbf660f88,0xbf1c420c,0xbf4ac77f,0xbf24d225,0xbf43e200,0xbf4cae79,0xbf19c200,0xbf4f7a1f,0xbf15f6d9,0xbf6e46be,0xbebb31a0,0xbf6db293,0xbebe1d4a,0xbf7e9bc9,0xbdd53db9,
0xbf7dabcc,0xbe09cf86,0xbf7c7eb0,0x3e28defc,0xbf7e70b0,0x3de1bc2e,0xbf6816a8,0x3ed8106b,0xbf6ff573,0x3eb263ef,0xbf42de29,0x3f26050a,0xbf531849,0x3f10d3cd,0xbf0f8784,0x3f53fac3,
0x3f7f0e58,0xbdafb680,0x3f7eea9d,0xbdbc3ac3,0x3f68bf3c,0xbed53641,0x3f655e0b,0xbee363fa,0x3f373a23,0xbf32c8c9,0x3f2d1469,0xbf3ca003,0x3ee0924f,0xbf660f88,0x3ebb31a0,0xbf6e46be,
0x3de1bc2e,0xbf7e70b0,0x3bc90f88,0xbf7ffec4,0xbe6c9a7f,0xbf791298,0xbeb554ec,0xbf6f6830,0xbf0b9a6b,0xbf5695e5,0xbf2ac082,0xbf3ebc1b,0xbf4bbbf8,0xbf1b02c6,0xbf63f473,0xbee900b7,
0xbf740bdd,0xbe9aa086,0xbf7e9bc9,0xbdd53db9,0xbf7fd397,0x3d16c32c,0xbf772417,0x3e8586ce,0xbf6db293,0x3ebe1d4a,0xbf4e8d90,0x3f173c07,0xbf3fc767,0x3f299415,0xbf0a48ad,0x3f577026,
0xbef6e0cb,0x3f604621,0xbe4dee60,0x3f7ac516,0xbe22abb6,0x3f7cbfc9,0x3e28defc,0x3f7c7eb0,0x3e3b6ecf,0x3f7baccd,0x3f0242b1,0x3f5c61c7,0x3f00e7e4,0x3f5d2d53,0x3f48d8b3,0x3f1ebc12,
0x3f800000,0x0,0x3f800000,0x0,0x3f7ffec4,0xbbc90f88,0x3f7fe129,0xbcfb49ba,0x3f7ffb11,0xbc490e90,0x3f7f84ab,0xbd7b2b74,0x3f7ff4e6,0xbc96c9b6,0x3f7eea9d,0xbdbc3ac3,
0x3f7fec43,0xbcc90ab0,0x3f7e1324,0xbdfab273,0x3f7fe129,0xbcfb49ba,0x3f7cfe73,0xbe1c76de,0x3f7fd397,0xbd16c32c,0x3f7baccd,0xbe3b6ecf,0x3f7fc38f,0xbd2fe007,0x3f7a1e84,0xbe5a3997,
0x3f7fb10f,0xbd48fb30,0x3f7853f8,0xbe78cfcc,0x3f7f9c18,0xbd621469,0x3f764d97,0xbe8b9507,0x3f7f84ab,0xbd7b2b74,0x3f740bdd,0xbe9aa086,0x3f7f6ac7,0xbd8a200a,0x3f718f57,0xbea986c4,
0x3f7f4e6d,0xbd96a905,0x3f6ed89e,0xbeb8442a,0x3f7f2f9d,0xbda3308c,0x3f6be858,0xbec6d529,0x3f7f0e58,0xbdafb680,0x3f68bf3c,0xbed53641,0x3f7eea9d,0xbdbc3ac3,0x3f655e0b,0xbee363fa,
0x3f800000,0x0,0x3f800000,0x0,0x3f7e9bc9,0xbdd53db9,0x3f7de0b1,0xbe039502,0x3f7a7302,0xbe541501,0x3f778bc5,0xbe827dc0,0x3f73913f,0xbe9d9e78,0x3f6d1c1d,0xbec1071e,
0x3f6a09a7,0xbecf7bca,0x3f5ebe05,0xbefc5d27,0x3f5df6be,0xbeff17b2,0x3f4cae79,0xbf19c200,0x3f4f7a1f,0xbf15f6d9,0x3f373a23,0xbf32c8c9,0x3f3ebc1b,0xbf2ac082,0x3f1ebc12,0xbf48d8b3,
0x3f2beb4a,0xbf3daef9,0x3f039c3d,0xbf5b941a,0x3f173c07,0xbf4e8d90,0x3ecc9b8b,0xbf6aab7b,0x3f00e7e4,0xbf5d2d53,0x3e8e9a22,0xbf75dec6,0x3ed25a09,0xbf696591,0x3e1c76de,0xbf7cfe73,
0x3ea09ae5,0xbf731447,0x3cc90ab0,0xbf7fec43,0x3e5a3997,0xbf7a1e84,0xbdd53db9,0xbf7e9bc9,0x3de1bc2e,0xbf7e70b0,0xbe6c9a7f,0xbf791298,0x3bc90f88,0xbf7ffec4,0xbeb554ec,0xbf6f6830,
0x3f800000,0x0,0x3f800000,0x0,0x3f7ac516,0xbe4dee60,0x3f796e4e,0xbe667c66,0x3f6b4b0c,0xbec9b953,0x3f660f88,0xbee0924f,0x3f5233c6,0xbf121eb0,0x3f46e22a,0xbf212ff9,
0x3f3085bb,0xbf396842,0x3f1d7fd1,0xbf49d112,0x3f07a136,0xbf591e6a,0x3ed8106b,0xbf6816a8,0x3eb263ef,0xbf6ff573,0x3e541501,0xbf7a7302,0x3e1c76de,0xbf7cfe73,0xbc96c9b6,0xbf7ff4e6,
0xbd48fb30,0xbf7fb10f,0xbe78cfcc,0xbf7853f8,0xbe7ee6e1,0xbf77f110,0xbee900b7,0xbf63f473,0xbee0924f,0xbf660f88,0xbf24d225,0xbf43e200,0xbf1c420c,0xbf4ac77f,0xbf4cae79,0xbf19c200,
0xbf41d870,0xbf273656,0xbf6a09a7,0xbecf7bca,0xbf5f8327,0xbef9a02d,0xbf7b61fc,0xbe419b37,0xbf740bdd,0xbe9aa086,0xbf7fd397,0x3d16c32c,0xbf7e9bc9,0xbdd53db9,0xbf772417,0x3e8586ce,
0x3f800000,0x0,0x3f800000,0x0,0x3f748422,0xbe97a117,0x3f7294f8,0xbea395c5,0x3f531849,0xbf10d3cd,0x3f4bbbf8,0xbf1b02c6,0x3f1ebc12,0xbf48d8b3,0x3f0f8784,0xbf53fac3,
0x3eb8442a,0xbf6ed89e,0x3e888e93,0xbf76ba07,0x3d8a200a,0xbf7f6ac7,0xbd621469,0xbf7f9c18,0xbe6c9a7f,0xbf791298,0xbebe1d4a,0xbf6db293,0xbf0242b1,0xbf5c61c7,0xbf26050a,0xbf42de29,
0xbf3daef9,0xbf2beb4a,0xbf5b941a,0xbf039c3d,0xbf6816a8,0xbed8106b,0xbf7a1e84,0xbe5a3997,0xbf7dabcc,0xbe09cf86,0xbf7e70b0,0x3de1bc2e,0xbf7c7eb0,0x3e28defc,0xbf6816a8,0x3ed8106b,
0xbf64aa59,0x3ee63375,0xbf396842,0x3f3085bb,0xbf385216,0x3f31a81d,0xbeee9479,0x3f628210,0xbef6e0cb,0x3f604621,0xbe22abb6,0x3f7cbfc9,0xbe4dee60,0x3f7ac516,0x3e28defc,0x3f7c7eb0,
0x3f800000,0x0,0x3f800000,0x0,0x3f7f9c18,0xbd621469,0x3f7f2f9d,0xbda3308c,0x3f7e70b0,0xbde1bc2e,0x3f7cbfc9,0xbe22abb6,0x3f7c7eb0,0xbe28defc,0x3f78b47b,0xbe72b651,
0x3f79c79d,0xbe605c13,0x3f731447,0xbea09ae5,0x3f764d97,0xbe8b9507,0x3f6be858,0xbec6d529,0x3f721352,0xbea68f12,0x3f633c5a,0xbeebcbbb,0x3f6d1c1d,0xbec1071e,0x3f591e6a,0xbf07a136,
0x3f676bd8,0xbedae880,0x3f4d9f02,0xbf187fc0,0x3f6106f2,0xbef41f07,0x3f40d0da,0xbf286605,0x3f59f26a,0xbf064b82,0x3f32c8c9,0xbf373a23,0x3f5233c6,0xbf121eb0,0x3f239da9,0xbf44e3f5,
0x3f49d112,0xbf1d7fd1,0x3f13682a,0xbf514d3d,0x3f40d0da,0xbf286605,0x3f0242b1,0xbf5c61c7,0x3f373a23,0xbf32c8c9,0x3ee0924f,0xbf660f88,0x3f2d1469,0xbf3ca003,0x3ebb31a0,0xbf6e46be,
0x3f800000,0x0,0x3f800000,0x0,0x3f7cfe73,0xbe1c76de,0x3f7bf531,0xbe354098,0x3f740bdd,0xbe9aa086,0x3f6ff573,0xbeb263ef,0x3f655e0b,0xbee363fa,0x3f5c61c7,0xbf0242b1,
0x3f514d3d,0xbf13682a,0x3f41d870,0xbf273656,0x3f385216,0xbf31a81d,0x3f212ff9,0xbf46e22a,0x3f1b02c6,0xbf4bbbf8,0x3ef6e0cb,0xbf604621,0x3ef41f07,0xbf6106f2,0x3ea395c5,0xbf7294f8,
0x3eac7cd4,0xbf710908,0x3e164083,0xbf7d3aac,0x3e419b37,0xbf7b61fc,0xbcfb49ba,0xbf7fe129,0x3d16c32c,0xbf7fd397,0xbe541501,0xbf7a7302,0xbdee3876,0xbf7e4323,0xbec1071e,0xbf6d1c1d,
0xbe888e93,0xbf76ba07,0xbf08f59b,0xbf584853,0xbed25a09,0xbf696591,0xbf2d1469,0xbf3ca003,0xbf0b9a6b,0xbf5695e5,0xbf4bbbf8,0xbf1b02c6,0xbf2ac082,0xbf3ebc1b,0xbf63f473,0xbee900b7,
0x3f800000,0x0,0x3f800000,0x0,0x3f77f110,0xbe7ee6e1,0x3f764d97,0xbe8b9507,0x3f604621,0xbef6e0cb,0x3f59f26a,0xbf064b82,0x3f3a7ca4,0xbf2f61a5,0x3f2d1469,0xbf3ca003,
0x3f08f59b,0xbf584853,0x3ee63375,0xbf64aa59,0x3e9d9e78,0xbf73913f,0x3e419b37,0xbf7b61fc,0x3d7b2b74,0xbf7f84ab,0xbdafb680,0xbf7f0e58,0xbe419b37,0xbf7b61fc,0xbeb554ec,0xbf6f6830,
0xbedae880,0xbf676bd8,0xbf187fc0,0xbf4d9f02,0xbf239da9,0xbf44e3f5,0xbf4ac77f,0xbf1c420c,0xbf4f7a1f,0xbf15f6d9,0xbf6db293,0xbebe1d4a,0xbf6e46be,0xbebb31a0,0xbf7e9bc9,0xbdd53db9,
0xbf7e1324,0xbdfab273,0xbf7c3b28,0x3e2f10a2,0xbf7de0b1,0x3e039502,0xbf66becc,0x3eddbe79,0xbf6db293,0x3ebe1d4a,0xbf3fc767,0x3f299415,0xbf4e8d90,0x3f173c07,0xbf0a48ad,0x3f577026,
0x3f800000,0x0,0x3f800000,0x0,0x3f708066,0xbeaf713a,0x3f6e46be,0xbebb31a0,0x3f43e200,0xbf24d225,0x3f3b8f3b,0xbf2e3bde,0x3eff17b2,0xbf5df6be,0x3eddbe79,0xbf66becc,
0x3e2f10a2,0xbf7c3b28,0x3d96a905,0xbf7f4e6d,0xbe354098,0xbf7bf531,0xbe97a117,0xbf748422,0xbf00e7e4,0xbf5d2d53,0xbf1ff6cb,0xbf47de65,0xbf44e3f5,0xbf239da9,0xbf5df6be,0xbeff17b2,
0xbf710908,0xbeac7cd4,0xbf7d3aac,0xbe164083,0xbf7ffec4,0x3bc90f88,0xbf796e4e,0x3e667c66,0xbf6ff573,0x3eb263ef,0xbf531849,0x3f10d3cd,0xbf42de29,0x3f26050a,0xbf0f8784,0x3f53fac3,
0xbefc5d27,0x3f5ebe05,0xbe605c13,0x3f79c79d,0xbe28defc,0x3f7c7eb0,0x3e1c76de,0x3f7cfe73,0x3e3b6ecf,0x3f7baccd,0x3f00e7e4,0x3f5d2d53,0x3f0242b1,0x3f5c61c7,0x3f48d8b3,0x3f1ebc12,
0x80002c00,0x20b22bea,0x24a52ba8,0x26d72b3b,0x80002c00,0x281d2add,0x2b0e278b,0x2bfe9a48,0x80002c00,0x2a6d28c4,0x2ba8a4a5,0x2564ab88,0x80002c00,0x2bc323c6,0x278bab0e,0xa9eda95f,
0x80002c00,0x2beaa0b2,0xa4a5aba8,0xab3b26d7,0x80002c00,0x2adda81d,0xab0ea78b,0x1a482bfe,0x80002c00,0x28c4aa6d,0xaba824a5,0x2b882564,0x80002c00,0x23c6abc3,0xa78b2b0e,0x295fa9ed,
0x80002c00,0xa0b2abea,0x24a52ba8,0xa6d7ab3b,0x80002c00,0xa81daadd,0x2b0e278b,0xabfe1a48,0x80002c00,0xaa6da8c4,0x2ba8a4a5,0xa5642b88,0x80002c00,0xabc3a3c6,0x278bab0e,0x29ed295f,
0x80002c00,0xabea20b2,0xa4a5aba8,0x2b3ba6d7,0x80002c00,0xaadd281d,0xab0ea78b,0x9a48abfe,0x80002c00,0xa8c42a6d,0xaba824a5,0xab88a564,0x80002c00,0xa3c62bc3,0xa78b2b0e,0xa95f29ed,
0x28722aa7,0x295f29ed,0x2a2f2913,0x2add281d,0x2aa7a872,0x26d7ab3b,0x9e46abf6,0xa8c4aa6d,0xa872aaa7,0xabfe9a48,0xa9132a2f,0x23c62bc3,0xaaa72872,0x25642b88,0x2bf69e46,0x20b2abea,
0x28722aa7,0x29eda95f,0xaa2fa913,0xa81d2add,0x2aa7a872,0xab3ba6d7,0x1e462bf6,0x2a6da8c4,0xa872aaa7,0x9a482bfe,0x2913aa2f,0xabc323c6,0xaaa72872,0x2b88a564,0xabf61e46,0x2bea20b2,
0x28722aa7,0xa95fa9ed,0x2a2f2913,0xaadda81d,0x2aa7a872,0xa6d72b3b,0x9e46abf6,0x28c42a6d,0xa872aaa7,0x2bfe1a48,0xa9132a2f,0xa3c6abc3,0xaaa72872,0xa564ab88,0x2bf69e46,0xa0b22bea,
0x28722aa7,0xa9ed295f,0xaa2fa913,0x281daadd,0x2aa7a872,0x2b3b26d7,0x1e462bf6,0xaa6d28c4,0xa872aaa7,0x1a48abfe,0x2913aa2f,0x2bc3a3c6,0xaaa72872,0xab882564,0xabf61e46,0xabeaa0b2,
0x2b64261f,0x2bc323c6,0x2bf61e46,0x2bfe9a48,0xab64a61f,0xabea20b2,0xaa2f2913,0xa5642b88,0x2b64261f,0x2adda81d,0x1e46abf6,0xa9eda95f,0xab64a61f,0xa8c42a6d,0x29132a2f,0x2b3ba6d7,
0x2b64261f,0x23c6abc3,0xabf69e46,0x1a482bfe,0xab64a61f,0x20b22bea,0x2a2fa913,0xab88a564,0x2b64261f,0xa81daadd,0x9e462bf6,0x295fa9ed,0xab64a61f,0x2a6d28c4,0xa913aa2f,0x26d72b3b,
0x2b64261f,0xabc3a3c6,0x2bf61e46,0xabfe1a48,0xab64a61f,0x2beaa0b2,0xaa2f2913,0x2564ab88,0x2b64261f,0xaadd281d,0x1e46abf6,0x29ed295f,0xab64a61f,0x28c4aa6d,0x29132a2f,0xab3b26d7,
0x2b64261f,0xa3c62bc3,0xabf69e46,0x9a48abfe,0xab64a61f,0xa0b2abea,0x2a2fa913,0x2b882564,0x2b64261f,0x281d2add,0x9e462bf6,0xa95f29ed,0xab64a61f,0xaa6da8c4,0xa913aa2f,0xa6d7ab3b,
0x2bd9a23e,0x2b88a564,0x2b0ea78b,0x2a6da8c4,0x223e2bd9,0x295f29ed,0x2ba824a5,0x2bc3a3c6,0xabd9223e,0xa6d72b3b,0x278b2b0e,0x2bea20b2,0xa23eabd9,0xabfe9a48,0xa4a52ba8,0x2add281d,
0x2bd9a23e,0xa564ab88,0xab0e278b,0x28c42a6d,0x223e2bd9,0x29eda95f,0xaba8a4a5,0x23c62bc3,0xabd9223e,0x2b3b26d7,0xa78bab0e,0xa0b22bea,0xa23eabd9,0x9a482bfe,0x24a5aba8,0xa81d2add,
0x2bd9a23e,0xab882564,0x2b0ea78b,0xaa6d28c4,0x223e2bd9,0xa95fa9ed,0x2ba824a5,0xabc323c6,0xabd9223e,0x26d7ab3b,0x278b2b0e,0xabeaa0b2,0xa23eabd9,0x2bfe1a48,0xa4a52ba8,0xaadda81d,
0x2bd9a23e,0x25642b88,0xab0e278b,0xa8c4aa6d,0x223e2bd9,0xa9ed295f,0xaba8a4a5,0xa3c6abc3,0xabd9223e,0xab3ba6d7,0xa78bab0e,0x20b2abea,0xa23eabd9,0x1a48abfe,0x24a5aba8,0x281daadd,
0x2c000000,0x2beaa0b2,0x2ba8a4a5,0x2b3ba6d7,0x2c000000,0x2adda81d,0x278bab0e,0x9a48abfe,0x2c000000,0x28c4aa6d,0xa4a5aba8,0xab88a564,0x2c000000,0x23c6abc3,0xab0ea78b,0xa95f29ed,
0x2c000000,0xa0b2abea,0xaba824a5,0x26d72b3b,0x2c000000,0xa81daadd,0xa78b2b0e,0x2bfe9a48,0x2c000000,0xaa6da8c4,0x24a52ba8,0x2564ab88,0x2c000000,0xabc3a3c6,0x2b0e278b,0xa9eda95f,
0x2c000000,0xabea20b2,0x2ba8a4a5,0xab3b26d7,0x2c000000,0xaadd281d,0x278bab0e,0x1a482bfe,0x2c000000,0xa8c42a6d,0xa4a5aba8,0x2b882564,0x2c000000,0xa3c62bc3,0xab0ea78b,0x295fa9ed,
0x2c000000,0x20b22bea,0xaba824a5,0xa6d7ab3b,0x2c000000,0x281d2add,0xa78b2b0e,0xabfe1a48,0x2c000000,0x2a6d28c4,0x24a52ba8,0xa5642b88,0x2c000000,0x2bc323c6,0x2b0e278b,0x29ed295f,
0x2aa7a872,0x29eda95f,0x2913aa2f,0x281daadd,0xa872aaa7,0xab3ba6d7,0xabf61e46,0xaa6d28c4,0xaaa72872,0x9a482bfe,0x2a2f2913,0x2bc3a3c6,0x28722aa7,0x2b88a564,0x9e46abf6,0xabeaa0b2,
0x2aa7a872,0xa95fa9ed,0xa9132a2f,0x2add281d,0xa872aaa7,0xa6d72b3b,0x2bf69e46,0xa8c4aa6d,0xaaa72872,0x2bfe1a48,0xaa2fa913,0x23c62bc3,0x28722aa7,0xa564ab88,0x1e462bf6,0x20b2abea,
0x2aa7a872,0xa9ed295f,0x2913aa2f,0xa81d2add,0xa872aaa7,0x2b3b26d7,0xabf61e46,0x2a6da8c4,0xaaa72872,0x1a48abfe,0x2a2f2913,0xabc323c6,0x28722aa7,0xab882564,0x9e46abf6,0x2bea20b2,
0x2aa7a872,0x295f29ed,0xa9132a2f,0xaadda81d,0xa872aaa7,0x26d7ab3b,0x2bf69e46,0x28c42a6d,0xaaa72872,0xabfe9a48,0xaa2fa913,0xa3c6abc3,0x28722aa7,0x25642b88,0x1e462bf6,0xa0b22bea,
0x261fab64,0x23c6abc3,0x1e46abf6,0x9a48abfe,0xa61f2b64,0x20b22bea,0x29132a2f,0x2b882564,0x261fab64,0xa81daadd,0xabf69e46,0xa95f29ed,0xa61f2b64,0x2a6d28c4,0x2a2fa913,0xa6d7ab3b,
0x261fab64,0xabc3a3c6,0x9e462bf6,0x2bfe9a48,0xa61f2b64,0x2beaa0b2,0xa913aa2f,0xa5642b88,0x261fab64,0xaadd281d,0x2bf61e46,0xa9eda95f,0xa61f2b64,0x28c4aa6d,0xaa2f2913,0x2b3ba6d7,
0x261fab64,0xa3c62bc3,0x1e46abf6,0x1a482bfe,0xa61f2b64,0xa0b2abea,0x29132a2f,0xab88a564,0x261fab64,0x281d2add,0xabf69e46,0x295fa9ed,0xa61f2b64,0xaa6da8c4,0x2a2fa913,0x26d72b3b,
0x261fab64,0x2bc323c6,0x9e462bf6,0xabfe1a48,0xa61f2b64,0xabea20b2,0xa913aa2f,0x2564ab88,0x261fab64,0x2adda81d,0x2bf61e46,0x29ed295f,0xa61f2b64,0xa8c42a6d,0xaa2f2913,0xab3b26d7,
0xa23eabd9,0xa564ab88,0xa78bab0e,0xa8c4aa6d,0x2bd9a23e,0x29eda95f,0x24a5aba8,0xa3c6abc3,0x223e2bd9,0x2b3b26d7,0x2b0ea78b,0x20b2abea,0xabd9223e,0x9a482bfe,0x2ba824a5,0x281daadd,
0xa23eabd9,0xab882564,0x278b2b0e,0x2a6da8c4,0x2bd9a23e,0xa95fa9ed,0xa4a52ba8,0x2bc3a3c6,0x223e2bd9,0x26d7ab3b,0xab0e278b,0x2bea20b2,0xabd9223e,0x2bfe1a48,0xaba8a4a5,0x2add281d,
0xa23eabd9,0x25642b88,0xa78bab0e,0x28c42a6d,0x2bd9a23e,0xa9ed295f,0x24a5aba8,0x23c62bc3,0x223e2bd9,0xab3ba6d7,0x2b0ea78b,0xa0b22bea,0xabd9223e,0x1a48abfe,0x2ba824a5,0xa81d2add,
0xa23eabd9,0x2b88a564,0x278b2b0e,0xaa6d28c4,0x2bd9a23e,0x295f29ed,0xa4a52ba8,0xabc323c6,0x223e2bd9,0xa6d72b3b,0xab0e278b,0xabeaa0b2,0xabd9223e,0xabfe9a48,0xaba8a4a5,0xaadda81d,
0x80002c00,0x80002c00,0x80002c00,0x80002c00,0x20b22bea,0x281d2add,0x2a6d28c4,0x2bc323c6,0x24a52ba8,0x2b0e278b,0x2ba8a4a5,0x278bab0e,0x26d72b3b,0x2bfe9a48,0x2564ab88,0xa9eda95f,
0x28722aa7,0x2aa7a872,0xa872aaa7,0xaaa72872,0x295f29ed,0x26d7ab3b,0xabfe9a48,0x25642b88,0x2a2f2913,0x9e46abf6,0xa9132a2f,0x2bf69e46,0x2add281d,0xa8c4aa6d,0x23c62bc3,0x20b2abea,
0x2b64261f,0xab64a61f,0x2b64261f,0xab64a61f,0x2bc323c6,0xabea20b2,0x2adda81d,0xa8c42a6d,0x2bf61e46,0xaa2f2913,0x1e46abf6,0x29132a2f,0x2bfe9a48,0xa5642b88,0xa9eda95f,0x2b3ba6d7,
0x2bd9a23e,0x223e2bd9,0xabd9223e,0xa23eabd9,0x2b88a564,0x295f29ed,0xa6d72b3b,0xabfe9a48,0x2b0ea78b,0x2ba824a5,0x278b2b0e,0xa4a52ba8,0x2a6da8c4,0x2bc3a3c6,0x2bea20b2,0x2add281d,
0x80002c00,0x80002c00,0x80002c00,0x80002c00,0x2beaa0b2,0x2adda81d,0x28c4aa6d,0x23c6abc3,0xa4a5aba8,0xab0ea78b,0xaba824a5,0xa78b2b0e,0xab3b26d7,0x1a482bfe,0x2b882564,0x295fa9ed,
0x28722aa7,0x2aa7a872,0xa872aaa7,0xaaa72872,0x29eda95f,0xab3ba6d7,0x9a482bfe,0x2b88a564,0xaa2fa913,0x1e462bf6,0x2913aa2f,0xabf61e46,0xa81d2add,0x2a6da8c4,0xabc323c6,0x2bea20b2,
0x2b64261f,0xab64a61f,0x2b64261f,0xab64a61f,0x23c6abc3,0x20b22bea,0xa81daadd,0x2a6d28c4,0xabf69e46,0x2a2fa913,0x9e462bf6,0xa913aa2f,0x1a482bfe,0xab88a564,0x295fa9ed,0x26d72b3b,
0x2bd9a23e,0x223e2bd9,0xabd9223e,0xa23eabd9,0xa564ab88,0x29eda95f,0x2b3b26d7,0x9a482bfe,0xab0e278b,0xaba8a4a5,0xa78bab0e,0x24a5aba8,0x28c42a6d,0x23c62bc3,0xa0b22bea,0xa81d2add,
0x80002c00,0x80002c00,0x80002c00,0x80002c00,0xa0b2abea,0xa81daadd,0xaa6da8c4,0xabc3a3c6,0x24a52ba8,0x2b0e278b,0x2ba8a4a5,0x278bab0e,0xa6d7ab3b,0xabfe1a48,0xa5642b88,0x29ed295f,
0x28722aa7,0x2aa7a872,0xa872aaa7,0xaaa72872,0xa95fa9ed,0xa6d72b3b,0x2bfe1a48,0xa564ab88,0x2a2f2913,0x9e46abf6,0xa9132a2f,0x2bf69e46,0xaadda81d,0x28c42a6d,0xa3c6abc3,0xa0b22bea,
0x2b64261f,0xab64a61f,0x2b64261f,0xab64a61f,0xabc3a3c6,0x2beaa0b2,0xaadd281d,0x28c4aa6d,0x2bf61e46,0xaa2f2913,0x1e46abf6,0x29132a2f,0xabfe1a48,0x2564ab88,0x29ed295f,0xab3b26d7,
0x2bd9a23e,0x223e2bd9,0xabd9223e,0xa23eabd9,0xab882564,0xa95fa9ed,0x26d7ab3b,0x2bfe1a48,0x2b0ea78b,0x2ba824a5,0x278b2b0e,0xa4a52ba8,0xaa6d28c4,0xabc323c6,0xabeaa0b2,0xaadda81d,
0x80002c00,0x80002c00,0x80002c00,0x80002c00,0xabea20b2,0xaadd281d,0xa8c42a6d,0xa3c62bc3,0xa4a5aba8,0xab0ea78b,0xaba824a5,0xa78b2b0e,0x2b3ba6d7,0x9a48abfe,0xab88a564,0xa95f29ed,
0x28722aa7,0x2aa7a872,0xa872aaa7,0xaaa72872,0xa9ed295f,0x2b3b26d7,0x1a48abfe,0xab882564,0xaa2fa913,0x1e462bf6,0x2913aa2f,0xabf61e46,0x281daadd,0xaa6d28c4,0x2bc3a3c6,0xabeaa0b2,
0x2b64261f,0xab64a61f,0x2b64261f,0xab64a61f,0xa3c62bc3,0xa0b2abea,0x281d2add,0xaa6da8c4,0xabf69e46,0x2a2fa913,0x9e462bf6,0xa913aa2f,0x9a48abfe,0x2b882564,0xa95f29ed,0xa6d7ab3b,
0x2bd9a23e,0x223e2bd9,0xabd9223e,0xa23eabd9,0x25642b88,0xa9ed295f,0xab3ba6d7,0x1a48abfe,0xab0e278b,0xaba8a4a5,0xa78bab0e,0x24a5aba8,0xa8c4aa6d,0xa3c6abc3,0x20b2abea,0x281daadd,
0x2c000000,0x2c000000,0x2c000000,0x2c000000,0x2beaa0b2,0x2adda81d,0x28c4aa6d,0x23c6abc3,0x2ba8a4a5,0x278bab0e,0xa4a5aba8,0xab0ea78b,0x2b3ba6d7,0x9a48abfe,0xab88a564,0xa95f29ed,
0x2aa7a872,0xa872aaa7,0xaaa72872,0x28722aa7,0x29eda95f,0xab3ba6d7,0x9a482bfe,0x2b88a564,0x2913aa2f,0xabf61e46,0x2a2f2913,0x9e46abf6,0x281daadd,0xaa6d28c4,0x2bc3a3c6,0xabeaa0b2,
0x261fab64,0xa61f2b64,0x261fab64,0xa61f2b64,0x23c6abc3,0x20b22bea,0xa81daadd,0x2a6d28c4,0x1e46abf6,0x29132a2f,0xabf69e46,0x2a2fa913,0x9a48abfe,0x2b882564,0xa95f29ed,0xa6d7ab3b,
0xa23eabd9,0x2bd9a23e,0x223e2bd9,0xabd9223e,0xa564ab88,0x29eda95f,0x2b3b26d7,0x9a482bfe,0xa78bab0e,0x24a5aba8,0x2b0ea78b,0x2ba824a5,0xa8c4aa6d,0xa3c6abc3,0x20b2abea,0x281daadd,
0x2c000000,0x2c000000,0x2c000000,0x2c000000,0xa0b2abea,0xa81daadd,0xaa6da8c4,0xabc3a3c6,0xaba824a5,0xa78b2b0e,0x24a52ba8,0x2b0e278b,0x26d72b3b,0x2bfe9a48,0x2564ab88,0xa9eda95f,
0x2aa7a872,0xa872aaa7,0xaaa72872,0x28722aa7,0xa95fa9ed,0xa6d72b3b,0x2bfe1a48,0xa564ab88,0xa9132a2f,0x2bf69e46,0xaa2fa913,0x1e462bf6,0x2add281d,0xa8c4aa6d,0x23c62bc3,0x20b2abea,
0x261fab64,0xa61f2b64,0x261fab64,0xa61f2b64,0xabc3a3c6,0x2beaa0b2,0xaadd281d,0x28c4aa6d,0x9e462bf6,0xa913aa2f,0x2bf61e46,0xaa2f2913,0x2bfe9a48,0xa5642b88,0xa9eda95f,0x2b3ba6d7,
0xa23eabd9,0x2bd9a23e,0x223e2bd9,0xabd9223e,0xab882564,0xa95fa9ed,0x26d7ab3b,0x2bfe1a48,0x278b2b0e,0xa4a52ba8,0xab0e278b,0xaba8a4a5,0x2a6da8c4,0x2bc3a3c6,0x2bea20b2,0x2add281d,
0x2c000000,0x2c000000,0x2c000000,0x2c000000,0xabea20b2,0xaadd281d,0xa8c42a6d,0xa3c62bc3,0x2ba8a4a5,0x278bab0e,0xa4a5aba8,0xab0ea78b,0xab3b26d7,0x1a482bfe,0x2b882564,0x295fa9ed,
0x2aa7a872,0xa872aaa7,0xaaa72872,0x28722aa7,0xa9ed295f,0x2b3b26d7,0x1a48abfe,0xab882564,0x2913aa2f,0xabf61e46,0x2a2f2913,0x9e46abf6,0xa81d2add,0x2a6da8c4,0xabc323c6,0x2bea20b2,
0x261fab64,0xa61f2b64,0x261fab64,0xa61f2b64,0xa3c62bc3,0xa0b2abea,0x281d2add,0xaa6da8c4,0x1e46abf6,0x29132a2f,0xabf69e46,0x2a2fa913,0x1a482bfe,0xab88a564,0x295fa9ed,0x26d72b3b,
0xa23eabd9,0x2bd9a23e,0x223e2bd9,0xabd9223e,0x25642b88,0xa9ed295f,0xab3ba6d7,0x1a48abfe,0xa78bab0e,0x24a5aba8,0x2b0ea78b,0x2ba824a5,0x28c42a6d,0x23c62bc3,0xa0b22bea,0xa81d2add,
0x2c000000,0x2c000000,0x2c000000,0x2c000000,0x20b22bea,0x281d2add,0x2a6d28c4,0x2bc323c6,0xaba824a5,0xa78b2b0e,0x24a52ba8,0x2b0e278b,0xa6d7ab3b,0xabfe1a48,0xa5642b88,0x29ed295f,
0x2aa7a872,0xa872aaa7,0xaaa72872,0x28722aa7,0x295f29ed,0x26d7ab3b,0xabfe9a48,0x25642b88,0xa9132a2f,0x2bf69e46,0xaa2fa913,0x1e462bf6,0xaadda81d,0x28c42a6d,0xa3c6abc3,0xa0b22bea,
0x261fab64,0xa61f2b64,0x261fab64,0xa61f2b64,0x2bc323c6,0xabea20b2,0x2adda81d,0xa8c42a6d,0x9e462bf6,0xa913aa2f,0x2bf61e46,0xaa2f2913,0xabfe1a48,0x2564ab88,0x29ed295f,0xab3b26d7,
0xa23eabd9,0x2bd9a23e,0x223e2bd9,0xabd9223e,0x2b88a564,0x295f29ed,0xa6d72b3b,0xabfe9a48,0x278b2b0e,0xa4a52ba8,0xab0e278b,0xaba8a4a5,0xaa6d28c4,0xabc323c6,0xabeaa0b2,0xaadda81d,
0x80002c00,0x80002c00,0x80002c00,0x80002c00,0x80002c00,0x261f2b64,0x29a829a8,0x2b64261f,0x80002c00,0x29a829a8,0x2c000000,0x29a8a9a8,0x80002c00,0x2b64261f,0x29a8a9a8,0xa61fab64,
0x80002c00,0x2c000000,0x8000ac00,0xac000000,0x80002c00,0x2b64a61f,0xa9a8a9a8,0xa61f2b64,0x80002c00,0x29a8a9a8,0xac000000,0x29a829a8,0x80002c00,0x261fab64,0xa9a829a8,0x2b64a61f,
0x80002c00,0x8000ac00,0x80002c00,0x8000ac00,0x80002c00,0xa61fab64,0x29a829a8,0xab64a61f,0x80002c00,0xa9a8a9a8,0x2c000000,0xa9a829a8,0x80002c00,0xab64a61f,0x29a8a9a8,0x261f2b64,
0x80002c00,0xac000000,0x8000ac00,0x2c000000,0x80002c00,0xab64261f,0xa9a8a9a8,0x261fab64,0x80002c00,0xa9a829a8,0xac000000,0xa9a8a9a8,0x80002c00,0xa61f2b64,0xa9a829a8,0xab64261f,
0x80002c00,0x80002c00,0x80002c00,0x80002c00,0x2c000000,0x2b64a61f,0x29a8a9a8,0x261fab64,0x8000ac00,0xa9a8a9a8,0xac000000,0xa9a829a8,0xac000000,0xa61f2b64,0x29a829a8,0x2b64a61f,
0x80002c00,0x2c000000,0x8000ac00,0xac000000,0x2c000000,0xa61fab64,0xa9a829a8,0x2b64261f,0x8000ac00,0xa9a829a8,0x2c000000,0xa9a8a9a8,0xac000000,0x2b64261f,0xa9a8a9a8,0x261f2b64,
0x80002c00,0x8000ac00,0x80002c00,0x8000ac00,0x2c000000,0xab64261f,0x29a8a9a8,0xa61f2b64,0x8000ac00,0x29a829a8,0xac000000,0x29a8a9a8,0xac000000,0x261fab64,0x29a829a8,0xab64261f,
0x80002c00,0xac000000,0x8000ac00,0x2c000000,0x2c000000,0x261f2b64,0xa9a829a8,0xab64a61f,0x8000ac00,0x29a8a9a8,0x2c000000,0x29a829a8,0xac000000,0xab64a61f,0xa9a8a9a8,0xa61fab64,
0x80002c00,0x80002c00,0x80002c00,0x80002c00,0x8000ac00,0xa61fab64,0xa9a8a9a8,0xab64a61f,0x80002c00,0x29a829a8,0x2c000000,0x29a8a9a8,0x8000ac00,0xab64a61f,0xa9a829a8,0x261f2b64,
0x80002c00,0x2c000000,0x8000ac00,0xac000000,0x8000ac00,0xab64261f,0x29a829a8,0x261fab64,0x80002c00,0x29a8a9a8,0xac000000,0x29a829a8,0x8000ac00,0xa61f2b64,0x29a8a9a8,0xab64261f,
0x80002c00,0x8000ac00,0x80002c00,0x8000ac00,0x8000ac00,0x261f2b64,0xa9a8a9a8,0x2b64261f,0x80002c00,0xa9a8a9a8,0x2c000000,0xa9a829a8,0x8000ac00,0x2b64261f,0xa9a829a8,0xa61fab64,
0x80002c00,0xac000000,0x8000ac00,0x2c000000,0x8000ac00,0x2b64a61f,0x29a829a8,0xa61f2b64,0x80002c00,0xa9a829a8,0xac000000,0xa9a8a9a8,0x8000ac00,0x261fab64,0x29a8a9a8,0x2b64a61f,
0x80002c00,0x80002c00,0x80002c00,0x80002c00,0xac000000,0xab64261f,0xa9a829a8,0xa61f2b64,0x8000ac00,0xa9a8a9a8,0xac000000,0xa9a829a8,0x2c000000,0x261fab64,0xa9a8a9a8,0xab64261f,
0x80002c00,0x2c000000,0x8000ac00,0xac000000,0xac000000,0x261f2b64,0x29a8a9a8,0xab64a61f,0x8000ac00,0xa9a829a8,0x2c000000,0xa9a8a9a8,0x2c000000,0xab64a61f,0x29a829a8,0xa61fab64,
0x80002c00,0x8000ac00,0x80002c00,0x8000ac00,0xac000000,0x2b64a61f,0xa9a829a8,0x261fab64,0x8000ac00,0x29a829a8,0xac000000,0x29a8a9a8,0x2c000000,0xa61f2b64,0xa9a8a9a8,0x2b64a61f,
0x80002c00,0xac000000,0x8000ac00,0x2c000000,0xac000000,0xa61fab64,0x29a8a9a8,0x2b64261f,0x8000ac00,0x29a8a9a8,0x2c000000,0x29a829a8,0x2c000000,0x2b64261f,0x29a829a8,0x261f2b64,
0x2c000000,0x2c000000,0x2c000000,0x2c000000,0x2c000000,0x2b64a61f,0x29a8a9a8,0x261fab64,0x2c000000,0x29a8a9a8,0xac00,0xa9a8a9a8,0x2c000000,0x261fab64,0xa9a8a9a8,0xab64261f,
0x2c000000,0xac00,0xac000000,0x2c00,0x2c000000,0xa61fab64,0xa9a829a8,0x2b64261f,0x2c000000,0xa9a8a9a8,0x2c00,0x29a8a9a8,0x2c000000,0xab64a61f,0x29a829a8,0xa61fab64,
0x2c000000,0xac000000,0x2c000000,0xac000000,0x2c000000,0xab64261f,0x29a8a9a8,0xa61f2b64,0x2c000000,0xa9a829a8,0xac00,0x29a829a8,0x2c000000,0xa61f2b64,0xa9a8a9a8,0x2b64a61f,
0x2c000000,0x2c00,0xac000000,0xac00,0x2c000000,0x261f2b64,0xa9a829a8,0xab64a61f,0x2c000000,0x29a829a8,0x2c00,0xa9a829a8,0x2c000000,0x2b64261f,0x29a829a8,0x261f2b64,
0x2c000000,0x2c000000,0x2c000000,0x2c000000,0xac00,0xa61fab64,0xa9a8a9a8,0xab64a61f,0xac000000,0xa9a829a8,0x2c00,0x29a829a8,0x2c00,0x2b64261f,0x29a8a9a8,0xa61fab64,
0x2c000000,0xac00,0xac000000,0x2c00,0xac00,0xab64261f,0x29a829a8,0x261fab64,0xac000000,0x29a829a8,0xac00,0xa9a829a8,0x2c00,0x261fab64,0xa9a829a8,0x2b64a61f,
0x2c000000,0xac000000,0x2c000000,0xac000000,0xac00,0x261f2b64,0xa9a8a9a8,0x2b64261f,0xac000000,0x29a8a9a8,0x2c00,0xa9a8a9a8,0x2c00,0xab64a61f,0x29a8a9a8,0x261f2b64,
0x2c000000,0x2c00,0xac000000,0xac00,0xac00,0x2b64a61f,0x29a829a8,0xa61f2b64,0xac000000,0xa9a8a9a8,0xac00,0x29a8a9a8,0x2c00,0xa61f2b64,0xa9a829a8,0xab64261f,
0x2c000000,0x2c000000,0x2c000000,0x2c000000,0xac000000,0xab64261f,0xa9a829a8,0xa61f2b64,0x2c000000,0x29a8a9a8,0xac00,0xa9a8a9a8,0xac000000,0xa61f2b64,0x29a829a8,0x2b64a61f,
0x2c000000,0xac00,0xac000000,0x2c00,0xac000000,0x261f2b64,0x29a8a9a8,0xab64a61f,0x2c000000,0xa9a8a9a8,0x2c00,0x29a8a9a8,0xac000000,0x2b64261f,0xa9a8a9a8,0x261f2b64,
0x2c000000,0xac000000,0x2c000000,0xac000000,0xac000000,0x2b64a61f,0xa9a829a8,0x261fab64,0x2c000000,0xa9a829a8,0xac00,0x29a829a8,0xac000000,0x261fab64,0x29a829a8,0xab64261f,
0x2c000000,0x2c00,0xac000000,0xac00,0xac000000,0xa61fab64,0x29a8a9a8,0x2b64261f,0x2c000000,0x29a829a8,0x2c00,0xa9a829a8,0xac000000,0xab64a61f,0xa9a8a9a8,0xa61fab64,
0x2c000000,0x2c000000,0x2c000000,0x2c000000,0x2c00,0x261f2b64,0x29a829a8,0x2b64261f,0xac000000,0xa9a829a8,0x2c00,0x29a829a8,0xac00,0xab64a61f,0xa9a829a8,0x261f2b64,
0x2c000000,0xac00,0xac000000,0x2c00,0x2c00,0x2b64a61f,0xa9a8a9a8,0xa61f2b64,0xac000000,0x29a829a8,0xac00,0xa9a829a8,0xac00,0xa61f2b64,0x29a8a9a8,0xab64261f,
0x2c000000,0xac000000,0x2c000000,0xac000000,0x2c00,0xa61fab64,0x29a829a8,0xab64a61f,0xac000000,0x29a8a9a8,0x2c00,0xa9a8a9a8,0xac00,0x2b64261f,0xa9a829a8,0xa61fab64,
0x2c000000,0x2c00,0xac000000,0xac00,0x2c00,0xab64261f,0xa9a8a9a8,0x261fab64,0xac000000,0xa9a8a9a8,0xac00,0x29a8a9a8,0xac00,0x261fab64,0x29a8a9a8,0x2b64a61f,
0x3f800000,0x0,0x3f7ffd39,0xbc16cb58,0x3f800000,0x0,0x3f7fdaaf,0xbd0a342f,0x3f800000,0x0,0x3f7f90b1,0xbd6ea038,0x3f800000,0x0,0x3f7f1f49,0xbda973ba,
0x3f800000,0x0,0x3f7e868b,0xbddb7d37,0x3f800000,0x0,0x3f7dc68c,0xbe06b26e,0x3f800000,0x0,0x3f7cdf6c,0xbe1f917b,0x3f800000,0x0,0x3f7bd14d,0xbe3857ec,
0x3f800000,0x0,0x3f7a9c59,0xbe5101f1,0x3f800000,0x0,0x3f7940c0,0xbe698bba,0x3f800000,0x0,0x3f77beb7,0xbe80f8c0,0x3f800000,0x0,0x3f76167a,0xbe8d17c0,
0x3f800000,0x0,0x3f74484b,0xbe9920fe,0x3f800000,0x0,0x3f725470,0xbea5129f,0x3f800000,0x0,0x3f703b37,0xbeb0eacb,0x3f800000,0x0,0x3f6dfcf2,0xbebca7af,
0x3f7fd397,0xbd16c32c,0x3f7fba9e,0xbd3c6dd5,0x3f7dabcc,0xbe09cf86,0x3f7c5d3a,0xbe2bf804,0x3f791298,0xbe6c9a7f,0x3f75341d,0xbe931f35,0x3f721352,0xbea68f12,0x3f6a5ad9,0xbece0bea,
0x3f68bf3c,0xbed53641,0x3f5bfb34,0xbf02ef9f,0x3f5d2d53,0xbf00e7e4,0x3f4a4c87,0xbf1ce11f,0x3f4f7a1f,0xbf15f6d9,0x3f3592e7,0xbf34768f,0x3f3fc767,0xbf299415,0x3f1e1e22,0xbf495521,
0x3f2e3bde,0xbf3b8f3b,0x3f044889,0xbf5b2c79,0x3f1b02c6,0xbf4bbbf8,0x3ed0eb2a,0xbf69b7e4,0x3f064b82,0xbf59f26a,0x3e9620d2,0xbf74bf62,0x3ee0924f,0xbf660f88,0x3e3228d4,0xbf7c187a,
0x3eb263ef,0xbf6ff573,0x3d55880e,0xbf7fa6e3,0x3e827dc0,0xbf778bc5,0xbd9064b4,0xbf7f5ce9,0x3e22abb6,0xbf7cbfc9,0xbe44b0b9,0xbf7b3bab,0x3d7b2b74,0xbf7f84ab,0xbe9f1cdf,0xbf73530e,
0x3f7f4e6d,0xbd96a905,0x3f7f1f49,0xbda973ba,0x3f76ba07,0xbe888e93,0x3f74484b,0xbe9920fe,0x3f64aa59,0xbee63375,0x3f5d924d,0xbf003a06,0x3f49d112,0xbf1d7fd1,0x3f3c17d9,0xbf2da859,
0x3f273656,0xbf41d870,0x3f11796b,0xbf52a649,0x3efc5d27,0xbf5ebe05,0x3ebf926f,0xbf6d67a1,0x3ea09ae5,0xbf731447,0x3e25c58c,0xbf7c9f8a,0x3dfab273,0xbf7e1324,0xbd6ea038,0xbf7f90b1,
0xbd96a905,0xbf7f4e6d,0xbe8d17c0,0xbf76167a,0xbe888e93,0xbf76ba07,0xbef58035,0xbf60a6cf,0xbee63375,0xbf64aa59,0xbf28fd41,0xbf404c5c,0xbf1d7fd1,0xbf49d112,0xbf4f0417,0xbf16999f,
0xbf41d870,0xbf273656,0xbf6afb8c,0xbecb2aae,0xbf5ebe05,0xbefc5d27,0xbf7b87b2,0xbe3e853e,0xbf731447,0xbea09ae5,0xbf7fdaaf,0x3d0a342f,0xbf7e1324,0xbdfab273,0xbf77beb7,0x3e80f8c0,
0x3f7e70b0,0xbde1bc2e,0x3f7e2b72,0xbdf475c0,0x3f6b4b0c,0xbec9b953,0x3f67c187,0xbed97cb9,0x3f43e200,0xbf24d225,0x3f39f2ac,0xbf2ff3e6,0x3f0b9a6b,0xbf5695e5,0x3ef2bd43,0xbf61668a,
0x3e8e9a22,0xbf75dec6,0x3e3228d4,0xbf7c187a,0xbc490e90,0xbf7ffb11,0xbe1324ca,0xbf7d57de,0xbe9aa086,0xbf740bdd,0xbee4cbfe,0xbf650479,0xbf10d3cd,0xbf531849,0xbf34768f,0xbf3592e7,
0xbf47de65,0xbf1ff6cb,0xbf644fac,0xbee79a5d,0xbf6db293,0xbebe1d4a,0xbf7d1cdd,0xbe195be0,0xbf7f0e58,0xbdafb680,0xbf7c5d3a,0x3e2bf804,0xbf7a7302,0x3e541501,0xbf62241a,0x3eeff7fb,
0xbf604621,0x3ef6e0cb,0xbf311722,0x3f38dd65,0xbf32c8c9,0x3f373a23,0xbedc53c1,0x3f671599,0xbeebcbbb,0x3f633c5a,0xbe007745,0x3f7dfa38,0xbe3b6ecf,0x3f7baccd,0x3e44b0b9,0x3f7b3bab,
0x3f7ff4e6,0xbc96c9b6,0x3f7fe705,0xbce22a7a,0x3f7f6ac7,0xbd8a200a,0x3f7eb069,0xbdcefdb7,0x3f7e4323,0xbdee3876,0x3f7c187a,0xbe3228d4,0x3f7c7eb0,0xbe28defc,0x3f7822d1,0xbe7bdba4,
0x3f7a1e84,0xbe5a3997,0x3f72d4eb,0xbea21887,0x3f772417,0xbe8586ce,0x3f6c3624,0xbec5625c,0x3f73913f,0xbe9d9e78,0x3f644fac,0xbee79a5d,0x3f6f6830,0xbeb554ec,0x3f5b2c79,0xbf044889,
0x3f6aab7b,0xbecc9b8b,0x3f50d937,0xbf140c5f,0x3f655e0b,0xbee363fa,0x3f456439,0xbf2302d3,0x3f5f8327,0xbef9a02d,0x3f38dd65,0xbf311722,0x3f591e6a,0xbf07a136,0x3f2b561b,0xbf3e35c5,
0x3f5233c6,0xbf121eb0,0x3f1ce11f,0xbf4a4c87,0x3f4ac77f,0xbf1c420c,0x3f0d9281,0xbf554aa4,0x3f42de29,0xbf26050a,0x3efafef7,0xbf5f20db,0x3f3a7ca4,0xbf2f61a5,0x3ed97cb9,0xbf67c187,
0x3f7f9c18,0xbd621469,0x3f7f7808,0xbd83db0a,0x3f7ac516,0xbe4dee60,0x3f78e3d6,0xbe6fa8b2,0x3f708066,0xbeaf713a,0x3f6afb8c,0xbecb2aae,0x3f6106f2,0xbef41f07,0x3f5627fe,0xbf0c42c9,
0x3f4cae79,0xbf19c200,0x3f3b0629,0xbf2ecef7,0x3f33e7bc,0xbf36206c,0x3f1a6293,0xbf4c3578,0x3f173c07,0xbf4e8d90,0x3eea6681,0xbf6398ac,0x3eee9479,0xbf628210,0x3e9920fe,0xbf74484b,
0x3ea986c4,0xbf718f57,0x3e06b26e,0xbf7dc68c,0x3e419b37,0xbf7b61fc,0xbd2351cb,0xbf7fcbe2,0x3d2fe007,0xbf7fc38f,0xbe57278f,0xbf7a4910,0xbdd53db9,0xbf7e9bc9,0xbebf926f,0xbf6d67a1,
0xbe7ee6e1,0xbf77f110,0xbf06f686,0xbf5988ad,0xbec6d529,0xbf6be858,0xbf2a2a80,0xbf3f41fc,0xbf04f484,0xbf5ac450,0xbf485bca,0xbf1f599f,0xbf239da9,0xbf44e3f5,0xbf60a6cf,0xbef58035,
0x3f7eea9d,0xbdbc3ac3,0x3f7eb069,0xbdcefdb7,0x3f718f57,0xbea986c4,0x3f6e8ff8,0xbeb9bb1e,0x3f55b993,0xbf0cead0,0x3f4d26fd,0xbf19210f,0x3f2d1469,0xbf3ca003,0x3f1ce11f,0xbf4a4c87,
0x3ef41f07,0xbf6106f2,0x3ec27b55,0xbf6cd007,0x3e7ee6e1,0xbf77f110,0x3df475c0,0xbf7e2b72,0x3bc90f88,0xbf7ffec4,0xbe195be0,0xbf7d1cdd,0xbe72b651,0xbf78b47b,0xbed0eb2a,0xbf69b7e4,
0xbeee9479,0xbf628210,0xbf2302d3,0xbf456439,0xbf2ac082,0xbf3ebc1b,0xbf51c0c2,0xbf12c39a,0xbf53fac3,0xbf0f8784,0xbf714c7a,0xbeab0201,0xbf708066,0xbeaf713a,0xbf7f5ce9,0xbd9064b4,
0xbf7e9bc9,0xbdd53db9,0xbf7aed37,0x3e4ada4f,0xbf7d7474,0x3e1008b7,0xbf644fac,0x3ee79a5d,0xbf6d1c1d,0x3ec1071e,0xbf3d27b8,0x3f2c800f,0xbf4e8d90,0x3f173c07,0xbf084b92,0x3f58b3a1,
0x3f7de0b1,0xbe039502,0x3f7d906e,0xbe0cec4a,0x3f63f473,0xbee900b7,0x3f5fe4e9,0xbef840c8,0x3f2f61a5,0xbf3a7ca4,0x3f24381a,0xbf446337,0x3ecc9b8b,0xbf6aab7b,0x3ea5129f,0xbf725470,
0x3d8a200a,0xbf7f6ac7,0xbd2351cb,0xbf7fcbe2,0xbe8b9507,0xbf764d97,0xbecb2aae,0xbf6afb8c,0xbf14b039,0xbf5064af,0xbf33587a,0xbf36ad7f,0xbf5233c6,0xbf121eb0,0xbf6912ae,0xbed3c867,
0xbf772417,0xbe8586ce,0xbf7f90b1,0xbd6ea038,0xbf7f2f9d,0x3da3308c,0xbf73ced9,0x3e9c1faf,0xbf696591,0x3ed25a09,0xbf476085,0x3f209393,0xbf385216,0x3f31a81d,0xbf003a06,0x3f5d924d,
0xbee363fa,0x3f655e0b,0xbe1f917b,0x3f7cdf6c,0xbdee3876,0x3f7e4323,0x3e57278f,0x3f7a4910,0x3e667c66,0x3f796e4e,0x3f0c42c9,0x3f5627fe,0x3f0a48ad,0x3f577026,0x3f4fefa8,0x3f1553b7,
0x3f800000,0x0,0x3f800000,0x0,0x3f7ffd39,0xbc16cb58,0x3f7fdaaf,0xbd0a342f,0x3f7ff4e6,0xbc96c9b6,0x3f7f6ac7,0xbd8a200a,0x3f7fe705,0xbce22a7a,0x3f7eb069,0xbdcefdb7,
0x3f7fd397,0xbd16c32c,0x3f7dabcc,0xbe09cf86,0x3f7fba9e,0xbd3c6dd5,0x3f7c5d3a,0xbe2bf804,0x3f7f9c18,0xbd621469,0x3f7ac516,0xbe4dee60,0x3f7f7808,0xbd83db0a,0x3f78e3d6,0xbe6fa8b2,
0x3f7f4e6d,0xbd96a905,0x3f76ba07,0xbe888e93,0x3f7f1f49,0xbda973ba,0x3f74484b,0xbe9920fe,0x3f7eea9d,0xbdbc3ac3,0x3f718f57,0xbea986c4,0x3f7eb069,0xbdcefdb7,0x3f6e8ff8,0xbeb9bb1e,
0x3f7e70b0,0xbde1bc2e,0x3f6b4b0c,0xbec9b953,0x3f7e2b72,0xbdf475c0,0x3f67c187,0xbed97cb9,0x3f7de0b1,0xbe039502,0x3f63f473,0xbee900b7,0x3f7d906e,0xbe0cec4a,0x3f5fe4e9,0xbef840c8,
0x3f800000,0x0,0x3f800000,0x0,0x3f7e868b,0xbddb7d37,0x3f7dc68c,0xbe06b26e,0x3f7a1e84,0xbe5a3997,0x3f772417,0xbe8586ce,0x3f72d4eb,0xbea21887,0x3f6c3624,0xbec5625c,
0x3f68bf3c,0xbed53641,0x3f5d2d53,0xbf00e7e4,0x3f5bfb34,0xbf02ef9f,0x3f4a4c87,0xbf1ce11f,0x3f4cae79,0xbf19c200,0x3f33e7bc,0xbf36206c,0x3f3b0629,0xbf2ecef7,0x3f1a6293,0xbf4c3578,
0x3f273656,0xbf41d870,0x3efc5d27,0xbf5ebe05,0x3f11796b,0xbf52a649,0x3ebf926f,0xbf6d67a1,0x3ef41f07,0xbf6106f2,0x3e7ee6e1,0xbf77f110,0x3ec27b55,0xbf6cd007,0x3df475c0,0xbf7e2b72,
0x3e8e9a22,0xbf75dec6,0xbc490e90,0xbf7ffb11,0x3e3228d4,0xbf7c187a,0xbe1324ca,0xbf7d57de,0x3d8a200a,0xbf7f6ac7,0xbe8b9507,0xbf764d97,0xbd2351cb,0xbf7fcbe2,0xbecb2aae,0xbf6afb8c,
0x3f800000,0x0,0x3f800000,0x0,0x3f7a9c59,0xbe5101f1,0x3f7940c0,0xbe698bba,0x3f6aab7b,0xbecc9b8b,0x3f655e0b,0xbee363fa,0x3f50d937,0xbf140c5f,0x3f456439,0xbf2302d3,
0x3f2e3bde,0xbf3b8f3b,0x3f1b02c6,0xbf4bbbf8,0x3f044889,0xbf5b2c79,0x3ed0eb2a,0xbf69b7e4,0x3ea986c4,0xbf718f57,0x3e419b37,0xbf7b61fc,0x3e06b26e,0xbf7dc68c,0xbd2351cb,0xbf7fcbe2,
0xbd96a905,0xbf7f4e6d,0xbe888e93,0xbf76ba07,0xbe8d17c0,0xbf76167a,0xbef58035,0xbf60a6cf,0xbeee9479,0xbf628210,0xbf2ac082,0xbf3ebc1b,0xbf2302d3,0xbf456439,0xbf51c0c2,0xbf12c39a,
0xbf47de65,0xbf1ff6cb,0xbf6db293,0xbebe1d4a,0xbf644fac,0xbee79a5d,0xbf7d1cdd,0xbe195be0,0xbf772417,0xbe8586ce,0xbf7f2f9d,0x3da3308c,0xbf7f90b1,0xbd6ea038,0xbf73ced9,0x3e9c1faf,
0x3f800000,0x0,0x3f800000,0x0,0x3f74484b,0xbe9920fe,0x3f725470,0xbea5129f,0x3f5233c6,0xbf121eb0,0x3f4ac77f,0xbf1c420c,0x3f1ce11f,0xbf4a4c87,0x3f0d9281,0xbf554aa4,
0x3eb263ef,0xbf6ff573,0x3e827dc0,0xbf778bc5,0x3d55880e,0xbf7fa6e3,0xbd9064b4,0xbf7f5ce9,0xbe7ee6e1,0xbf77f110,0xbec6d529,0xbf6be858,0xbf06f686,0xbf5988ad,0xbf2a2a80,0xbf3f41fc,
0xbf41d870,0xbf273656,0xbf5ebe05,0xbefc5d27,0xbf6afb8c,0xbecb2aae,0xbf7b87b2,0xbe3e853e,0xbf7e9bc9,0xbdd53db9,0xbf7d7474,0x3e1008b7,0xbf7aed37,0x3e4ada4f,0xbf644fac,0x3ee79a5d,
0xbf604621,0x3ef6e0cb,0xbf32c8c9,0x3f373a23,0xbf311722,0x3f38dd65,0xbedc53c1,0x3f671599,0xbee363fa,0x3f655e0b,0xbdee3876,0x3f7e4323,0xbe1f917b,0x3f7cdf6c,0x3e57278f,0x3f7a4910,
0x3f800000,0x0,0x3f800000,0x0,0x3f7f90b1,0xbd6ea038,0x3f7f1f49,0xbda973ba,0x3f7e4323,0xbdee3876,0x3f7c7eb0,0xbe28defc,0x3f7c187a,0xbe3228d4,0x3f7822d1,0xbe7bdba4,
0x3f791298,0xbe6c9a7f,0x3f721352,0xbea68f12,0x3f75341d,0xbe931f35,0x3f6a5ad9,0xbece0bea,0x3f708066,0xbeaf713a,0x3f6106f2,0xbef41f07,0x3f6afb8c,0xbecb2aae,0x3f5627fe,0xbf0c42c9,
0x3f64aa59,0xbee63375,0x3f49d112,0xbf1d7fd1,0x3f5d924d,0xbf003a06,0x3f3c17d9,0xbf2da859,0x3f55b993,0xbf0cead0,0x3f2d1469,0xbf3ca003,0x3f4d26fd,0xbf19210f,0x3f1ce11f,0xbf4a4c87,
0x3f43e200,0xbf24d225,0x3f0b9a6b,0xbf5695e5,0x3f39f2ac,0xbf2ff3e6,0x3ef2bd43,0xbf61668a,0x3f2f61a5,0xbf3a7ca4,0x3ecc9b8b,0xbf6aab7b,0x3f24381a,0xbf446337,0x3ea5129f,0xbf725470,
0x3f800000,0x0,0x3f800000,0x0,0x3f7cdf6c,0xbe1f917b,0x3f7bd14d,0xbe3857ec,0x3f73913f,0xbe9d9e78,0x3f6f6830,0xbeb554ec,0x3f644fac,0xbee79a5d,0x3f5b2c79,0xbf044889,
0x3f4f7a1f,0xbf15f6d9,0x3f3fc767,0xbf299415,0x3f3592e7,0xbf34768f,0x3f1e1e22,0xbf495521,0x3f173c07,0xbf4e8d90,0x3eee9479,0xbf628210,0x3eea6681,0xbf6398ac,0x3e9920fe,0xbf74484b,
0x3ea09ae5,0xbf731447,0x3dfab273,0xbf7e1324,0x3e25c58c,0xbf7c9f8a,0xbd6ea038,0xbf7f90b1,0x3bc90f88,0xbf7ffec4,0xbe72b651,0xbf78b47b,0xbe195be0,0xbf7d1cdd,0xbed0eb2a,0xbf69b7e4,
0xbe9aa086,0xbf740bdd,0xbf10d3cd,0xbf531849,0xbee4cbfe,0xbf650479,0xbf34768f,0xbf3592e7,0xbf14b039,0xbf5064af,0xbf5233c6,0xbf121eb0,0xbf33587a,0xbf36ad7f,0xbf6912ae,0xbed3c867,
0x3f800000,0x0,0x3f800000,0x0,0x3f77beb7,0xbe80f8c0,0x3f76167a,0xbe8d17c0,0x3f5f8327,0xbef9a02d,0x3f591e6a,0xbf07a136,0x3f38dd65,0xbf311722,0x3f2b561b,0xbf3e35c5,
0x3f064b82,0xbf59f26a,0x3ee0924f,0xbf660f88,0x3e9620d2,0xbf74bf62,0x3e3228d4,0xbf7c187a,0x3d2fe007,0xbf7fc38f,0xbdd53db9,0xbf7e9bc9,0xbe57278f,0xbf7a4910,0xbebf926f,0xbf6d67a1,
0xbee63375,0xbf64aa59,0xbf1d7fd1,0xbf49d112,0xbf28fd41,0xbf404c5c,0xbf4f0417,0xbf16999f,0xbf53fac3,0xbf0f8784,0xbf708066,0xbeaf713a,0xbf714c7a,0xbeab0201,0xbf7f5ce9,0xbd9064b4,
0xbf7f0e58,0xbdafb680,0xbf7a7302,0x3e541501,0xbf7c5d3a,0x3e2bf804,0xbf62241a,0x3eeff7fb,0xbf696591,0x3ed25a09,0xbf385216,0x3f31a81d,0xbf476085,0x3f209393,0xbf003a06,0x3f5d924d,
0x3f800000,0x0,0x3f800000,0x0,0x3f703b37,0xbeb0eacb,0x3f6dfcf2,0xbebca7af,0x3f42de29,0xbf26050a,0x3f3a7ca4,0xbf2f61a5,0x3efafef7,0xbf5f20db,0x3ed97cb9,0xbf67c187,
0x3e22abb6,0xbf7cbfc9,0x3d7b2b74,0xbf7f84ab,0xbe44b0b9,0xbf7b3bab,0xbe9f1cdf,0xbf73530e,0xbf04f484,0xbf5ac450,0xbf239da9,0xbf44e3f5,0xbf485bca,0xbf1f599f,0xbf60a6cf,0xbef58035,
0xbf731447,0xbea09ae5,0xbf7e1324,0xbdfab273,0xbf7fdaaf,0x3d0a342f,0xbf77beb7,0x3e80f8c0,0xbf6d1c1d,0x3ec1071e,0xbf4e8d90,0x3f173c07,0xbf3d27b8,0x3f2c800f,0xbf084b92,0x3f58b3a1,
0xbeebcbbb,0x3f633c5a,0xbe3b6ecf,0x3f7baccd,0xbe007745,0x3f7dfa38,0x3e44b0b9,0x3f7b3bab,0x3e667c66,0x3f796e4e,0x3f0a48ad,0x3f577026,0x3f0c42c9,0x3f5627fe,0x3f4fefa8,0x3f1553b7,
0x80002c00,0x223e2bd9,0x261f2b64,0x28722aa7,0x80002c00,0x28722aa7,0x2b64261f,0x2bd9a23e,0x80002c00,0x2aa72872,0x2b64a61f,0x223eabd9,0x80002c00,0x2bd9223e,0x261fab64,0xaaa7a872,
0x80002c00,0x2bd9a23e,0xa61fab64,0xaaa72872,0x80002c00,0x2aa7a872,0xab64a61f,0x223e2bd9,0x80002c00,0x2872aaa7,0xab64261f,0x2bd9223e,0x80002c00,0x223eabd9,0xa61f2b64,0x2872aaa7,
0x80002c00,0xa23eabd9,0x261f2b64,0xa872aaa7,0x80002c00,0xa872aaa7,0x2b64261f,0xabd9223e,0x80002c00,0xaaa7a872,0x2b64a61f,0xa23e2bd9,0x80002c00,0xabd9a23e,0x261fab64,0x2aa72872,
0x80002c00,0xabd9223e,0xa61fab64,0x2aa7a872,0x80002c00,0xaaa72872,0xab64a61f,0xa23eabd9,0x80002c00,0xa8722aa7,0xab64261f,0xabd9a23e,0x80002c00,0xa23e2bd9,0xa61f2b64,0xa8722aa7,
0x29a829a8,0x2aa72872,0x2b64261f,0x2bd9223e,0x29a8a9a8,0x223eabd9,0xa61fab64,0xaaa7a872,0xa9a8a9a8,0xabd9223e,0xa61f2b64,0x28722aa7,0xa9a829a8,0x28722aa7,0x2b64a61f,0xa23eabd9,
0x29a829a8,0x2872aaa7,0xab64a61f,0xa23e2bd9,0x29a8a9a8,0xabd9a23e,0x261f2b64,0x2872aaa7,0xa9a8a9a8,0x223e2bd9,0x261fab64,0xaaa72872,0xa9a829a8,0x2aa7a872,0xab64261f,0x2bd9a23e,
0x29a829a8,0xaaa7a872,0x2b64261f,0xabd9a23e,0x29a8a9a8,0xa23e2bd9,0xa61fab64,0x2aa72872,0xa9a8a9a8,0x2bd9a23e,0xa61f2b64,0xa872aaa7,0xa9a829a8,0xa872aaa7,0x2b64a61f,0x223e2bd9,
0x29a829a8,0xa8722aa7,0xab64a61f,0x223eabd9,0x29a8a9a8,0x2bd9223e,0x261f2b64,0xa8722aa7,0xa9a8a9a8,0xa23eabd9,0x261fab64,0x2aa7a872,0xa9a829a8,0xaaa72872,0xab64261f,0xabd9223e,
0x2c000000,0x2bd9a23e,0x2b64a61f,0x2aa7a872,0xac000000,0xaaa72872,0xa61f2b64,0x223e2bd9,0x2c000000,0x2872aaa7,0xa61fab64,0xabd9a23e,0xac000000,0xa23e2bd9,0x2b64261f,0x2872aaa7,
0x2c000000,0xa23eabd9,0xab64261f,0x28722aa7,0xac000000,0x28722aa7,0x261fab64,0xabd9223e,0x2c000000,0xaaa7a872,0x261f2b64,0x223eabd9,0xac000000,0x2bd9223e,0xab64a61f,0x2aa72872,
0x2c000000,0xabd9223e,0x2b64a61f,0xaaa72872,0xac000000,0x2aa7a872,0xa61f2b64,0xa23eabd9,0x2c000000,0xa8722aa7,0xa61fab64,0x2bd9223e,0xac000000,0x223eabd9,0x2b64261f,0xa8722aa7,
0x2c000000,0x223e2bd9,0xab64261f,0xa872aaa7,0xac000000,0xa872aaa7,0x261fab64,0x2bd9a23e,0x2c000000,0x2aa72872,0x261f2b64,0xa23e2bd9,0xac000000,0xabd9a23e,0xab64a61f,0xaaa7a872,
0x29a8a9a8,0x2872aaa7,0x261fab64,0x223eabd9,0x29a829a8,0x2bd9223e,0x2b64a61f,0x2872aaa7,0xa9a829a8,0x223e2bd9,0x2b64261f,0x2aa7a872,0xa9a8a9a8,0xaaa72872,0x261f2b64,0x2bd9a23e,
0x29a8a9a8,0xaaa7a872,0xa61f2b64,0x2bd9223e,0x29a829a8,0x223eabd9,0xab64261f,0x2aa72872,0xa9a829a8,0x2bd9a23e,0xab64a61f,0x28722aa7,0xa9a8a9a8,0x28722aa7,0xa61fab64,0x223e2bd9,
0x29a8a9a8,0xa8722aa7,0x261fab64,0xa23e2bd9,0x29a829a8,0xabd9a23e,0x2b64a61f,0xa8722aa7,0xa9a829a8,0xa23eabd9,0x2b64261f,0xaaa72872,0xa9a8a9a8,0x2aa7a872,0x261f2b64,0xabd9223e,
0x29a8a9a8,0x2aa72872,0xa61f2b64,0xabd9a23e,0x29a829a8,0xa23e2bd9,0xab64261f,0xaaa7a872,0xa9a829a8,0xabd9223e,0xab64a61f,0xa872aaa7,0xa9a8a9a8,0xa872aaa7,0xa61fab64,0xa23eabd9,
0x2c000000,0x2bd9a23e,0x2b64a61f,0x2aa7a872,0x2c000000,0x2aa7a872,0x261fab64,0xa23eabd9,0x2c000000,0x2872aaa7,0xa61fab64,0xabd9a23e,0x2c000000,0x223eabd9,0xab64a61f,0xa8722aa7,
0x2c000000,0xa23eabd9,0xab64261f,0x28722aa7,0x2c000000,0xa872aaa7,0xa61f2b64,0x2bd9a23e,0x2c000000,0xaaa7a872,0x261f2b64,0x223eabd9,0x2c000000,0xabd9a23e,0x2b64261f,0xaaa7a872,
0x2c000000,0xabd9223e,0x2b64a61f,0xaaa72872,0x2c000000,0xaaa72872,0x261fab64,0x223e2bd9,0x2c000000,0xa8722aa7,0xa61fab64,0x2bd9223e,0x2c000000,0xa23e2bd9,0xab64a61f,0x2872aaa7,
0x2c000000,0x223e2bd9,0xab64261f,0xa872aaa7,0x2c000000,0x28722aa7,0xa61f2b64,0xabd9223e,0x2c000000,0x2aa72872,0x261f2b64,0xa23e2bd9,0x2c000000,0x2bd9223e,0x2b64261f,0x2aa72872,
0x29a8a9a8,0x2872aaa7,0x261fab64,0x223eabd9,0xa9a8a9a8,0xabd9a23e,0xab64261f,0xa8722aa7,0xa9a829a8,0x223e2bd9,0x2b64261f,0x2aa7a872,0x29a829a8,0x2aa7a872,0xa61fab64,0xabd9223e,
0x29a8a9a8,0xaaa7a872,0xa61f2b64,0x2bd9223e,0xa9a8a9a8,0xa23e2bd9,0x2b64a61f,0xaaa7a872,0xa9a829a8,0x2bd9a23e,0xab64a61f,0x28722aa7,0x29a829a8,0xa872aaa7,0x261f2b64,0xa23eabd9,
0x29a8a9a8,0xa8722aa7,0x261fab64,0xa23e2bd9,0xa9a8a9a8,0x2bd9223e,0xab64261f,0x2872aaa7,0xa9a829a8,0xa23eabd9,0x2b64261f,0xaaa72872,0x29a829a8,0xaaa72872,0xa61fab64,0x2bd9a23e,
0x29a8a9a8,0x2aa72872,0xa61f2b64,0xabd9a23e,0xa9a8a9a8,0x223eabd9,0x2b64a61f,0x2aa72872,0xa9a829a8,0xabd9223e,0xab64a61f,0xa872aaa7,0x29a829a8,0x28722aa7,0x261f2b64,0x223e2bd9,
0xac00,0xa23eabd9,0xa61fab64,0xa872aaa7,0x2c00,0x28722aa7,0x2b64261f,0x2bd9a23e,0xac00,0xaaa7a872,0xab64261f,0xa23e2bd9,0x2c00,0x2bd9223e,0x261fab64,0xaaa7a872,
0xac00,0xabd9223e,0x261f2b64,0x2aa7a872,0x2c00,0x2aa7a872,0xab64a61f,0x223e2bd9,0xac00,0xa8722aa7,0x2b64a61f,0xabd9a23e,0x2c00,0x223eabd9,0xa61f2b64,0x2872aaa7,
0xac00,0x223e2bd9,0xa61fab64,0x28722aa7,0x2c00,0xa872aaa7,0x2b64261f,0xabd9223e,0xac00,0x2aa72872,0xab64261f,0x223eabd9,0x2c00,0xabd9a23e,0x261fab64,0x2aa72872,
0xac00,0x2bd9a23e,0x261f2b64,0xaaa72872,0x2c00,0xaaa72872,0xab64a61f,0xa23eabd9,0xac00,0x2872aaa7,0x2b64a61f,0x2bd9223e,0x2c00,0xa23e2bd9,0xa61f2b64,0xa8722aa7,
0xa9a8a9a8,0xaaa7a872,0xab64a61f,0xabd9a23e,0x29a8a9a8,0x223eabd9,0xa61fab64,0xaaa7a872,0x29a829a8,0x2bd9a23e,0x261fab64,0xa872aaa7,0xa9a829a8,0x28722aa7,0x2b64a61f,0xa23eabd9,
0xa9a8a9a8,0xa8722aa7,0x2b64261f,0x223eabd9,0x29a8a9a8,0xabd9a23e,0x261f2b64,0x2872aaa7,0x29a829a8,0xa23eabd9,0xa61f2b64,0x2aa7a872,0xa9a829a8,0x2aa7a872,0xab64261f,0x2bd9a23e,
0xa9a8a9a8,0x2aa72872,0xab64a61f,0x2bd9223e,0x29a8a9a8,0xa23e2bd9,0xa61fab64,0x2aa72872,0x29a829a8,0xabd9223e,0x261fab64,0x28722aa7,0xa9a829a8,0xa872aaa7,0x2b64a61f,0x223e2bd9,
0xa9a8a9a8,0x2872aaa7,0x2b64261f,0xa23e2bd9,0x29a8a9a8,0x2bd9223e,0x261f2b64,0xa8722aa7,0x29a829a8,0x223e2bd9,0xa61f2b64,0xaaa72872,0xa9a829a8,0xaaa72872,0xab64261f,0xabd9223e,
0x80002c00,0x80002c00,0x80002c00,0x80002c00,0x223e2bd9,0x28722aa7,0x2aa72872,0x2bd9223e,0x261f2b64,0x2b64261f,0x2b64a61f,0x261fab64,0x28722aa7,0x2bd9a23e,0x223eabd9,0xaaa7a872,
0x29a829a8,0x29a8a9a8,0xa9a8a9a8,0xa9a829a8,0x2aa72872,0x223eabd9,0xabd9223e,0x28722aa7,0x2b64261f,0xa61fab64,0xa61f2b64,0x2b64a61f,0x2bd9223e,0xaaa7a872,0x28722aa7,0xa23eabd9,
0x2c000000,0xac000000,0x2c000000,0xac000000,0x2bd9a23e,0xaaa72872,0x2872aaa7,0xa23e2bd9,0x2b64a61f,0xa61f2b64,0xa61fab64,0x2b64261f,0x2aa7a872,0x223e2bd9,0xabd9a23e,0x2872aaa7,
0x29a8a9a8,0x29a829a8,0xa9a829a8,0xa9a8a9a8,0x2872aaa7,0x2bd9223e,0x223e2bd9,0xaaa72872,0x261fab64,0x2b64a61f,0x2b64261f,0x261f2b64,0x223eabd9,0x2872aaa7,0x2aa7a872,0x2bd9a23e,
0x80002c00,0x80002c00,0x80002c00,0x80002c00,0x2bd9a23e,0x2aa7a872,0x2872aaa7,0x223eabd9,0xa61fab64,0xab64a61f,0xab64261f,0xa61f2b64,0xaaa72872,0x223e2bd9,0x2bd9223e,0x2872aaa7,
0x29a829a8,0x29a8a9a8,0xa9a8a9a8,0xa9a829a8,0x2872aaa7,0xabd9a23e,0x223e2bd9,0x2aa7a872,0xab64a61f,0x261f2b64,0x261fab64,0xab64261f,0xa23e2bd9,0x2872aaa7,0xaaa72872,0x2bd9a23e,
0x2c000000,0xac000000,0x2c000000,0xac000000,0xa23eabd9,0x28722aa7,0xaaa7a872,0x2bd9223e,0xab64261f,0x261fab64,0x261f2b64,0xab64a61f,0x28722aa7,0xabd9223e,0x223eabd9,0x2aa72872,
0x29a8a9a8,0x29a829a8,0xa9a829a8,0xa9a8a9a8,0xaaa7a872,0x223eabd9,0x2bd9a23e,0x28722aa7,0xa61f2b64,0xab64261f,0xab64a61f,0xa61fab64,0x2bd9223e,0x2aa72872,0x28722aa7,0x223e2bd9,
0x80002c00,0x80002c00,0x80002c00,0x80002c00,0xa23eabd9,0xa872aaa7,0xaaa7a872,0xabd9a23e,0x261f2b64,0x2b64261f,0x2b64a61f,0x261fab64,0xa872aaa7,0xabd9223e,0xa23e2bd9,0x2aa72872,
0x29a829a8,0x29a8a9a8,0xa9a8a9a8,0xa9a829a8,0xaaa7a872,0xa23e2bd9,0x2bd9a23e,0xa872aaa7,0x2b64261f,0xa61fab64,0xa61f2b64,0x2b64a61f,0xabd9a23e,0x2aa72872,0xa872aaa7,0x223e2bd9,
0x2c000000,0xac000000,0x2c000000,0xac000000,0xabd9223e,0x2aa7a872,0xa8722aa7,0x223eabd9,0x2b64a61f,0xa61f2b64,0xa61fab64,0x2b64261f,0xaaa72872,0xa23eabd9,0x2bd9223e,0xa8722aa7,
0x29a8a9a8,0x29a829a8,0xa9a829a8,0xa9a8a9a8,0xa8722aa7,0xabd9a23e,0xa23eabd9,0x2aa7a872,0x261fab64,0x2b64a61f,0x2b64261f,0x261f2b64,0xa23e2bd9,0xa8722aa7,0xaaa72872,0xabd9223e,
0x80002c00,0x80002c00,0x80002c00,0x80002c00,0xabd9223e,0xaaa72872,0xa8722aa7,0xa23e2bd9,0xa61fab64,0xab64a61f,0xab64261f,0xa61f2b64,0x2aa7a872,0xa23eabd9,0xabd9a23e,0xa8722aa7,
0x29a829a8,0x29a8a9a8,0xa9a8a9a8,0xa9a829a8,0xa8722aa7,0x2bd9223e,0xa23eabd9,0xaaa72872,0xab64a61f,0x261f2b64,0x261fab64,0xab64261f,0x223eabd9,0xa8722aa7,0x2aa7a872,0xabd9223e,
0x2c000000,0xac000000,0x2c000000,0xac000000,0x223e2bd9,0xa872aaa7,0x2aa72872,0xabd9a23e,0xab64261f,0x261fab64,0x261f2b64,0xab64a61f,0xa872aaa7,0x2bd9a23e,0xa23e2bd9,0xaaa7a872,
0x29a8a9a8,0x29a829a8,0xa9a829a8,0xa9a8a9a8,0x2aa72872,0xa23e2bd9,0xabd9223e,0xa872aaa7,0xa61f2b64,0xab64261f,0xab64a61f,0xa61fab64,0xabd9a23e,0xaaa7a872,0xa872aaa7,0xa23eabd9,
0x2c000000,0x2c000000,0x2c000000,0x2c000000,0x2bd9a23e,0x2aa7a872,0x2872aaa7,0x223eabd9,0x2b64a61f,0x261fab64,0xa61fab64,0xab64a61f,0x2aa7a872,0xa23eabd9,0xabd9a23e,0xa8722aa7,
0x29a8a9a8,0xa9a8a9a8,0xa9a829a8,0x29a829a8,0x2872aaa7,0xabd9a23e,0x223e2bd9,0x2aa7a872,0x261fab64,0xab64261f,0x2b64261f,0xa61fab64,0x223eabd9,0xa8722aa7,0x2aa7a872,0xabd9223e,
0xac00,0x2c00,0xac00,0x2c00,0xa23eabd9,0x28722aa7,0xaaa7a872,0x2bd9223e,0xa61fab64,0x2b64261f,0xab64261f,0x261fab64,0xa872aaa7,0x2bd9a23e,0xa23e2bd9,0xaaa7a872,
0xa9a8a9a8,0x29a8a9a8,0x29a829a8,0xa9a829a8,0xaaa7a872,0x223eabd9,0x2bd9a23e,0x28722aa7,0xab64a61f,0xa61fab64,0x261fab64,0x2b64a61f,0xabd9a23e,0xaaa7a872,0xa872aaa7,0xa23eabd9,
0x2c000000,0x2c000000,0x2c000000,0x2c000000,0xa23eabd9,0xa872aaa7,0xaaa7a872,0xabd9a23e,0xab64261f,0xa61f2b64,0x261f2b64,0x2b64261f,0x28722aa7,0x2bd9a23e,0x223eabd9,0xaaa7a872,
0x29a8a9a8,0xa9a8a9a8,0xa9a829a8,0x29a829a8,0xaaa7a872,0xa23e2bd9,0x2bd9a23e,0xa872aaa7,0xa61f2b64,0x2b64a61f,0xab64a61f,0x261f2b64,0x2bd9223e,0xaaa7a872,0x28722aa7,0xa23eabd9,
0xac00,0x2c00,0xac00,0x2c00,0xabd9223e,0x2aa7a872,0xa8722aa7,0x223eabd9,0x261f2b64,0xab64a61f,0x2b64a61f,0xa61f2b64,0x2aa7a872,0x223e2bd9,0xabd9a23e,0x2872aaa7,
0xa9a8a9a8,0x29a8a9a8,0x29a829a8,0xa9a829a8,0xa8722aa7,0xabd9a23e,0xa23eabd9,0x2aa7a872,0x2b64261f,0x261f2b64,0xa61f2b64,0xab64261f,0x223eabd9,0x2872aaa7,0x2aa7a872,0x2bd9a23e,
0x2c000000,0x2c000000,0x2c000000,0x2c000000,0xabd9223e,0xaaa72872,0xa8722aa7,0xa23e2bd9,0x2b64a61f,0x261fab64,0xa61fab64,0xab64a61f,0xaaa72872,0x223e2bd9,0x2bd9223e,0x2872aaa7,
0x29a8a9a8,0xa9a8a9a8,0xa9a829a8,0x29a829a8,0xa8722aa7,0x2bd9223e,0xa23eabd9,0xaaa72872,0x261fab64,0xab64261f,0x2b64261f,0xa61fab64,0xa23e2bd9,0x2872aaa7,0xaaa72872,0x2bd9a23e,
0xac00,0x2c00,0xac00,0x2c00,0x223e2bd9,0xa872aaa7,0x2aa72872,0xabd9a23e,0xa61fab64,0x2b64261f,0xab64261f,0x261fab64,0x28722aa7,0xabd9223e,0x223eabd9,0x2aa72872,
0xa9a8a9a8,0x29a8a9a8,0x29a829a8,0xa9a829a8,0x2aa72872,0xa23e2bd9,0xabd9223e,0xa872aaa7,0xab64a61f,0xa61fab64,0x261fab64,0x2b64a61f,0x2bd9223e,0x2aa72872,0x28722aa7,0x223e2bd9,
0x2c000000,0x2c000000,0x2c000000,0x2c000000,0x223e2bd9,0x28722aa7,0x2aa72872,0x2bd9223e,0xab64261f,0xa61f2b64,0x261f2b64,0x2b64261f,0xa872aaa7,0xabd9223e,0xa23e2bd9,0x2aa72872,
0x29a8a9a8,0xa9a8a9a8,0xa9a829a8,0x29a829a8,0x2aa72872,0x223eabd9,0xabd9223e,0x28722aa7,0xa61f2b64,0x2b64a61f,0xab64a61f,0x261f2b64,0xabd9a23e,0x2aa72872,0xa872aaa7,0x223e2bd9,
0xac00,0x2c00,0xac00,0x2c00,0x2bd9a23e,0xaaa72872,0x2872aaa7,0xa23e2bd9,0x261f2b64,0xab64a61f,0x2b64a61f,0xa61f2b64,0xaaa72872,0xa23eabd9,0x2bd9223e,0xa8722aa7,
0xa9a8a9a8,0x29a8a9a8,0x29a829a8,0xa9a829a8,0x2872aaa7,0x2bd9223e,0x223e2bd9,0xaaa72872,0x2b64261f,0x261f2b64,0xa61f2b64,0xab64261f,0xa23e2bd9,0xa8722aa7,0xaaa72872,0xabd9223e,
0x80002c00,0x80002c00,0x80002c00,0x80002c00,0x80002c00,0x261f2b64,0x29a829a8,0x2b64261f,0x80002c00,0x29a829a8,0x2c000000,0x29a8a9a8,0x80002c00,0x2b64261f,0x29a8a9a8,0xa61fab64,
0x80002c00,0x2c000000,0x8000ac00,0xac000000,0x80002c00,0x2b64a61f,0xa9a8a9a8,0xa61f2b64,0x80002c00,0x29a8a9a8,0xac000000,0x29a829a8,0x80002c00,0x261fab64,0xa9a829a8,0x2b64a61f,
0x80002c00,0x8000ac00,0x80002c00,0x8000ac00,0x80002c00,0xa61fab64,0x29a829a8,0xab64a61f,0x80002c00,0xa9a8a9a8,0x2c000000,0xa9a829a8,0x80002c00,0xab64a61f,0x29a8a9a8,0x261f2b64,
0x80002c00,0xac000000,0x8000ac00,0x2c000000,0x80002c00,0xab64261f,0xa9a8a9a8,0x261fab64,0x80002c00,0xa9a829a8,0xac000000,0xa9a8a9a8,0x80002c00,0xa61f2b64,0xa9a829a8,0xab64261f,
0x80002c00,0x80002c00,0x80002c00,0x80002c00,0x2c000000,0x2b64a61f,0x29a8a9a8,0x261fab64,0x8000ac00,0xa9a8a9a8,0xac000000,0xa9a829a8,0xac000000,0xa61f2b64,0x29a829a8,0x2b64a61f,
0x80002c00,0x2c000000,0x8000ac00,0xac000000,0x2c000000,0xa61fab64,0xa9a829a8,0x2b64261f,0x8000ac00,0xa9a829a8,0x2c000000,0xa9a8a9a8,0xac000000,0x2b64261f,0xa9a8a9a8,0x261f2b64,
0x80002c00,0x8000ac00,0x80002c00,0x8000ac00,0x2c000000,0xab64261f,0x29a8a9a8,0xa61f2b64,0x8000ac00,0x29a829a8,0xac000000,0x29a8a9a8,0xac000000,0x261fab64,0x29a829a8,0xab64261f,
0x80002c00,0xac000000,0x8000ac00,0x2c000000,0x2c000000,0x261f2b64,0xa9a829a8,0xab64a61f,0x8000ac00,0x29a8a9a8,0x2c000000,0x29a829a8,0xac000000,0xab64a61f,0xa9a8a9a8,0xa61fab64,
0x80002c00,0x80002c00,0x80002c00,0x80002c00,0x8000ac00,0xa61fab64,0xa9a8a9a8,0xab64a61f,0x80002c00,0x29a829a8,0x2c000000,0x29a8a9a8,0x8000ac00,0xab64a61f,0xa9a829a8,0x261f2b64,
0x80002c00,0x2c000000,0x8000ac00,0xac000000,0x8000ac00,0xab64261f,0x29a829a8,0x261fab64,0x80002c00,0x29a8a9a8,0xac000000,0x29a829a8,0x8000ac00,0xa61f2b64,0x29a8a9a8,0xab64261f,
0x80002c00,0x8000ac00,0x80002c00,0x8000ac00,0x8000ac00,0x261f2b64,0xa9a8a9a8,0x2b64261f,0x80002c00,0xa9a8a9a8,0x2c000000,0xa9a829a8,0x8000ac00,0x2b64261f,0xa9a829a8,0xa61fab64,
0x80002c00,0xac000000,0x8000ac00,0x2c000000,0x8000ac00,0x2b64a61f,0x29a829a8,0xa61f2b64,0x80002c00,0xa9a829a8,0xac000000,0xa9a8a9a8,0x8000ac00,0x261fab64,0x29a8a9a8,0x2b64a61f,
0x80002c00,0x80002c00,0x80002c00,0x80002c00,0xac000000,0xab64261f,0xa9a829a8,0xa61f2b64,0x8000ac00,0xa9a8a9a8,0xac000000,0xa9a829a8,0x2c000000,0x261fab64,0xa9a8a9a8,0xab64261f,
0x80002c00,0x2c000000,0x8000ac00,0xac000000,0xac000000,0x261f2b64,0x29a8a9a8,0xab64a61f,0x8000ac00,0xa9a829a8,0x2c000000,0xa9a8a9a8,0x2c000000,0xab64a61f,0x29a829a8,0xa61fab64,
0x80002c00,0x8000ac00,0x80002c00,0x8000ac00,0xac000000,0x2b64a61f,0xa9a829a8,0x261fab64,0x8000ac00,0x29a829a8,0xac000000,0x29a8a9a8,0x2c000000,0xa61f2b64,0xa9a8a9a8,0x2b64a61f,
0x80002c00,0xac000000,0x8000ac00,0x2c000000,0xac000000,0xa61fab64,0x29a8a9a8,0x2b64261f,0x8000ac00,0x29a8a9a8,0x2c000000,0x29a829a8,0x2c000000,0x2b64261f,0x29a829a8,0x261f2b64,
0x2c000000,0x2c000000,0x2c000000,0x2c000000,0x2c000000,0x2b64a61f,0x29a8a9a8,0x261fab64,0x2c000000,0x29a8a9a8,0xac00,0xa9a8a9a8,0x2c000000,0x261fab64,0xa9a8a9a8,0xab64261f,
0x2c000000,0xac00,0xac000000,0x2c00,0x2c000000,0xa61fab64,0xa9a829a8,0x2b64261f,0x2c000000,0xa9a8a9a8,0x2c00,0x29a8a9a8,0x2c000000,0xab64a61f,0x29a829a8,0xa61fab64,
0x2c000000,0xac000000,0x2c000000,0xac000000,0x2c000000,0xab64261f,0x29a8a9a8,0xa61f2b64,0x2c000000,0xa9a829a8,0xac00,0x29a829a8,0x2c000000,0xa61f2b64,0xa9a8a9a8,0x2b64a61f,
0x2c000000,0x2c00,0xac000000,0xac00,0x2c000000,0x261f2b64,0xa9a829a8,0xab64a61f,0x2c000000,0x29a829a8,0x2c00,0xa9a829a8,0x2c000000,0x2b64261f,0x29a829a8,0x261f2b64,
0x2c000000,0x2c000000,0x2c000000,0x2c000000,0xac00,0xa61fab64,0xa9a8a9a8,0xab64a61f,0xac000000,0xa9a829a8,0x2c00,0x29a829a8,0x2c00,0x2b64261f,0x29a8a9a8,0xa61fab64,
0x2c000000,0xac00,0xac000000,0x2c00,0xac00,0xab64261f,0x29a829a8,0x261fab64,0xac000000,0x29a829a8,0xac00,0xa9a829a8,0x2c00,0x261fab64,0xa9a829a8,0x2b64a61f,
0x2c000000,0xac000000,0x2c000000,0xac000000,0xac00,0x261f2b64,0xa9a8a9a8,0x2b64261f,0xac000000,0x29a8a9a8,0x2c00,0xa9a8a9a8,0x2c00,0xab64a61f,0x29a8a9a8,0x261f2b64,
0x2c000000,0x2c00,0xac000000,0xac00,0xac00,0x2b64a61f,0x29a829a8,0xa61f2b64,0xac000000,0xa9a8a9a8,0xac00,0x29a8a9a8,0x2c00,0xa61f2b64,0xa9a829a8,0xab64261f,
0x2c000000,0x2c000000,0x2c000000,0x2c000000,0xac000000,0xab64261f,0xa9a829a8,0xa61f2b64,0x2c000000,0x29a8a9a8,0xac00,0xa9a8a9a8,0xac000000,0xa61f2b64,0x29a829a8,0x2b64a61f,
0x2c000000,0xac00,0xac000000,0x2c00,0xac000000,0x261f2b64,0x29a8a9a8,0xab64a61f,0x2c000000,0xa9a8a9a8,0x2c00,0x29a8a9a8,0xac000000,0x2b64261f,0xa9a8a9a8,0x261f2b64,
0x2c000000,0xac000000,0x2c000000,0xac000000,0xac000000,0x2b64a61f,0xa9a829a8,0x261fab64,0x2c000000,0xa9a829a8,0xac00,0x29a829a8,0xac000000,0x261fab64,0x29a829a8,0xab64261f,
0x2c000000,0x2c00,0xac000000,0xac00,0xac000000,0xa61fab64,0x29a8a9a8,0x2b64261f,0x2c000000,0x29a829a8,0x2c00,0xa9a829a8,0xac000000,0xab64a61f,0xa9a8a9a8,0xa61fab64,
0x2c000000,0x2c000000,0x2c000000,0x2c000000,0x2c00,0x261f2b64,0x29a829a8,0x2b64261f,0xac000000,0xa9a829a8,0x2c00,0x29a829a8,0xac00,0xab64a61f,0xa9a829a8,0x261f2b64,
0x2c000000,0xac00,0xac000000,0x2c00,0x2c00,0x2b64a61f,0xa9a8a9a8,0xa61f2b64,0xac000000,0x29a829a8,0xac00,0xa9a829a8,0xac00,0xa61f2b64,0x29a8a9a8,0xab64261f,
0x2c000000,0xac000000,0x2c000000,0xac000000,0x2c00,0xa61fab64,0x29a829a8,0xab64a61f,0xac000000,0x29a8a9a8,0x2c00,0xa9a8a9a8,0xac00,0x2b64261f,0xa9a829a8,0xa61fab64,
0x2c000000,0x2c00,0xac000000,0xac00,0x2c00,0xab64261f,0xa9a8a9a8,0x261fab64,0xac000000,0xa9a8a9a8,0xac00,0x29a8a9a8,0xac00,0x261fab64,0x29a8a9a8,0x2b64a61f,
0x3f800000,0x0,0x3f7ffb11,0xbc490e90,0x3f800000,0x0,0x3f7fd397,0xbd16c32c,0x3f800000,0x0,0x3f7f84ab,0xbd7b2b74,0x3f800000,0x0,0x3f7f0e58,0xbdafb680,
0x3f800000,0x0,0x3f7e70b0,0xbde1bc2e,0x3f800000,0x0,0x3f7dabcc,0xbe09cf86,0x3f800000,0x0,0x3f7cbfc9,0xbe22abb6,0x3f800000,0x0,0x3f7baccd,0xbe3b6ecf,
0x3f800000,0x0,0x3f7a7302,0xbe541501,0x3f800000,0x0,0x3f791298,0xbe6c9a7f,0x3f800000,0x0,0x3f778bc5,0xbe827dc0,0x3f800000,0x0,0x3f75dec6,0xbe8e9a22,
0x3f800000,0x0,0x3f740bdd,0xbe9aa086,0x3f800000,0x0,0x3f721352,0xbea68f12,0x3f800000,0x0,0x3f6ff573,0xbeb263ef,0x3f800000,0x0,0x3f6db293,0xbebe1d4a,
0x3f7fb10f,0xbd48fb30,0x3f7f84ab,0xbd7b2b74,0x3f7d3aac,0xbe164083,0x3f7baccd,0xbe3b6ecf,0x3f7853f8,0xbe78cfcc,0x3f740bdd,0xbe9aa086,0x3f710908,0xbeac7cd4,0x3f68bf3c,0xbed53641,
0x3f676bd8,0xbedae880,0x3f59f26a,0xbf064b82,0x3f5b941a,0xbf039c3d,0x3f47de65,0xbf1ff6cb,0x3f4d9f02,0xbf187fc0,0x3f32c8c9,0xbf373a23,0x3f3daef9,0xbf2beb4a,0x3f1b02c6,0xbf4bbbf8,
0x3f2beb4a,0xbf3daef9,0x3f00e7e4,0xbf5d2d53,0x3f187fc0,0xbf4d9f02,0x3ec9b953,0xbf6b4b0c,0x3f039c3d,0xbf5b941a,0x3e8e9a22,0xbf75dec6,0x3edae880,0xbf676bd8,0x3e22abb6,0xbf7cbfc9,
0x3eac7cd4,0xbf710908,0x3d16c32c,0xbf7fd397,0x3e78cfcc,0xbf7853f8,0xbdafb680,0xbf7f0e58,0x3e164083,0xbf7d3aac,0xbe541501,0xbf7a7302,0x3d48fb30,0xbf7fb10f,0xbea68f12,0xbf721352,
0x3f7ec46d,0xbdc8bd36,0x3f7e70b0,0xbde1bc2e,0x3f74fa0b,0xbe94a031,0x3f721352,0xbea68f12,0x3f61c598,0xbef15aea,0x3f59f26a,0xbf064b82,0x3f45e403,0xbf226799,0x3f373a23,0xbf32c8c9,
0x3f226799,0xbf45e403,0x3f0b9a6b,0xbf5695e5,0x3ef15aea,0xbf61c598,0x3eb263ef,0xbf6ff573,0x3e94a031,0xbf74fa0b,0x3e09cf86,0xbf7dabcc,0x3dc8bd36,0xbf7ec46d,0xbdafb680,0xbf7f0e58,
0xbdc8bd36,0xbf7ec46d,0xbe9aa086,0xbf740bdd,0xbe94a031,0xbf74fa0b,0xbf00e7e4,0xbf5d2d53,0xbef15aea,0xbf61c598,0xbf2e3bde,0xbf3b8f3b,0xbf226799,0xbf45e403,0xbf531849,0xbf10d3cd,
0xbf45e403,0xbf226799,0xbf6db293,0xbebe1d4a,0xbf61c598,0xbef15aea,0xbf7cbfc9,0xbe22abb6,0xbf74fa0b,0xbe94a031,0xbf7f84ab,0x3d7b2b74,0xbf7ec46d,0xbdc8bd36,0xbf75dec6,0x3e8e9a22,
0x3f7d3aac,0xbe164083,0x3f7cbfc9,0xbe22abb6,0x3f676bd8,0xbedae880,0x3f633c5a,0xbeebcbbb,0x3f3daef9,0xbf2beb4a,0x3f32c8c9,0xbf373a23,0x3f039c3d,0xbf5b941a,0x3ee0924f,0xbf660f88,
0x3e78cfcc,0xbf7853f8,0x3e09cf86,0xbf7dabcc,0xbd48fb30,0xbf7fb10f,0xbe3b6ecf,0xbf7baccd,0xbeac7cd4,0xbf710908,0xbef6e0cb,0xbf604621,0xbf187fc0,0xbf4d9f02,0xbf3b8f3b,0xbf2e3bde,
0xbf4d9f02,0xbf187fc0,0xbf68bf3c,0xbed53641,0xbf710908,0xbeac7cd4,0xbf7e70b0,0xbde1bc2e,0xbf7fb10f,0xbd48fb30,0xbf7a7302,0x3e541501,0xbf7853f8,0x3e78cfcc,0xbf5d2d53,0x3f00e7e4,
0xbf5b941a,0x3f039c3d,0xbf299415,0x3f3fc767,0xbf2beb4a,0x3f3daef9,0xbec9b953,0x3f6b4b0c,0xbedae880,0x3f676bd8,0xbdafb680,0x3f7f0e58,0xbe164083,0x3f7d3aac,0x3e6c9a7f,0x3f791298,
0x3f7fec43,0xbcc90ab0,0x3f7fd397,0xbd16c32c,0x3f7f4e6d,0xbd96a905,0x3f7e70b0,0xbde1bc2e,0x3f7e1324,0xbdfab273,0x3f7baccd,0xbe3b6ecf,0x3f7c3b28,0xbe2f10a2,0x3f778bc5,0xbe827dc0,
0x3f79c79d,0xbe605c13,0x3f721352,0xbea68f12,0x3f76ba07,0xbe888e93,0x3f6b4b0c,0xbec9b953,0x3f731447,0xbea09ae5,0x3f633c5a,0xbeebcbbb,0x3f6ed89e,0xbeb8442a,0x3f59f26a,0xbf064b82,
0x3f6a09a7,0xbecf7bca,0x3f4f7a1f,0xbf15f6d9,0x3f64aa59,0xbee63375,0x3f43e200,0xbf24d225,0x3f5ebe05,0xbefc5d27,0x3f373a23,0xbf32c8c9,0x3f584853,0xbf08f59b,0x3f299415,0xbf3fc767,
0x3f514d3d,0xbf13682a,0x3f1b02c6,0xbf4bbbf8,0x3f49d112,0xbf1d7fd1,0x3f0b9a6b,0xbf5695e5,0x3f41d870,0xbf273656,0x3ef6e0cb,0xbf604621,0x3f396842,0xbf3085bb,0x3ed53641,0xbf68bf3c,
0x3f7f4e6d,0xbd96a905,0x3f7f0e58,0xbdafb680,0x3f79c79d,0xbe605c13,0x3f778bc5,0xbe827dc0,0x3f6ed89e,0xbeb8442a,0x3f68bf3c,0xbed53641,0x3f5ebe05,0xbefc5d27,0x3f531849,0xbf10d3cd,
0x3f49d112,0xbf1d7fd1,0x3f373a23,0xbf32c8c9,0x3f3085bb,0xbf396842,0x3f15f6d9,0xbf4f7a1f,0x3f13682a,0xbf514d3d,0x3ee0924f,0xbf660f88,0x3ee63375,0xbf64aa59,0x3e8e9a22,0xbf75dec6,
0x3ea09ae5,0xbf731447,0x3de1bc2e,0xbf7e70b0,0x3e2f10a2,0xbf7c3b28,0xbd7b2b74,0xbf7f84ab,0x3cc90ab0,0xbf7fec43,0xbe6c9a7f,0xbf791298,0xbdfab273,0xbf7e1324,0xbec9b953,0xbf6b4b0c,
0xbe888e93,0xbf76ba07,0xbf0b9a6b,0xbf5695e5,0xbecf7bca,0xbf6a09a7,0xbf2e3bde,0xbf3b8f3b,0xbf08f59b,0xbf584853,0xbf4bbbf8,0xbf1b02c6,0xbf273656,0xbf41d870,0xbf633c5a,0xbeebcbbb,
0x3f7e1324,0xbdfab273,0x3f7dabcc,0xbe09cf86,0x3f6ed89e,0xbeb8442a,0x3f6b4b0c,0xbec9b953,0x3f514d3d,0xbf13682a,0x3f47de65,0xbf1ff6cb,0x3f273656,0xbf41d870,0x3f15f6d9,0xbf4f7a1f,
0x3ee63375,0xbf64aa59,0x3eb263ef,0xbf6ff573,0x3e605c13,0xbf79c79d,0x3dafb680,0xbf7f0e58,0xbcc90ab0,0xbf7fec43,0xbe3b6ecf,0xbf7baccd,0xbe888e93,0xbf76ba07,0xbee0924f,0xbf660f88,
0xbefc5d27,0xbf5ebe05,0xbf299415,0xbf3fc767,0xbf3085bb,0xbf396842,0xbf5695e5,0xbf0b9a6b,0xbf584853,0xbf08f59b,0xbf740bdd,0xbe9aa086,0xbf731447,0xbea09ae5,0xbf7fd397,0xbd16c32c,
0xbf7f4e6d,0xbd96a905,0xbf791298,0x3e6c9a7f,0xbf7c3b28,0x3e2f10a2,0xbf604621,0x3ef6e0cb,0xbf6a09a7,0x3ecf7bca,0xbf373a23,0x3f32c8c9,0xbf49d112,0x3f1d7fd1,0xbf00e7e4,0x3f5d2d53,
0x3f7c3b28,0xbe2f10a2,0x3f7baccd,0xbe3b6ecf,0x3f5ebe05,0xbefc5d27,0x3f59f26a,0xbf064b82,0x3f273656,0xbf41d870,0x3f1b02c6,0xbf4bbbf8,0x3eb8442a,0xbf6ed89e,0x3e8e9a22,0xbf75dec6,
0x3cc90ab0,0xbf7fec43,0xbdafb680,0xbf7f0e58,0xbea09ae5,0xbf731447,0xbee0924f,0xbf660f88,0xbf1d7fd1,0xbf49d112,0xbf3b8f3b,0xbf2e3bde,0xbf584853,0xbf08f59b,0xbf6db293,0xbebe1d4a,
0xbf79c79d,0xbe605c13,0xbf7ffb11,0xbc490e90,0xbf7e1324,0x3dfab273,0xbf6ff573,0x3eb263ef,0xbf64aa59,0x3ee63375,0xbf3fc767,0x3f299415,0xbf3085bb,0x3f396842,0xbeebcbbb,0x3f633c5a,
0xbecf7bca,0x3f6a09a7,0xbde1bc2e,0x3f7e70b0,0xbd96a905,0x3f7f4e6d,0x3e827dc0,0x3f778bc5,0x3e888e93,0x3f76ba07,0x3f15f6d9,0x3f4f7a1f,0x3f13682a,0x3f514d3d,0x3f5695e5,0x3f0b9a6b,
0x3f800000,0x0,0x3f800000,0x0,0x3f7ffb11,0xbc490e90,0x3f7fd397,0xbd16c32c,0x3f7fec43,0xbcc90ab0,0x3f7f4e6d,0xbd96a905,0x3f7fd397,0xbd16c32c,0x3f7e70b0,0xbde1bc2e,
0x3f7fb10f,0xbd48fb30,0x3f7d3aac,0xbe164083,0x3f7f84ab,0xbd7b2b74,0x3f7baccd,0xbe3b6ecf,0x3f7f4e6d,0xbd96a905,0x3f79c79d,0xbe605c13,0x3f7f0e58,0xbdafb680,0x3f778bc5,0xbe827dc0,
0x3f7ec46d,0xbdc8bd36,0x3f74fa0b,0xbe94a031,0x3f7e70b0,0xbde1bc2e,0x3f721352,0xbea68f12,0x3f7e1324,0xbdfab273,0x3f6ed89e,0xbeb8442a,0x3f7dabcc,0xbe09cf86,0x3f6b4b0c,0xbec9b953,
0x3f7d3aac,0xbe164083,0x3f676bd8,0xbedae880,0x3f7cbfc9,0xbe22abb6,0x3f633c5a,0xbeebcbbb,0x3f7c3b28,0xbe2f10a2,0x3f5ebe05,0xbefc5d27,0x3f7baccd,0xbe3b6ecf,0x3f59f26a,0xbf064b82,
0x3f800000,0x0,0x3f800000,0x0,0x3f7e70b0,0xbde1bc2e,0x3f7dabcc,0xbe09cf86,0x3f79c79d,0xbe605c13,0x3f76ba07,0xbe888e93,0x3f721352,0xbea68f12,0x3f6b4b0c,0xbec9b953,
0x3f676bd8,0xbedae880,0x3f5b941a,0xbf039c3d,0x3f59f26a,0xbf064b82,0x3f47de65,0xbf1ff6cb,0x3f49d112,0xbf1d7fd1,0x3f3085bb,0xbf396842,0x3f373a23,0xbf32c8c9,0x3f15f6d9,0xbf4f7a1f,
0x3f226799,0xbf45e403,0x3ef15aea,0xbf61c598,0x3f0b9a6b,0xbf5695e5,0x3eb263ef,0xbf6ff573,0x3ee63375,0xbf64aa59,0x3e605c13,0xbf79c79d,0x3eb263ef,0xbf6ff573,0x3dafb680,0xbf7f0e58,
0x3e78cfcc,0xbf7853f8,0xbd48fb30,0xbf7fb10f,0x3e09cf86,0xbf7dabcc,0xbe3b6ecf,0xbf7baccd,0x3cc90ab0,0xbf7fec43,0xbea09ae5,0xbf731447,0xbdafb680,0xbf7f0e58,0xbee0924f,0xbf660f88,
0x3f800000,0x0,0x3f800000,0x0,0x3f7a7302,0xbe541501,0x3f791298,0xbe6c9a7f,0x3f6a09a7,0xbecf7bca,0x3f64aa59,0xbee63375,0x3f4f7a1f,0xbf15f6d9,0x3f43e200,0xbf24d225,
0x3f2beb4a,0xbf3daef9,0x3f187fc0,0xbf4d9f02,0x3f00e7e4,0xbf5d2d53,0x3ec9b953,0xbf6b4b0c,0x3ea09ae5,0xbf731447,0x3e2f10a2,0xbf7c3b28,0x3de1bc2e,0xbf7e70b0,0xbd7b2b74,0xbf7f84ab,
0xbdc8bd36,0xbf7ec46d,0xbe94a031,0xbf74fa0b,0xbe9aa086,0xbf740bdd,0xbf00e7e4,0xbf5d2d53,0xbefc5d27,0xbf5ebe05,0xbf3085bb,0xbf396842,0xbf299415,0xbf3fc767,0xbf5695e5,0xbf0b9a6b,
0xbf4d9f02,0xbf187fc0,0xbf710908,0xbeac7cd4,0xbf68bf3c,0xbed53641,0xbf7e70b0,0xbde1bc2e,0xbf79c79d,0xbe605c13,0xbf7e1324,0x3dfab273,0xbf7ffb11,0xbc490e90,0xbf6ff573,0x3eb263ef,
0x3f800000,0x0,0x3f800000,0x0,0x3f740bdd,0xbe9aa086,0x3f721352,0xbea68f12,0x3f514d3d,0xbf13682a,0x3f49d112,0xbf1d7fd1,0x3f1b02c6,0xbf4bbbf8,0x3f0b9a6b,0xbf5695e5,
0x3eac7cd4,0xbf710908,0x3e78cfcc,0xbf7853f8,0x3d16c32c,0xbf7fd397,0xbdafb680,0xbf7f0e58,0xbe888e93,0xbf76ba07,0xbecf7bca,0xbf6a09a7,0xbf0b9a6b,0xbf5695e5,0xbf2e3bde,0xbf3b8f3b,
0xbf45e403,0xbf226799,0xbf61c598,0xbef15aea,0xbf6db293,0xbebe1d4a,0xbf7cbfc9,0xbe22abb6,0xbf7f4e6d,0xbd96a905,0xbf7c3b28,0x3e2f10a2,0xbf791298,0x3e6c9a7f,0xbf604621,0x3ef6e0cb,
0xbf5b941a,0x3f039c3d,0xbf2beb4a,0x3f3daef9,0xbf299415,0x3f3fc767,0xbec9b953,0x3f6b4b0c,0xbecf7bca,0x3f6a09a7,0xbd96a905,0x3f7f4e6d,0xbde1bc2e,0x3f7e70b0,0x3e827dc0,0x3f778bc5,
0x3f800000,0x0,0x3f800000,0x0,0x3f7f84ab,0xbd7b2b74,0x3f7f0e58,0xbdafb680,0x3f7e1324,0xbdfab273,0x3f7c3b28,0xbe2f10a2,0x3f7baccd,0xbe3b6ecf,0x3f778bc5,0xbe827dc0,
0x3f7853f8,0xbe78cfcc,0x3f710908,0xbeac7cd4,0x3f740bdd,0xbe9aa086,0x3f68bf3c,0xbed53641,0x3f6ed89e,0xbeb8442a,0x3f5ebe05,0xbefc5d27,0x3f68bf3c,0xbed53641,0x3f531849,0xbf10d3cd,
0x3f61c598,0xbef15aea,0x3f45e403,0xbf226799,0x3f59f26a,0xbf064b82,0x3f373a23,0xbf32c8c9,0x3f514d3d,0xbf13682a,0x3f273656,0xbf41d870,0x3f47de65,0xbf1ff6cb,0x3f15f6d9,0xbf4f7a1f,
0x3f3daef9,0xbf2beb4a,0x3f039c3d,0xbf5b941a,0x3f32c8c9,0xbf373a23,0x3ee0924f,0xbf660f88,0x3f273656,0xbf41d870,0x3eb8442a,0xbf6ed89e,0x3f1b02c6,0xbf4bbbf8,0x3e8e9a22,0xbf75dec6,
0x3f800000,0x0,0x3f800000,0x0,0x3f7cbfc9,0xbe22abb6,0x3f7baccd,0xbe3b6ecf,0x3f731447,0xbea09ae5,0x3f6ed89e,0xbeb8442a,0x3f633c5a,0xbeebcbbb,0x3f59f26a,0xbf064b82,
0x3f4d9f02,0xbf187fc0,0x3f3daef9,0xbf2beb4a,0x3f32c8c9,0xbf373a23,0x3f1b02c6,0xbf4bbbf8,0x3f13682a,0xbf514d3d,0x3ee63375,0xbf64aa59,0x3ee0924f,0xbf660f88,0x3e8e9a22,0xbf75dec6,
0x3e94a031,0xbf74fa0b,0x3dc8bd36,0xbf7ec46d,0x3e09cf86,0xbf7dabcc,0xbdafb680,0xbf7f0e58,0xbcc90ab0,0xbf7fec43,0xbe888e93,0xbf76ba07,0xbe3b6ecf,0xbf7baccd,0xbee0924f,0xbf660f88,
0xbeac7cd4,0xbf710908,0xbf187fc0,0xbf4d9f02,0xbef6e0cb,0xbf604621,0xbf3b8f3b,0xbf2e3bde,0xbf1d7fd1,0xbf49d112,0xbf584853,0xbf08f59b,0xbf3b8f3b,0xbf2e3bde,0xbf6db293,0xbebe1d4a,
0x3f800000,0x0,0x3f800000,0x0,0x3f778bc5,0xbe827dc0,0x3f75dec6,0xbe8e9a22,0x3f5ebe05,0xbefc5d27,0x3f584853,0xbf08f59b,0x3f373a23,0xbf32c8c9,0x3f299415,0xbf3fc767,
0x3f039c3d,0xbf5b941a,0x3edae880,0xbf676bd8,0x3e8e9a22,0xbf75dec6,0x3e22abb6,0xbf7cbfc9,0x3cc90ab0,0xbf7fec43,0xbdfab273,0xbf7e1324,0xbe6c9a7f,0xbf791298,0xbec9b953,0xbf6b4b0c,
0xbef15aea,0xbf61c598,0xbf226799,0xbf45e403,0xbf2e3bde,0xbf3b8f3b,0xbf531849,0xbf10d3cd,0xbf584853,0xbf08f59b,0xbf731447,0xbea09ae5,0xbf740bdd,0xbe9aa086,0xbf7fd397,0xbd16c32c,
0xbf7fb10f,0xbd48fb30,0xbf7853f8,0x3e78cfcc,0xbf7a7302,0x3e541501,0xbf5d2d53,0x3f00e7e4,0xbf64aa59,0x3ee63375,0xbf3085bb,0x3f396842,0xbf3fc767,0x3f299415,0xbeebcbbb,0x3f633c5a,
0x3f800000,0x0,0x3f800000,0x0,0x3f6ff573,0xbeb263ef,0x3f6db293,0xbebe1d4a,0x3f41d870,0xbf273656,0x3f396842,0xbf3085bb,0x3ef6e0cb,0xbf604621,0x3ed53641,0xbf68bf3c,
0x3e164083,0xbf7d3aac,0x3d48fb30,0xbf7fb10f,0xbe541501,0xbf7a7302,0xbea68f12,0xbf721352,0xbf08f59b,0xbf584853,0xbf273656,0xbf41d870,0xbf4bbbf8,0xbf1b02c6,0xbf633c5a,0xbeebcbbb,
0xbf74fa0b,0xbe94a031,0xbf7ec46d,0xbdc8bd36,0xbf7f84ab,0x3d7b2b74,0xbf75dec6,0x3e8e9a22,0xbf6a09a7,0x3ecf7bca,0xbf49d112,0x3f1d7fd1,0xbf373a23,0x3f32c8c9,0xbf00e7e4,0x3f5d2d53,
0xbedae880,0x3f676bd8,0xbe164083,0x3f7d3aac,0xbdafb680,0x3f7f0e58,0x3e6c9a7f,0x3f791298,0x3e888e93,0x3f76ba07,0x3f13682a,0x3f514d3d,0x3f15f6d9,0x3f4f7a1f,0x3f5695e5,0x3f0b9a6b,
0x80002c00,0x23c62bc3,0x278b2b0e,0x295f29ed,0x80002c00,0x28c42a6d,0x2ba824a5,0x2b88a564,0x80002c00,0x2add281d,0x2b0ea78b,0x1a48abfe,0x80002c00,0x2bea20b2,0x24a5aba8,0xab3ba6d7,
0x80002c00,0x2bc3a3c6,0xa78bab0e,0xa9ed295f,0x80002c00,0x2a6da8c4,0xaba8a4a5,0x25642b88,0x80002c00,0x281daadd,0xab0e278b,0x2bfe1a48,0x80002c00,0x20b2abea,0xa4a52ba8,0x26d7ab3b,
0x80002c00,0xa3c6abc3,0x278b2b0e,0xa95fa9ed,0x80002c00,0xa8c4aa6d,0x2ba824a5,0xab882564,0x80002c00,0xaadda81d,0x2b0ea78b,0x9a482bfe,0x80002c00,0xabeaa0b2,0x24a5aba8,0x2b3b26d7,
0x80002c00,0xabc323c6,0xa78bab0e,0x29eda95f,0x80002c00,0xaa6d28c4,0xaba8a4a5,0xa564ab88,0x80002c00,0xa81d2add,0xab0e278b,0xabfe9a48,0x80002c00,0xa0b22bea,0xa4a52ba8,0xa6d72b3b,
0x2aa72872,0x2b882564,0x2bf61e46,0x2beaa0b2,0x2872aaa7,0x9a48abfe,0xa913aa2f,0xabc3a3c6,0xaaa7a872,0xab3b26d7,0x9e462bf6,0x2a6d28c4,0xa8722aa7,0x29ed295f,0x2a2fa913,0xa81daadd,
0x2aa72872,0x2564ab88,0xabf69e46,0x20b22bea,0x2872aaa7,0xabfe1a48,0x29132a2f,0x23c6abc3,0xaaa7a872,0x26d72b3b,0x1e46abf6,0xa8c42a6d,0xa8722aa7,0x295fa9ed,0xaa2f2913,0x2adda81d,
0x2aa72872,0xab88a564,0x2bf61e46,0xabea20b2,0x2872aaa7,0x1a482bfe,0xa913aa2f,0x2bc323c6,0xaaa7a872,0x2b3ba6d7,0x9e462bf6,0xaa6da8c4,0xa8722aa7,0xa9eda95f,0x2a2fa913,0x281d2add,
0x2aa72872,0xa5642b88,0xabf69e46,0xa0b2abea,0x2872aaa7,0x2bfe9a48,0x29132a2f,0xa3c62bc3,0xaaa7a872,0xa6d7ab3b,0x1e46abf6,0x28c4aa6d,0xa8722aa7,0xa95f29ed,0xaa2f2913,0xaadd281d,
0x2b64a61f,0x2a6da8c4,0x2913aa2f,0x26d7ab3b,0xab64261f,0xa81d2add,0x1e462bf6,0x295f29ed,0x2b64a61f,0x20b2abea,0xaa2fa913,0xab882564,0xab64261f,0x23c62bc3,0x2bf69e46,0x1a48abfe,
0x2b64a61f,0xa8c4aa6d,0xa9132a2f,0x2b3b26d7,0xab64261f,0x2add281d,0x9e46abf6,0xa9ed295f,0x2b64a61f,0xabeaa0b2,0x2a2f2913,0xa564ab88,0xab64261f,0x2bc3a3c6,0xabf61e46,0x2bfe1a48,
0x2b64a61f,0xaa6d28c4,0x2913aa2f,0xa6d72b3b,0xab64261f,0x281daadd,0x1e462bf6,0xa95fa9ed,0x2b64a61f,0xa0b22bea,0xaa2fa913,0x2b88a564,0xab64261f,0xa3c6abc3,0x2bf69e46,0x9a482bfe,
0x2b64a61f,0x28c42a6d,0xa9132a2f,0xab3ba6d7,0xab64261f,0xaadda81d,0x9e46abf6,0x29eda95f,0x2b64a61f,0x2bea20b2,0x2a2f2913,0x25642b88,0xab64261f,0xabc323c6,0xabf61e46,0xabfe9a48,
0x223eabd9,0x9a48abfe,0xa4a5aba8,0xa81daadd,0x2bd9223e,0x2b3ba6d7,0x278bab0e,0xa0b2abea,0xa23e2bd9,0x29ed295f,0x2ba8a4a5,0x23c6abc3,0xabd9a23e,0xa5642b88,0x2b0e278b,0x28c4aa6d,
0x223eabd9,0xabfe1a48,0x24a52ba8,0x2adda81d,0x2bd9223e,0xa6d7ab3b,0xa78b2b0e,0x2beaa0b2,0xa23e2bd9,0x295fa9ed,0xaba824a5,0x2bc323c6,0xabd9a23e,0x2b882564,0xab0ea78b,0x2a6d28c4,
0x223eabd9,0x1a482bfe,0xa4a5aba8,0x281d2add,0x2bd9223e,0xab3b26d7,0x278bab0e,0x20b22bea,0xa23e2bd9,0xa9eda95f,0x2ba8a4a5,0xa3c62bc3,0xabd9a23e,0x2564ab88,0x2b0e278b,0xa8c42a6d,
0x223eabd9,0x2bfe9a48,0x24a52ba8,0xaadd281d,0x2bd9223e,0x26d72b3b,0xa78b2b0e,0xabea20b2,0xa23e2bd9,0xa95f29ed,0xaba824a5,0xabc3a3c6,0xabd9a23e,0xab88a564,0xab0ea78b,0xaa6da8c4,
0x2c000000,0x2bc3a3c6,0x2b0ea78b,0x29eda95f,0x2c000000,0x2a6da8c4,0x24a5aba8,0xa564ab88,0x2c000000,0x281daadd,0xa78bab0e,0xabfe9a48,0x2c000000,0x20b2abea,0xaba8a4a5,0xa6d72b3b,
0x2c000000,0xa3c6abc3,0xab0e278b,0x295f29ed,0x2c000000,0xa8c4aa6d,0xa4a52ba8,0x2b88a564,0x2c000000,0xaadda81d,0x278b2b0e,0x1a48abfe,0x2c000000,0xabeaa0b2,0x2ba824a5,0xab3ba6d7,
0x2c000000,0xabc323c6,0x2b0ea78b,0xa9ed295f,0x2c000000,0xaa6d28c4,0x24a5aba8,0x25642b88,0x2c000000,0xa81d2add,0xa78bab0e,0x2bfe1a48,0x2c000000,0xa0b22bea,0xaba8a4a5,0x26d7ab3b,
0x2c000000,0x23c62bc3,0xab0e278b,0xa95fa9ed,0x2c000000,0x28c42a6d,0xa4a52ba8,0xab882564,0x2c000000,0x2add281d,0x278b2b0e,0x9a482bfe,0x2c000000,0x2bea20b2,0x2ba824a5,0x2b3b26d7,
0x2872aaa7,0x2564ab88,0x1e46abf6,0xa0b2abea,0xaaa7a872,0xabfe1a48,0xaa2f2913,0xa3c62bc3,0xa8722aa7,0x26d72b3b,0x2bf61e46,0x28c4aa6d,0x2aa72872,0x295fa9ed,0xa913aa2f,0xaadd281d,
0x2872aaa7,0xab88a564,0x9e462bf6,0x2beaa0b2,0xaaa7a872,0x1a482bfe,0x2a2fa913,0xabc3a3c6,0xa8722aa7,0x2b3ba6d7,0xabf69e46,0x2a6d28c4,0x2aa72872,0xa9eda95f,0x29132a2f,0xa81daadd,
0x2872aaa7,0xa5642b88,0x1e46abf6,0x20b22bea,0xaaa7a872,0x2bfe9a48,0xaa2f2913,0x23c6abc3,0xa8722aa7,0xa6d7ab3b,0x2bf61e46,0xa8c42a6d,0x2aa72872,0xa95f29ed,0xa913aa2f,0x2adda81d,
0x2872aaa7,0x2b882564,0x9e462bf6,0xabea20b2,0xaaa7a872,0x9a48abfe,0x2a2fa913,0x2bc323c6,0xa8722aa7,0xab3b26d7,0xabf69e46,0xaa6da8c4,0x2aa72872,0x29ed295f,0x29132a2f,0x281d2add,
0xa61fab64,0xa8c4aa6d,0xaa2fa913,0xab3ba6d7,0x261f2b64,0x2add281d,0x2bf69e46,0x29eda95f,0xa61fab64,0xabeaa0b2,0xa9132a2f,0x25642b88,0x261f2b64,0x2bc3a3c6,0x9e46abf6,0xabfe9a48,
0xa61fab64,0xaa6d28c4,0x2a2f2913,0x26d7ab3b,0x261f2b64,0x281daadd,0xabf61e46,0x295f29ed,0xa61fab64,0xa0b22bea,0x2913aa2f,0xab882564,0x261f2b64,0xa3c6abc3,0x1e462bf6,0x1a48abfe,
0xa61fab64,0x28c42a6d,0xaa2fa913,0x2b3b26d7,0x261f2b64,0xaadda81d,0x2bf69e46,0xa9ed295f,0xa61fab64,0x2bea20b2,0xa9132a2f,0xa564ab88,0x261f2b64,0xabc323c6,0x9e46abf6,0x2bfe1a48,
0xa61fab64,0x2a6da8c4,0x2a2f2913,0xa6d72b3b,0x261f2b64,0xa81d2add,0xabf61e46,0xa95fa9ed,0xa61fab64,0x20b2abea,0x2913aa2f,0x2b88a564,0x261f2b64,0x23c62bc3,0x1e462bf6,0x9a482bfe,
0xabd9a23e,0xabfe1a48,0xaba824a5,0xaadd281d,0x223eabd9,0xa6d7ab3b,0xab0ea78b,0xabea20b2,0x2bd9223e,0x295fa9ed,0xa4a5aba8,0xabc3a3c6,0xa23e2bd9,0x2b882564,0x278bab0e,0xaa6da8c4,
0xabd9a23e,0x1a482bfe,0x2ba8a4a5,0xa81daadd,0x223eabd9,0xab3b26d7,0x2b0e278b,0xa0b2abea,0x2bd9223e,0xa9eda95f,0x24a52ba8,0x23c6abc3,0xa23e2bd9,0x2564ab88,0xa78b2b0e,0x28c4aa6d,
0xabd9a23e,0x2bfe9a48,0xaba824a5,0x2adda81d,0x223eabd9,0x26d72b3b,0xab0ea78b,0x2beaa0b2,0x2bd9223e,0xa95f29ed,0xa4a5aba8,0x2bc323c6,0xa23e2bd9,0xab88a564,0x278bab0e,0x2a6d28c4,
0xabd9a23e,0x9a48abfe,0x2ba8a4a5,0x281d2add,0x223eabd9,0x2b3ba6d7,0x2b0e278b,0x20b22bea,0x2bd9223e,0x29ed295f,0x24a52ba8,0xa3c62bc3,0xa23e2bd9,0xa5642b88,0xa78b2b0e,0xa8c42a6d,
0x80002c00,0x80002c00,0x80002c00,0x80002c00,0x23c62bc3,0x28c42a6d,0x2add281d,0x2bea20b2,0x278b2b0e,0x2ba824a5,0x2b0ea78b,0x24a5aba8,0x295f29ed,0x2b88a564,0x1a48abfe,0xab3ba6d7,
0x2aa72872,0x2872aaa7,0xaaa7a872,0xa8722aa7,0x2b882564,0x9a48abfe,0xab3b26d7,0x29ed295f,0x2bf61e46,0xa913aa2f,0x9e462bf6,0x2a2fa913,0x2beaa0b2,0xabc3a3c6,0x2a6d28c4,0xa81daadd,
0x2b64a61f,0xab64261f,0x2b64a61f,0xab64261f,0x2a6da8c4,0xa81d2add,0x20b2abea,0x23c62bc3,0x2913aa2f,0x1e462bf6,0xaa2fa913,0x2bf69e46,0x26d7ab3b,0x295f29ed,0xab882564,0x1a48abfe,
0x223eabd9,0x2bd9223e,0xa23e2bd9,0xabd9a23e,0x9a48abfe,0x2b3ba6d7,0x29ed295f,0xa5642b88,0xa4a5aba8,0x278bab0e,0x2ba8a4a5,0x2b0e278b,0xa81daadd,0xa0b2abea,0x23c6abc3,0x28c4aa6d,
0x80002c00,0x80002c00,0x80002c00,0x80002c00,0x2bc3a3c6,0x2a6da8c4,0x281daadd,0x20b2abea,0xa78bab0e,0xaba8a4a5,0xab0e278b,0xa4a52ba8,0xa9ed295f,0x25642b88,0x2bfe1a48,0x26d7ab3b,
0x2aa72872,0x2872aaa7,0xaaa7a872,0xa8722aa7,0x2564ab88,0xabfe1a48,0x26d72b3b,0x295fa9ed,0xabf69e46,0x29132a2f,0x1e46abf6,0xaa2f2913,0x20b22bea,0x23c6abc3,0xa8c42a6d,0x2adda81d,
0x2b64a61f,0xab64261f,0x2b64a61f,0xab64261f,0xa8c4aa6d,0x2add281d,0xabeaa0b2,0x2bc3a3c6,0xa9132a2f,0x9e46abf6,0x2a2f2913,0xabf61e46,0x2b3b26d7,0xa9ed295f,0xa564ab88,0x2bfe1a48,
0x223eabd9,0x2bd9223e,0xa23e2bd9,0xabd9a23e,0xabfe1a48,0xa6d7ab3b,0x295fa9ed,0x2b882564,0x24a52ba8,0xa78b2b0e,0xaba824a5,0xab0ea78b,0x2adda81d,0x2beaa0b2,0x2bc323c6,0x2a6d28c4,
0x80002c00,0x80002c00,0x80002c00,0x80002c00,0xa3c6abc3,0xa8c4aa6d,0xaadda81d,0xabeaa0b2,0x278b2b0e,0x2ba824a5,0x2b0ea78b,0x24a5aba8,0xa95fa9ed,0xab882564,0x9a482bfe,0x2b3b26d7,
0x2aa72872,0x2872aaa7,0xaaa7a872,0xa8722aa7,0xab88a564,0x1a482bfe,0x2b3ba6d7,0xa9eda95f,0x2bf61e46,0xa913aa2f,0x9e462bf6,0x2a2fa913,0xabea20b2,0x2bc323c6,0xaa6da8c4,0x281d2add,
0x2b64a61f,0xab64261f,0x2b64a61f,0xab64261f,0xaa6d28c4,0x281daadd,0xa0b22bea,0xa3c6abc3,0x2913aa2f,0x1e462bf6,0xaa2fa913,0x2bf69e46,0xa6d72b3b,0xa95fa9ed,0x2b88a564,0x9a482bfe,
0x223eabd9,0x2bd9223e,0xa23e2bd9,0xabd9a23e,0x1a482bfe,0xab3b26d7,0xa9eda95f,0x2564ab88,0xa4a5aba8,0x278bab0e,0x2ba8a4a5,0x2b0e278b,0x281d2add,0x20b22bea,0xa3c62bc3,0xa8c42a6d,
0x80002c00,0x80002c00,0x80002c00,0x80002c00,0xabc323c6,0xaa6d28c4,0xa81d2add,0xa0b22bea,0xa78bab0e,0xaba8a4a5,0xab0e278b,0xa4a52ba8,0x29eda95f,0xa564ab88,0xabfe9a48,0xa6d72b3b,
0x2aa72872,0x2872aaa7,0xaaa7a872,0xa8722aa7,0xa5642b88,0x2bfe9a48,0xa6d7ab3b,0xa95f29ed,0xabf69e46,0x29132a2f,0x1e46abf6,0xaa2f2913,0xa0b2abea,0xa3c62bc3,0x28c4aa6d,0xaadd281d,
0x2b64a61f,0xab64261f,0x2b64a61f,0xab64261f,0x28c42a6d,0xaadda81d,0x2bea20b2,0xabc323c6,0xa9132a2f,0x9e46abf6,0x2a2f2913,0xabf61e46,0xab3ba6d7,0x29eda95f,0x25642b88,0xabfe9a48,
0x223eabd9,0x2bd9223e,0xa23e2bd9,0xabd9a23e,0x2bfe9a48,0x26d72b3b,0xa95f29ed,0xab88a564,0x24a52ba8,0xa78b2b0e,0xaba824a5,0xab0ea78b,0xaadd281d,0xabea20b2,0xabc3a3c6,0xaa6da8c4,
0x2c000000,0x2c000000,0x2c000000,0x2c000000,0x2bc3a3c6,0x2a6da8c4,0x281daadd,0x20b2abea,0x2b0ea78b,0x24a5aba8,0xa78bab0e,0xaba8a4a5,0x29eda95f,0xa564ab88,0xabfe9a48,0xa6d72b3b,
0x2872aaa7,0xaaa7a872,0xa8722aa7,0x2aa72872,0x2564ab88,0xabfe1a48,0x26d72b3b,0x295fa9ed,0x1e46abf6,0xaa2f2913,0x2bf61e46,0xa913aa2f,0xa0b2abea,0xa3c62bc3,0x28c4aa6d,0xaadd281d,
0xa61fab64,0x261f2b64,0xa61fab64,0x261f2b64,0xa8c4aa6d,0x2add281d,0xabeaa0b2,0x2bc3a3c6,0xaa2fa913,0x2bf69e46,0xa9132a2f,0x9e46abf6,0xab3ba6d7,0x29eda95f,0x25642b88,0xabfe9a48,
0xabd9a23e,0x223eabd9,0x2bd9223e,0xa23e2bd9,0xabfe1a48,0xa6d7ab3b,0x295fa9ed,0x2b882564,0xaba824a5,0xab0ea78b,0xa4a5aba8,0x278bab0e,0xaadd281d,0xabea20b2,0xabc3a3c6,0xaa6da8c4,
0x2c000000,0x2c000000,0x2c000000,0x2c000000,0xa3c6abc3,0xa8c4aa6d,0xaadda81d,0xabeaa0b2,0xab0e278b,0xa4a52ba8,0x278b2b0e,0x2ba824a5,0x295f29ed,0x2b88a564,0x1a48abfe,0xab3ba6d7,
0x2872aaa7,0xaaa7a872,0xa8722aa7,0x2aa72872,0xab88a564,0x1a482bfe,0x2b3ba6d7,0xa9eda95f,0x9e462bf6,0x2a2fa913,0xabf69e46,0x29132a2f,0x2beaa0b2,0xabc3a3c6,0x2a6d28c4,0xa81daadd,
0xa61fab64,0x261f2b64,0xa61fab64,0x261f2b64,0xaa6d28c4,0x281daadd,0xa0b22bea,0xa3c6abc3,0x2a2f2913,0xabf61e46,0x2913aa2f,0x1e462bf6,0x26d7ab3b,0x295f29ed,0xab882564,0x1a48abfe,
0xabd9a23e,0x223eabd9,0x2bd9223e,0xa23e2bd9,0x1a482bfe,0xab3b26d7,0xa9eda95f,0x2564ab88,0x2ba8a4a5,0x2b0e278b,0x24a52ba8,0xa78b2b0e,0xa81daadd,0xa0b2abea,0x23c6abc3,0x28c4aa6d,
0x2c000000,0x2c000000,0x2c000000,0x2c000000,0xabc323c6,0xaa6d28c4,0xa81d2add,0xa0b22bea,0x2b0ea78b,0x24a5aba8,0xa78bab0e,0xaba8a4a5,0xa9ed295f,0x25642b88,0x2bfe1a48,0x26d7ab3b,
0x2872aaa7,0xaaa7a872,0xa8722aa7,0x2aa72872,0xa5642b88,0x2bfe9a48,0xa6d7ab3b,0xa95f29ed,0x1e46abf6,0xaa2f2913,0x2bf61e46,0xa913aa2f,0x20b22bea,0x23c6abc3,0xa8c42a6d,0x2adda81d,
0xa61fab64,0x261f2b64,0xa61fab64,0x261f2b64,0x28c42a6d,0xaadda81d,0x2bea20b2,0xabc323c6,0xaa2fa913,0x2bf69e46,0xa9132a2f,0x9e46abf6,0x2b3b26d7,0xa9ed295f,0xa564ab88,0x2bfe1a48,
0xabd9a23e,0x223eabd9,0x2bd9223e,0xa23e2bd9,0x2bfe9a48,0x26d72b3b,0xa95f29ed,0xab88a564,0xaba824a5,0xab0ea78b,0xa4a5aba8,0x278bab0e,0x2adda81d,0x2beaa0b2,0x2bc323c6,0x2a6d28c4,
0x2c000000,0x2c000000,0x2c000000,0x2c000000,0x23c62bc3,0x28c42a6d,0x2add281d,0x2bea20b2,0xab0e278b,0xa4a52ba8,0x278b2b0e,0x2ba824a5,0xa95fa9ed,0xab882564,0x9a482bfe,0x2b3b26d7,
0x2872aaa7,0xaaa7a872,0xa8722aa7,0x2aa72872,0x2b882564,0x9a48abfe,0xab3b26d7,0x29ed295f,0x9e462bf6,0x2a2fa913,0xabf69e46,0x29132a2f,0xabea20b2,0x2bc323c6,0xaa6da8c4,0x281d2add,
0xa61fab64,0x261f2b64,0xa61fab64,0x261f2b64,0x2a6da8c4,0xa81d2add,0x20b2abea,0x23c62bc3,0x2a2f2913,0xabf61e46,0x2913aa2f,0x1e462bf6,0xa6d72b3b,0xa95fa9ed,0x2b88a564,0x9a482bfe,
0xabd9a23e,0x223eabd9,0x2bd9223e,0xa23e2bd9,0x9a48abfe,0x2b3ba6d7,0x29ed295f,0xa5642b88,0x2ba8a4a5,0x2b0e278b,0x24a52ba8,0xa78b2b0e,0x281d2add,0x20b22bea,0xa3c62bc3,0xa8c42a6d,
0x80002c00,0x80002c00,0x80002c00,0x80002c00,0x80002c00,0x261f2b64,0x29a829a8,0x2b64261f,0x80002c00,0x29a829a8,0x2c000000,0x29a8a9a8,0x80002c00,0x2b64261f,0x29a8a9a8,0xa61fab64,
0x80002c00,0x2c000000,0x8000ac00,0xac000000,0x80002c00,0x2b64a61f,0xa9a8a9a8,0xa61f2b64,0x80002c00,0x29a8a9a8,0xac000000,0x29a829a8,0x80002c00,0x261fab64,0xa9a829a8,0x2b64a61f,
0x80002c00,0x8000ac00,0x80002c00,0x8000ac00,0x80002c00,0xa61fab64,0x29a829a8,0xab64a61f,0x80002c00,0xa9a8a9a8,0x2c000000,0xa9a829a8,0x80002c00,0xab64a61f,0x29a8a9a8,0x261f2b64,
0x80002c00,0xac000000,0x8000ac00,0x2c000000,0x80002c00,0xab64261f,0xa9a8a9a8,0x261fab64,0x80002c00,0xa9a829a8,0xac000000,0xa9a8a9a8,0x80002c00,0xa61f2b64,0xa9a829a8,0xab64261f,
0x80002c00,0x80002c00,0x80002c00,0x80002c00,0x2c000000,0x2b64a61f,0x29a8a9a8,0x261fab64,0x8000ac00,0xa9a8a9a8,0xac000000,0xa9a829a8,0xac000000,0xa61f2b64,0x29a829a8,0x2b64a61f,
0x80002c00,0x2c000000,0x8000ac00,0xac000000,0x2c000000,0xa61fab64,0xa9a829a8,0x2b64261f,0x8000ac00,0xa9a829a8,0x2c000000,0xa9a8a9a8,0xac000000,0x2b64261f,0xa9a8a9a8,0x261f2b64,
0x80002c00,0x8000ac00,0x80002c00,0x8000ac00,0x2c000000,0xab64261f,0x29a8a9a8,0xa61f2b64,0x8000ac00,0x29a829a8,0xac000000,0x29a8a9a8,0xac000000,0x261fab64,0x29a829a8,0xab64261f,
0x80002c00,0xac000000,0x8000ac00,0x2c000000,0x2c000000,0x261f2b64,0xa9a829a8,0xab64a61f,0x8000ac00,0x29a8a9a8,0x2c000000,0x29a829a8,0xac000000,0xab64a61f,0xa9a8a9a8,0xa61fab64,
0x80002c00,0x80002c00,0x80002c00,0x80002c00,0x8000ac00,0xa61fab64,0xa9a8a9a8,0xab64a61f,0x80002c00,0x29a829a8,0x2c000000,0x29a8a9a8,0x8000ac00,0xab64a61f,0xa9a829a8,0x261f2b64,
0x80002c00,0x2c000000,0x8000ac00,0xac000000,0x8000ac00,0xab64261f,0x29a829a8,0x261fab64,0x80002c00,0x29a8a9a8,0xac000000,0x29a829a8,0x8000ac00,0xa61f2b64,0x29a8a9a8,0xab64261f,
0x80002c00,0x8000ac00,0x80002c00,0x8000ac00,0x8000ac00,0x261f2b64,0xa9a8a9a8,0x2b64261f,0x80002c00,0xa9a8a9a8,0x2c000000,0xa9a829a8,0x8000ac00,0x2b64261f,0xa9a829a8,0xa61fab64,
0x80002c00,0xac000000,0x8000ac00,0x2c000000,0x8000ac00,0x2b64a61f,0x29a829a8,0xa61f2b64,0x80002c00,0xa9a829a8,0xac000000,0xa9a8a9a8,0x8000ac00,0x261fab64,0x29a8a9a8,0x2b64a61f,
0x80002c00,0x80002c00,0x80002c00,0x80002c00,0xac000000,0xab64261f,0xa9a829a8,0xa61f2b64,0x8000ac00,0xa9a8a9a8,0xac000000,0xa9a829a8,0x2c000000,0x261fab64,0xa9a8a9a8,0xab64261f,
0x80002c00,0x2c000000,0x8000ac00,0xac000000,0xac000000,0x261f2b64,0x29a8a9a8,0xab64a61f,0x8000ac00,0xa9a829a8,0x2c000000,0xa9a8a9a8,0x2c000000,0xab64a61f,0x29a829a8,0xa61fab64,
0x80002c00,0x8000ac00,0x80002c00,0x8000ac00,0xac000000,0x2b64a61f,0xa9a829a8,0x261fab64,0x8000ac00,0x29a829a8,0xac000000,0x29a8a9a8,0x2c000000,0xa61f2b64,0xa9a8a9a8,0x2b64a61f,
0x80002c00,0xac000000,0x8000ac00,0x2c000000,0xac000000,0xa61fab64,0x29a8a9a8,0x2b64261f,0x8000ac00,0x29a8a9a8,0x2c000000,0x29a829a8,0x2c000000,0x2b64261f,0x29a829a8,0x261f2b64,
0x2c000000,0x2c000000,0x2c000000,0x2c000000,0x2c000000,0x2b64a61f,0x29a8a9a8,0x261fab64,0x2c000000,0x29a8a9a8,0xac00,0xa9a8a9a8,0x2c000000,0x261fab64,0xa9a8a9a8,0xab64261f,
0x2c000000,0xac00,0xac000000,0x2c00,0x2c000000,0xa61fab64,0xa9a829a8,0x2b64261f,0x2c000000,0xa9a8a9a8,0x2c00,0x29a8a9a8,0x2c000000,0xab64a61f,0x29a829a8,0xa61fab64,
0x2c000000,0xac000000,0x2c000000,0xac000000,0x2c000000,0xab64261f,0x29a8a9a8,0xa61f2b64,0x2c000000,0xa9a829a8,0xac00,0x29a829a8,0x2c000000,0xa61f2b64,0xa9a8a9a8,0x2b64a61f,
0x2c000000,0x2c00,0xac000000,0xac00,0x2c000000,0x261f2b64,0xa9a829a8,0xab64a61f,0x2c000000,0x29a829a8,0x2c00,0xa9a829a8,0x2c000000,0x2b64261f,0x29a829a8,0x261f2b64,
0x2c000000,0x2c000000,0x2c000000,0x2c000000,0xac00,0xa61fab64,0xa9a8a9a8,0xab64a61f,0xac000000,0xa9a829a8,0x2c00,0x29a829a8,0x2c00,0x2b64261f,0x29a8a9a8,0xa61fab64,
0x2c000000,0xac00,0xac000000,0x2c00,0xac00,0xab64261f,0x29a829a8,0x261fab64,0xac000000,0x29a829a8,0xac00,0xa9a829a8,0x2c00,0x261fab64,0xa9a829a8,0x2b64a61f,
0x2c000000,0xac000000,0x2c000000,0xac000000,0xac00,0x261f2b64,0xa9a8a9a8,0x2b64261f,0xac000000,0x29a8a9a8,0x2c00,0xa9a8a9a8,0x2c00,0xab64a61f,0x29a8a9a8,0x261f2b64,
0x2c000000,0x2c00,0xac000000,0xac00,0xac00,0x2b64a61f,0x29a829a8,0xa61f2b64,0xac000000,0xa9a8a9a8,0xac00,0x29a8a9a8,0x2c00,0xa61f2b64,0xa9a829a8,0xab64261f,
0x2c000000,0x2c000000,0x2c000000,0x2c000000,0xac000000,0xab64261f,0xa9a829a8,0xa61f2b64,0x2c000000,0x29a8a9a8,0xac00,0xa9a8a9a8,0xac000000,0xa61f2b64,0x29a829a8,0x2b64a61f,
0x2c000000,0xac00,0xac000000,0x2c00,0xac000000,0x261f2b64,0x29a8a9a8,0xab64a61f,0x2c000000,0xa9a8a9a8,0x2c00,0x29a8a9a8,0xac000000,0x2b64261f,0xa9a8a9a8,0x261f2b64,
0x2c000000,0xac000000,0x2c000000,0xac000000,0xac000000,0x2b64a61f,0xa9a829a8,0x261fab64,0x2c000000,0xa9a829a8,0xac00,0x29a829a8,0xac000000,0x261fab64,0x29a829a8,0xab64261f,
0x2c000000,0x2c00,0xac000000,0xac00,0xac000000,0xa61fab64,0x29a8a9a8,0x2b64261f,0x2c000000,0x29a829a8,0x2c00,0xa9a829a8,0xac000000,0xab64a61f,0xa9a8a9a8,0xa61fab64,
0x2c000000,0x2c000000,0x2c000000,0x2c000000,0x2c00,0x261f2b64,0x29a829a8,0x2b64261f,0xac000000,0xa9a829a8,0x2c00,0x29a829a8,0xac00,0xab64a61f,0xa9a829a8,0x261f2b64,
0x2c000000,0xac00,0xac000000,0x2c00,0x2c00,0x2b64a61f,0xa9a8a9a8,0xa61f2b64,0xac000000,0x29a829a8,0xac00,0xa9a829a8,0xac00,0xa61f2b64,0x29a8a9a8,0xab64261f,
0x2c000000,0xac000000,0x2c000000,0xac000000,0x2c00,0xa61fab64,0x29a829a8,0xab64a61f,0xac000000,0x29a8a9a8,0x2c00,0xa9a8a9a8,0xac00,0x2b64261f,0xa9a829a8,0xa61fab64,
0x2c000000,0x2c00,0xac000000,0xac00,0x2c00,0xab64261f,0xa9a8a9a8,0x261fab64,0xac000000,0xa9a8a9a8,0xac00,0x29a8a9a8,0xac00,0x261fab64,0x29a8a9a8,0x2b64a61f,
0x3f800000,0x0,0x3f7ff84a,0xbc7b514b,0x3f800000,0x0,0x3f7fcbe2,0xbd2351cb,0x3f800000,0x0,0x3f7f7808,0xbd83db0a,0x3f800000,0x0,0x3f7efcc9,0xbdb5f8da,
0x3f800000,0x0,0x3f7e5a38,0xbde7fa9a,0x3f800000,0x0,0x3f7d906e,0xbe0cec4a,0x3f800000,0x0,0x3f7c9f8a,0xbe25c58c,0x3f800000,0x0,0x3f7b87b2,0xbe3e853e,
0x3f800000,0x0,0x3f7a4910,0xbe57278f,0x3f800000,0x0,0x3f78e3d6,0xbe6fa8b2,0x3f800000,0x0,0x3f77583a,0xbe840270,0x3f800000,0x0,0x3f75a67b,0xbe901c2c,
0x3f800000,0x0,0x3f73ced9,0xbe9c1faf,0x3f800000,0x0,0x3f71d19f,0xbea80b1f,0x3f800000,0x0,0x3f6faf1b,0xbeb3dca5,0x3f800000,0x0,0x3f6d67a1,0xbebf926f,
0x3f7f84ab,0xbd7b2b74,0x3f7f3f54,0xbd9cecf9,0x3f7cbfc9,0xbe22abb6,0x3f7aed37,0xbe4ada4f,0x3f778bc5,0xbe827dc0,0x3f72d4eb,0xbea21887,0x3f6ff573,0xbeb263ef,0x3f671599,0xbedc53c1,
0x3f660f88,0xbee0924f,0x3f57dc7f,0xbf099f4e,0x3f59f26a,0xbf064b82,0x3f456439,0xbf2302d3,0x3f4bbbf8,0xbf1b02c6,0x3f2ff3e6,0xbf39f2ac,0x3f3b8f3b,0xbf2e3bde,0x3f17de12,0xbf4e1689,
0x3f299415,0xbf3fc767,0x3efafef7,0xbf5f20db,0x3f15f6d9,0xbf4f7a1f,0x3ec27b55,0xbf6cd007,0x3f00e7e4,0xbf5d2d53,0x3e870ada,0xbf76ef5b,0x3ed53641,0xbf68bf3c,0x3e1324ca,0xbf7d57de,
0x3ea68f12,0xbf721352,0x3cafea69,0xbf7ff0e3,0x3e6c9a7f,0xbf791298,0xbdcefdb7,0xbf7eb069,0x3e09cf86,0xbf7dabcc,0xbe636c83,0xbf799b43,0x3d16c32c,0xbf7fd397,0xbeadf73c,0xbf70c501,
0x3f7e1324,0xbdfab273,0x3f7d906e,0xbe0cec4a,0x3f731447,0xbea09ae5,0x3f6faf1b,0xbeb3dca5,0x3f5ebe05,0xbefc5d27,0x3f5627fe,0xbf0c42c9,0x3f41d870,0xbf273656,0x3f3238aa,0xbf37c655,
0x3f1d7fd1,0xbf49d112,0x3f05a02c,0xbf5a5ba0,0x3ee63375,0xbf64aa59,0x3ea5129f,0xbf725470,0x3e888e93,0xbf76ba07,0x3ddb7d37,0xbf7e868b,0x3d96a905,0xbf7f4e6d,0xbde7fa9a,0xbf7e5a38,
0xbdfab273,0xbf7e1324,0xbea80b1f,0xbf71d19f,0xbea09ae5,0xbf731447,0xbf06f686,0xbf5988ad,0xbefc5d27,0xbf5ebe05,0xbf33587a,0xbf36ad7f,0xbf273656,0xbf41d870,0xbf570348,0xbf0af1b7,
0xbf49d112,0xbf1d7fd1,0xbf703b37,0xbeb0eacb,0xbf64aa59,0xbee63375,0xbf7dc68c,0xbe06b26e,0xbf76ba07,0xbe888e93,0xbf7efcc9,0x3db5f8da,0xbf7f4e6d,0xbd96a905,0xbf73ced9,0x3e9c1faf,
0x3f7baccd,0xbe3b6ecf,0x3f7aed37,0xbe4ada4f,0x3f633c5a,0xbeebcbbb,0x3f5e5aa6,0xbefdbabb,0x3f373a23,0xbf32c8c9,0x3f2b561b,0xbf3e35c5,0x3ef6e0cb,0xbf604621,0x3ece0bea,0xbf6a5ad9,
0x3e541501,0xbf7a7302,0x3dc27c39,0xbf7ed7d4,0xbdafb680,0xbf7f0e58,0xbe636c83,0xbf799b43,0xbebe1d4a,0xbf6db293,0xbf044889,0xbf5b2c79,0xbf1ff6cb,0xbf47de65,0xbf425b89,0xbf269de3,
0xbf531849,0xbf10d3cd,0xbf6cd007,0xbec27b55,0xbf740bdd,0xbe9aa086,0xbf7f5ce9,0xbd9064b4,0xbf7ffb11,0xbc490e90,0xbf7822d1,0x3e7bdba4,0xbf75dec6,0x3e8e9a22,0xbf57dc7f,0x3f099f4e,
0xbf5695e5,0x3f0b9a6b,0xbf21cbfb,0x3f466354,0xbf24d225,0x3f43e200,0xbeb6ccc3,0x3f6f20b0,0xbec9b953,0x3f6b4b0c,0xbd3c6dd5,0x3f7fba9e,0xbde1bc2e,0x3f7e70b0,0x3e8a11f7,0x3f76841b,
0x3f7fe129,0xbcfb49ba,0x3f7fba9e,0xbd3c6dd5,0x3f7f2f9d,0xbda3308c,0x3f7e2b72,0xbdf475c0,0x3f7de0b1,0xbe039502,0x3f7b3bab,0xbe44b0b9,0x3f7bf531,0xbe354098,0x3f76ef5b,0xbe870ada,
0x3f796e4e,0xbe667c66,0x3f714c7a,0xbeab0201,0x3f764d97,0xbe8b9507,0x3f6a5ad9,0xbece0bea,0x3f7294f8,0xbea395c5,0x3f62241a,0xbeeff7fb,0x3f6e46be,0xbebb31a0,0x3f58b3a1,0xbf084b92,
0x3f696591,0xbed25a09,0x3f4e1689,0xbf17de12,0x3f63f473,0xbee900b7,0x3f425b89,0xbf269de3,0x3f5df6be,0xbeff17b2,0x3f3592e7,0xbf34768f,0x3f577026,0xbf0a48ad,0x3f27ce61,0xbf4154e1,
0x3f5064af,0xbf14b039,0x3f19210f,0xbf4d26fd,0x3f48d8b3,0xbf1ebc12,0x3f099f4e,0xbf57dc7f,0x3f40d0da,0xbf286605,0x3ef2bd43,0xbf61668a,0x3f385216,0xbf31a81d,0x3ed0eb2a,0xbf69b7e4,
0x3f7eea9d,0xbdbc3ac3,0x3f7e868b,0xbddb7d37,0x3f78b47b,0xbe72b651,0x3f76167a,0xbe8d17c0,0x3f6d1c1d,0xbec1071e,0x3f666771,0xbedf28a9,0x3f5c61c7,0xbf0242b1,0x3f4fefa8,0xbf1553b7,
0x3f46e22a,0xbf212ff9,0x3f33587a,0xbf36ad7f,0x3f2d1469,0xbf3ca003,0x3f11796b,0xbf52a649,0x3f0f8784,0xbf53fac3,0x3ed6a399,0xbf686b39,0x3eddbe79,0xbf66becc,0x3e840270,0xbf77583a,
0x3e97a117,0xbf748422,0x3db5f8da,0xbf7efcc9,0x3e1c76de,0xbf7cfe73,0xbda973ba,0xbf7f1f49,0x3bc90f88,0xbf7ffec4,0xbe80f8c0,0xbf77beb7,0xbe1008b7,0xbf7d7474,0xbed3c867,0xbf6912ae,
0xbe919ddd,0xbf756d97,0xbf102dd5,0xbf5389c7,0xbed8106b,0xbf6816a8,0xbf3238aa,0xbf37c655,0xbf0cead0,0xbf55b993,0xbf4f0417,0xbf16999f,0xbf2ac082,0xbf3ebc1b,0xbf65b710,0xbee1fb6a,
0x3f7cfe73,0xbe1c76de,0x3f7c5d3a,0xbe2bf804,0x3f6be858,0xbec6d529,0x3f67c187,0xbed97cb9,0x3f4cae79,0xbf19c200,0x3f425b89,0xbf269de3,0x3f212ff9,0xbf46e22a,0x3f0ee0db,0xbf546b3b,
0x3ed8106b,0xbf6816a8,0x3ea21887,0xbf72d4eb,0x3e419b37,0xbf7b61fc,0x3d55880e,0xbf7fa6e3,0xbd621469,0xbf7f9c18,0xbe5d4b1a,0xbf79f35e,0xbe97a117,0xbf748422,0xbeeff7fb,0xbf62241a,
0xbf04f484,0xbf5ac450,0xbf2ff3e6,0xbf39f2ac,0xbf36206c,0xbf33e7bc,0xbf5b2c79,0xbf044889,0xbf5c61c7,0xbf0242b1,0xbf76841b,0xbe8a11f7,0xbf756d97,0xbe919ddd,0xbf7fffb1,0xbb490fc6,
0xbf7fc38f,0xbd2fe007,0xbf76ef5b,0x3e870ada,0xbf7ac516,0x3e4dee60,0xbf5bfb34,0x3f02ef9f,0xbf66becc,0x3eddbe79,0xbf311722,0x3f38dd65,0xbf44e3f5,0x3f239da9,0xbef2bd43,0x3f61668a,
0x3f7a1e84,0xbe5a3997,0x3f7940c0,0xbe698bba,0x3f591e6a,0xbf07a136,0x3f5389c7,0xbf102dd5,0x3f1ebc12,0xbf48d8b3,0x3f11796b,0xbf52a649,0x3ea395c5,0xbf7294f8,0x3e6fa8b2,0xbf78e3d6,
0xbc96c9b6,0xbf7ff4e6,0xbe06b26e,0xbf7dc68c,0xbeb554ec,0xbf6f6830,0xbef58035,0xbf60a6cf,0xbf26050a,0xbf42de29,0xbf436051,0xbf256bcb,0xbf5df6be,0xbeff17b2,0xbf71d19f,0xbea80b1f,
0xbf7bf531,0xbe354098,0xbf7fdaaf,0x3d0a342f,0xbf7c7eb0,0x3e28defc,0xbf6b99fb,0x3ec8477c,0xbf5f8327,0x3ef9a02d,0xbf37c655,0x3f3238aa,0xbf286605,0x3f40d0da,0xbed6a399,0x3f686b39,
0xbebb31a0,0x3f6e46be,0xbd83db0a,0x3f7f7808,0xbcfb49ba,0x3f7fe129,0x3e9920fe,0x3f74484b,0x3e9d9e78,0x3f73913f,0x3f1f599f,0x3f485bca,0x3f1c420c,0x3f4ac77f,0x3f5cc7d1,0x3f019573,
0x3f800000,0x0,0x3f800000,0x0,0x3f7ff84a,0xbc7b514b,0x3f7fcbe2,0xbd2351cb,0x3f7fe129,0xbcfb49ba,0x3f7f2f9d,0xbda3308c,0x3f7fba9e,0xbd3c6dd5,0x3f7e2b72,0xbdf475c0,
0x3f7f84ab,0xbd7b2b74,0x3f7cbfc9,0xbe22abb6,0x3f7f3f54,0xbd9cecf9,0x3f7aed37,0xbe4ada4f,0x3f7eea9d,0xbdbc3ac3,0x3f78b47b,0xbe72b651,0x3f7e868b,0xbddb7d37,0x3f76167a,0xbe8d17c0,
0x3f7e1324,0xbdfab273,0x3f731447,0xbea09ae5,0x3f7d906e,0xbe0cec4a,0x3f6faf1b,0xbeb3dca5,0x3f7cfe73,0xbe1c76de,0x3f6be858,0xbec6d529,0x3f7c5d3a,0xbe2bf804,0x3f67c187,0xbed97cb9,
0x3f7baccd,0xbe3b6ecf,0x3f633c5a,0xbeebcbbb,0x3f7aed37,0xbe4ada4f,0x3f5e5aa6,0xbefdbabb,0x3f7a1e84,0xbe5a3997,0x3f591e6a,0xbf07a136,0x3f7940c0,0xbe698bba,0x3f5389c7,0xbf102dd5,
0x3f800000,0x0,0x3f800000,0x0,0x3f7e5a38,0xbde7fa9a,0x3f7d906e,0xbe0cec4a,0x3f796e4e,0xbe667c66,0x3f764d97,0xbe8b9507,0x3f714c7a,0xbeab0201,0x3f6a5ad9,0xbece0bea,
0x3f660f88,0xbee0924f,0x3f59f26a,0xbf064b82,0x3f57dc7f,0xbf099f4e,0x3f456439,0xbf2302d3,0x3f46e22a,0xbf212ff9,0x3f2d1469,0xbf3ca003,0x3f33587a,0xbf36ad7f,0x3f11796b,0xbf52a649,
0x3f1d7fd1,0xbf49d112,0x3ee63375,0xbf64aa59,0x3f05a02c,0xbf5a5ba0,0x3ea5129f,0xbf725470,0x3ed8106b,0xbf6816a8,0x3e419b37,0xbf7b61fc,0x3ea21887,0xbf72d4eb,0x3d55880e,0xbf7fa6e3,
0x3e541501,0xbf7a7302,0xbdafb680,0xbf7f0e58,0x3dc27c39,0xbf7ed7d4,0xbe636c83,0xbf799b43,0xbc96c9b6,0xbf7ff4e6,0xbeb554ec,0xbf6f6830,0xbe06b26e,0xbf7dc68c,0xbef58035,0xbf60a6cf,
0x3f800000,0x0,0x3f800000,0x0,0x3f7a4910,0xbe57278f,0x3f78e3d6,0xbe6fa8b2,0x3f696591,0xbed25a09,0x3f63f473,0xbee900b7,0x3f4e1689,0xbf17de12,0x3f425b89,0xbf269de3,
0x3f299415,0xbf3fc767,0x3f15f6d9,0xbf4f7a1f,0x3efafef7,0xbf5f20db,0x3ec27b55,0xbf6cd007,0x3e97a117,0xbf748422,0x3e1c76de,0xbf7cfe73,0x3db5f8da,0xbf7efcc9,0xbda973ba,0xbf7f1f49,
0xbdfab273,0xbf7e1324,0xbea09ae5,0xbf731447,0xbea80b1f,0xbf71d19f,0xbf06f686,0xbf5988ad,0xbf04f484,0xbf5ac450,0xbf36206c,0xbf33e7bc,0xbf2ff3e6,0xbf39f2ac,0xbf5b2c79,0xbf044889,
0xbf531849,0xbf10d3cd,0xbf740bdd,0xbe9aa086,0xbf6cd007,0xbec27b55,0xbf7f5ce9,0xbd9064b4,0xbf7bf531,0xbe354098,0xbf7c7eb0,0x3e28defc,0xbf7fdaaf,0x3d0a342f,0xbf6b99fb,0x3ec8477c,
0x3f800000,0x0,0x3f800000,0x0,0x3f73ced9,0xbe9c1faf,0x3f71d19f,0xbea80b1f,0x3f5064af,0xbf14b039,0x3f48d8b3,0xbf1ebc12,0x3f19210f,0xbf4d26fd,0x3f099f4e,0xbf57dc7f,
0x3ea68f12,0xbf721352,0x3e6c9a7f,0xbf791298,0x3cafea69,0xbf7ff0e3,0xbdcefdb7,0xbf7eb069,0xbe919ddd,0xbf756d97,0xbed8106b,0xbf6816a8,0xbf102dd5,0xbf5389c7,0xbf3238aa,0xbf37c655,
0xbf49d112,0xbf1d7fd1,0xbf64aa59,0xbee63375,0xbf703b37,0xbeb0eacb,0xbf7dc68c,0xbe06b26e,0xbf7fc38f,0xbd2fe007,0xbf7ac516,0x3e4dee60,0xbf76ef5b,0x3e870ada,0xbf5bfb34,0x3f02ef9f,
0xbf5695e5,0x3f0b9a6b,0xbf24d225,0x3f43e200,0xbf21cbfb,0x3f466354,0xbeb6ccc3,0x3f6f20b0,0xbebb31a0,0x3f6e46be,0xbcfb49ba,0x3f7fe129,0xbd83db0a,0x3f7f7808,0x3e9920fe,0x3f74484b,
0x3f800000,0x0,0x3f800000,0x0,0x3f7f7808,0xbd83db0a,0x3f7efcc9,0xbdb5f8da,0x3f7de0b1,0xbe039502,0x3f7bf531,0xbe354098,0x3f7b3bab,0xbe44b0b9,0x3f76ef5b,0xbe870ada,
0x3f778bc5,0xbe827dc0,0x3f6ff573,0xbeb263ef,0x3f72d4eb,0xbea21887,0x3f671599,0xbedc53c1,0x3f6d1c1d,0xbec1071e,0x3f5c61c7,0xbf0242b1,0x3f666771,0xbedf28a9,0x3f4fefa8,0xbf1553b7,
0x3f5ebe05,0xbefc5d27,0x3f41d870,0xbf273656,0x3f5627fe,0xbf0c42c9,0x3f3238aa,0xbf37c655,0x3f4cae79,0xbf19c200,0x3f212ff9,0xbf46e22a,0x3f425b89,0xbf269de3,0x3f0ee0db,0xbf546b3b,
0x3f373a23,0xbf32c8c9,0x3ef6e0cb,0xbf604621,0x3f2b561b,0xbf3e35c5,0x3ece0bea,0xbf6a5ad9,0x3f1ebc12,0xbf48d8b3,0x3ea395c5,0xbf7294f8,0x3f11796b,0xbf52a649,0x3e6fa8b2,0xbf78e3d6,
0x3f800000,0x0,0x3f800000,0x0,0x3f7c9f8a,0xbe25c58c,0x3f7b87b2,0xbe3e853e,0x3f7294f8,0xbea395c5,0x3f6e46be,0xbebb31a0,0x3f62241a,0xbeeff7fb,0x3f58b3a1,0xbf084b92,
0x3f4bbbf8,0xbf1b02c6,0x3f3b8f3b,0xbf2e3bde,0x3f2ff3e6,0xbf39f2ac,0x3f17de12,0xbf4e1689,0x3f0f8784,0xbf53fac3,0x3eddbe79,0xbf66becc,0x3ed6a399,0xbf686b39,0x3e840270,0xbf77583a,
0x3e888e93,0xbf76ba07,0x3d96a905,0xbf7f4e6d,0x3ddb7d37,0xbf7e868b,0xbde7fa9a,0xbf7e5a38,0xbd621469,0xbf7f9c18,0xbe97a117,0xbf748422,0xbe5d4b1a,0xbf79f35e,0xbeeff7fb,0xbf62241a,
0xbebe1d4a,0xbf6db293,0xbf1ff6cb,0xbf47de65,0xbf044889,0xbf5b2c79,0xbf425b89,0xbf269de3,0xbf26050a,0xbf42de29,0xbf5df6be,0xbeff17b2,0xbf436051,0xbf256bcb,0xbf71d19f,0xbea80b1f,
0x3f800000,0x0,0x3f800000,0x0,0x3f77583a,0xbe840270,0x3f75a67b,0xbe901c2c,0x3f5df6be,0xbeff17b2,0x3f577026,0xbf0a48ad,0x3f3592e7,0xbf34768f,0x3f27ce61,0xbf4154e1,
0x3f00e7e4,0xbf5d2d53,0x3ed53641,0xbf68bf3c,0x3e870ada,0xbf76ef5b,0x3e1324ca,0xbf7d57de,0x3bc90f88,0xbf7ffec4,0xbe1008b7,0xbf7d7474,0xbe80f8c0,0xbf77beb7,0xbed3c867,0xbf6912ae,
0xbefc5d27,0xbf5ebe05,0xbf273656,0xbf41d870,0xbf33587a,0xbf36ad7f,0xbf570348,0xbf0af1b7,0xbf5c61c7,0xbf0242b1,0xbf756d97,0xbe919ddd,0xbf76841b,0xbe8a11f7,0xbf7fffb1,0xbb490fc6,
0xbf7ffb11,0xbc490e90,0xbf75dec6,0x3e8e9a22,0xbf7822d1,0x3e7bdba4,0xbf57dc7f,0x3f099f4e,0xbf5f8327,0x3ef9a02d,0xbf286605,0x3f40d0da,0xbf37c655,0x3f3238aa,0xbed6a399,0x3f686b39,
0x3f800000,0x0,0x3f800000,0x0,0x3f6faf1b,0xbeb3dca5,0x3f6d67a1,0xbebf926f,0x3f40d0da,0xbf286605,0x3f385216,0xbf31a81d,0x3ef2bd43,0xbf61668a,0x3ed0eb2a,0xbf69b7e4,
0x3e09cf86,0xbf7dabcc,0x3d16c32c,0xbf7fd397,0xbe636c83,0xbf799b43,0xbeadf73c,0xbf70c501,0xbf0cead0,0xbf55b993,0xbf2ac082,0xbf3ebc1b,0xbf4f0417,0xbf16999f,0xbf65b710,0xbee1fb6a,
0xbf76ba07,0xbe888e93,0xbf7f4e6d,0xbd96a905,0xbf7efcc9,0x3db5f8da,0xbf73ced9,0x3e9c1faf,0xbf66becc,0x3eddbe79,0xbf44e3f5,0x3f239da9,0xbf311722,0x3f38dd65,0xbef2bd43,0x3f61668a,
0xbec9b953,0x3f6b4b0c,0xbde1bc2e,0x3f7e70b0,0xbd3c6dd5,0x3f7fba9e,0x3e8a11f7,0x3f76841b,0x3e9d9e78,0x3f73913f,0x3f1c420c,0x3f4ac77f,0x3f1f599f,0x3f485bca,0x3f5cc7d1,0x3f019573,
0x80002c00,0x24a52ba8,0x28722aa7,0x2a2f2913,0x80002c00,0x29132a2f,0x2bd9223e,0x2b0ea78b,0x80002c00,0x2b0e278b,0x2aa7a872,0x9e46abf6,0x80002c00,0x2bf61e46,0x223eabd9,0xaba8a4a5,
0x80002c00,0x2ba8a4a5,0xa872aaa7,0xa9132a2f,0x80002c00,0x2a2fa913,0xabd9a23e,0x278b2b0e,0x80002c00,0x278bab0e,0xaaa72872,0x2bf69e46,0x80002c00,0x1e46abf6,0xa23e2bd9,0x24a5aba8,
0x80002c00,0xa4a5aba8,0x28722aa7,0xaa2fa913,0x80002c00,0xa913aa2f,0x2bd9223e,0xab0e278b,0x80002c00,0xab0ea78b,0x2aa7a872,0x1e462bf6,0x80002c00,0xabf69e46,0x223eabd9,0x2ba824a5,
0x80002c00,0xaba824a5,0xa872aaa7,0x2913aa2f,0x80002c00,0xaa2f2913,0xabd9a23e,0xa78bab0e,0x80002c00,0xa78b2b0e,0xaaa72872,0xabf61e46,0x80002c00,0x9e462bf6,0xa23e2bd9,0xa4a52ba8,
0x2b64261f,0x2bf61e46,0x2bd9a23e,0x2b0ea78b,0x261fab64,0xa4a5aba8,0xaaa7a872,0xabf61e46,0xab64a61f,0xaa2f2913,0x223e2bd9,0x2ba824a5,0xa61f2b64,0x2b0e278b,0x2872aaa7,0xaa2fa913,
0x2b64261f,0x1e46abf6,0xabd9223e,0x278b2b0e,0x261fab64,0xaba824a5,0x2aa72872,0x9e46abf6,0xab64a61f,0x29132a2f,0xa23eabd9,0xa4a52ba8,0xa61f2b64,0x278bab0e,0xa8722aa7,0x2913aa2f,
0x2b64261f,0xabf69e46,0x2bd9a23e,0xab0e278b,0x261fab64,0x24a52ba8,0xaaa7a872,0x2bf69e46,0xab64a61f,0x2a2fa913,0x223e2bd9,0xaba8a4a5,0xa61f2b64,0xab0ea78b,0x2872aaa7,0x2a2f2913,
0x2b64261f,0x9e462bf6,0xabd9223e,0xa78bab0e,0x261fab64,0x2ba8a4a5,0x2aa72872,0x1e462bf6,0xab64a61f,0xa913aa2f,0xa23eabd9,0x24a5aba8,0xa61f2b64,0xa78b2b0e,0xa8722aa7,0xa9132a2f,
0x29a8a9a8,0x278bab0e,0x223eabd9,0x9e46abf6,0xa9a829a8,0x9e462bf6,0x28722aa7,0x2ba824a5,0x29a8a9a8,0xa4a5aba8,0xabd9a23e,0xa9132a2f,0xa9a829a8,0x29132a2f,0x2aa7a872,0xa78bab0e,
0x29a8a9a8,0xab0ea78b,0xa23e2bd9,0x2bf69e46,0xa9a829a8,0x2bf61e46,0xa872aaa7,0xa4a52ba8,0x29a8a9a8,0xaba824a5,0x2bd9223e,0xaa2fa913,0xa9a829a8,0x2a2fa913,0xaaa72872,0x2b0ea78b,
0x29a8a9a8,0xa78b2b0e,0x223eabd9,0x1e462bf6,0xa9a829a8,0x1e46abf6,0x28722aa7,0xaba8a4a5,0x29a8a9a8,0x24a52ba8,0xabd9a23e,0x2913aa2f,0xa9a829a8,0xa913aa2f,0x2aa7a872,0x278b2b0e,
0x29a8a9a8,0x2b0e278b,0xa23e2bd9,0xabf61e46,0xa9a829a8,0xabf69e46,0xa872aaa7,0x24a5aba8,0x29a8a9a8,0x2ba8a4a5,0x2bd9223e,0x2a2f2913,0xa9a829a8,0xaa2f2913,0xaaa72872,0xab0e278b,
0xa61fab64,0xa913aa2f,0xaaa7a872,0xaba8a4a5,0x2b64a61f,0x278bab0e,0xa23eabd9,0xaa2fa913,0x261f2b64,0x2bf61e46,0x2872aaa7,0xa78bab0e,0xab64261f,0x24a52ba8,0x2bd9a23e,0x9e46abf6,
0xa61fab64,0xaa2f2913,0x2aa72872,0x24a5aba8,0x2b64a61f,0xab0ea78b,0x223e2bd9,0x2913aa2f,0x261f2b64,0x1e46abf6,0xa8722aa7,0x2b0ea78b,0xab64261f,0x2ba8a4a5,0xabd9223e,0x2bf69e46,
0xa61fab64,0x29132a2f,0xaaa7a872,0x2ba824a5,0x2b64a61f,0xa78b2b0e,0xa23eabd9,0x2a2f2913,0x261f2b64,0xabf69e46,0x2872aaa7,0x278b2b0e,0xab64261f,0xa4a5aba8,0x2bd9a23e,0x1e462bf6,
0xa61fab64,0x2a2fa913,0x2aa72872,0xa4a52ba8,0x2b64a61f,0x2b0e278b,0x223e2bd9,0xa9132a2f,0x261f2b64,0x9e462bf6,0xa8722aa7,0xab0e278b,0xab64261f,0xaba824a5,0xabd9223e,0xabf61e46,
0x2c000000,0x2ba8a4a5,0x2aa7a872,0x2913aa2f,0x2c000000,0x2a2fa913,0x223eabd9,0xa78bab0e,0x2c000000,0x278bab0e,0xa872aaa7,0xabf61e46,0x2c000000,0x1e46abf6,0xabd9a23e,0xa4a52ba8,
0x2c000000,0xa4a5aba8,0xaaa72872,0x2a2f2913,0x2c000000,0xa913aa2f,0xa23e2bd9,0x2b0ea78b,0x2c000000,0xab0ea78b,0x28722aa7,0x9e46abf6,0x2c000000,0xabf69e46,0x2bd9223e,0xaba8a4a5,
0x2c000000,0xaba824a5,0x2aa7a872,0xa9132a2f,0x2c000000,0xaa2f2913,0x223eabd9,0x278b2b0e,0x2c000000,0xa78b2b0e,0xa872aaa7,0x2bf69e46,0x2c000000,0x9e462bf6,0xabd9a23e,0x24a5aba8,
0x2c000000,0x24a52ba8,0xaaa72872,0xaa2fa913,0x2c000000,0x29132a2f,0xa23e2bd9,0xab0e278b,0x2c000000,0x2b0e278b,0x28722aa7,0x1e462bf6,0x2c000000,0x2bf61e46,0x2bd9223e,0x2ba824a5,
0x261fab64,0x1e46abf6,0xa23eabd9,0xa78bab0e,0xab64a61f,0xaba824a5,0xa8722aa7,0x1e462bf6,0xa61f2b64,0x29132a2f,0x2bd9a23e,0x24a5aba8,0x2b64261f,0x278bab0e,0xaaa7a872,0xa9132a2f,
0x261fab64,0xabf69e46,0x223e2bd9,0x2b0ea78b,0xab64a61f,0x24a52ba8,0x2872aaa7,0xabf61e46,0xa61f2b64,0x2a2fa913,0xabd9223e,0x2ba824a5,0x2b64261f,0xab0ea78b,0x2aa72872,0xaa2fa913,
0x261fab64,0x9e462bf6,0xa23eabd9,0x278b2b0e,0xab64a61f,0x2ba8a4a5,0xa8722aa7,0x9e46abf6,0xa61f2b64,0xa913aa2f,0x2bd9a23e,0xa4a52ba8,0x2b64261f,0xa78b2b0e,0xaaa7a872,0x2913aa2f,
0x261fab64,0x2bf61e46,0x223e2bd9,0xab0e278b,0xab64a61f,0xa4a5aba8,0x2872aaa7,0x2bf69e46,0xa61f2b64,0xaa2f2913,0xabd9223e,0xaba8a4a5,0x2b64261f,0x2b0e278b,0x2aa72872,0x2a2f2913,
0xa9a8a9a8,0xab0ea78b,0xabd9a23e,0xabf61e46,0x29a829a8,0x2bf61e46,0x2aa7a872,0x24a5aba8,0xa9a8a9a8,0xaba824a5,0xa23e2bd9,0x2a2f2913,0x29a829a8,0x2a2fa913,0xa872aaa7,0xab0e278b,
0xa9a8a9a8,0xa78b2b0e,0x2bd9223e,0x9e46abf6,0x29a829a8,0x1e46abf6,0xaaa72872,0x2ba824a5,0xa9a8a9a8,0x24a52ba8,0x223eabd9,0xa9132a2f,0x29a829a8,0xa913aa2f,0x28722aa7,0xa78bab0e,
0xa9a8a9a8,0x2b0e278b,0xabd9a23e,0x2bf69e46,0x29a829a8,0xabf69e46,0x2aa7a872,0xa4a52ba8,0xa9a8a9a8,0x2ba8a4a5,0xa23e2bd9,0xaa2fa913,0x29a829a8,0xaa2f2913,0xa872aaa7,0x2b0ea78b,
0xa9a8a9a8,0x278bab0e,0x2bd9223e,0x1e462bf6,0x29a829a8,0x9e462bf6,0xaaa72872,0xaba8a4a5,0xa9a8a9a8,0xa4a5aba8,0x223eabd9,0x2913aa2f,0x29a829a8,0x29132a2f,0x28722aa7,0x278b2b0e,
0xab64261f,0xaa2f2913,0xa8722aa7,0xa4a52ba8,0xa61fab64,0xab0ea78b,0xabd9223e,0xa9132a2f,0x2b64a61f,0x1e46abf6,0xaaa7a872,0xab0e278b,0x261f2b64,0x2ba8a4a5,0xa23eabd9,0xabf61e46,
0xab64261f,0x29132a2f,0x2872aaa7,0xaba8a4a5,0xa61fab64,0xa78b2b0e,0x2bd9a23e,0xaa2fa913,0x2b64a61f,0xabf69e46,0x2aa72872,0xa78bab0e,0x261f2b64,0xa4a5aba8,0x223e2bd9,0x9e46abf6,
0xab64261f,0x2a2fa913,0xa8722aa7,0x24a5aba8,0xa61fab64,0x2b0e278b,0xabd9223e,0x2913aa2f,0x2b64a61f,0x9e462bf6,0xaaa7a872,0x2b0ea78b,0x261f2b64,0xaba824a5,0xa23eabd9,0x2bf69e46,
0xab64261f,0xa913aa2f,0x2872aaa7,0x2ba824a5,0xa61fab64,0x278bab0e,0x2bd9a23e,0x2a2f2913,0x2b64a61f,0x2bf61e46,0x2aa72872,0x278b2b0e,0x261f2b64,0x24a52ba8,0x223e2bd9,0x1e462bf6,
0x80002c00,0x80002c00,0x80002c00,0x80002c00,0x24a52ba8,0x29132a2f,0x2b0e278b,0x2bf61e46,0x28722aa7,0x2bd9223e,0x2aa7a872,0x223eabd9,0x2a2f2913,0x2b0ea78b,0x9e46abf6,0xaba8a4a5,
0x2b64261f,0x261fab64,0xab64a61f,0xa61f2b64,0x2bf61e46,0xa4a5aba8,0xaa2f2913,0x2b0e278b,0x2bd9a23e,0xaaa7a872,0x223e2bd9,0x2872aaa7,0x2b0ea78b,0xabf61e46,0x2ba824a5,0xaa2fa913,
0x29a8a9a8,0xa9a829a8,0x29a8a9a8,0xa9a829a8,0x278bab0e,0x9e462bf6,0xa4a5aba8,0x29132a2f,0x223eabd9,0x28722aa7,0xabd9a23e,0x2aa7a872,0x9e46abf6,0x2ba824a5,0xa9132a2f,0xa78bab0e,
0xa61fab64,0x2b64a61f,0x261f2b64,0xab64261f,0xa913aa2f,0x278bab0e,0x2bf61e46,0x24a52ba8,0xaaa7a872,0xa23eabd9,0x2872aaa7,0x2bd9a23e,0xaba8a4a5,0xaa2fa913,0xa78bab0e,0x9e46abf6,
0x80002c00,0x80002c00,0x80002c00,0x80002c00,0x2ba8a4a5,0x2a2fa913,0x278bab0e,0x1e46abf6,0xa872aaa7,0xabd9a23e,0xaaa72872,0xa23e2bd9,0xa9132a2f,0x278b2b0e,0x2bf69e46,0x24a5aba8,
0x2b64261f,0x261fab64,0xab64a61f,0xa61f2b64,0x1e46abf6,0xaba824a5,0x29132a2f,0x278bab0e,0xabd9223e,0x2aa72872,0xa23eabd9,0xa8722aa7,0x278b2b0e,0x9e46abf6,0xa4a52ba8,0x2913aa2f,
0x29a8a9a8,0xa9a829a8,0x29a8a9a8,0xa9a829a8,0xab0ea78b,0x2bf61e46,0xaba824a5,0x2a2fa913,0xa23e2bd9,0xa872aaa7,0x2bd9223e,0xaaa72872,0x2bf69e46,0xa4a52ba8,0xaa2fa913,0x2b0ea78b,
0xa61fab64,0x2b64a61f,0x261f2b64,0xab64261f,0xaa2f2913,0xab0ea78b,0x1e46abf6,0x2ba8a4a5,0x2aa72872,0x223e2bd9,0xa8722aa7,0xabd9223e,0x24a5aba8,0x2913aa2f,0x2b0ea78b,0x2bf69e46,
0x80002c00,0x80002c00,0x80002c00,0x80002c00,0xa4a5aba8,0xa913aa2f,0xab0ea78b,0xabf69e46,0x28722aa7,0x2bd9223e,0x2aa7a872,0x223eabd9,0xaa2fa913,0xab0e278b,0x1e462bf6,0x2ba824a5,
0x2b64261f,0x261fab64,0xab64a61f,0xa61f2b64,0xabf69e46,0x24a52ba8,0x2a2fa913,0xab0ea78b,0x2bd9a23e,0xaaa7a872,0x223e2bd9,0x2872aaa7,0xab0e278b,0x2bf69e46,0xaba8a4a5,0x2a2f2913,
0x29a8a9a8,0xa9a829a8,0x29a8a9a8,0xa9a829a8,0xa78b2b0e,0x1e46abf6,0x24a52ba8,0xa913aa2f,0x223eabd9,0x28722aa7,0xabd9a23e,0x2aa7a872,0x1e462bf6,0xaba8a4a5,0x2913aa2f,0x278b2b0e,
0xa61fab64,0x2b64a61f,0x261f2b64,0xab64261f,0x29132a2f,0xa78b2b0e,0xabf69e46,0xa4a5aba8,0xaaa7a872,0xa23eabd9,0x2872aaa7,0x2bd9a23e,0x2ba824a5,0x2a2f2913,0x278b2b0e,0x1e462bf6,
0x80002c00,0x80002c00,0x80002c00,0x80002c00,0xaba824a5,0xaa2f2913,0xa78b2b0e,0x9e462bf6,0xa872aaa7,0xabd9a23e,0xaaa72872,0xa23e2bd9,0x2913aa2f,0xa78bab0e,0xabf61e46,0xa4a52ba8,
0x2b64261f,0x261fab64,0xab64a61f,0xa61f2b64,0x9e462bf6,0x2ba8a4a5,0xa913aa2f,0xa78b2b0e,0xabd9223e,0x2aa72872,0xa23eabd9,0xa8722aa7,0xa78bab0e,0x1e462bf6,0x24a5aba8,0xa9132a2f,
0x29a8a9a8,0xa9a829a8,0x29a8a9a8,0xa9a829a8,0x2b0e278b,0xabf69e46,0x2ba8a4a5,0xaa2f2913,0xa23e2bd9,0xa872aaa7,0x2bd9223e,0xaaa72872,0xabf61e46,0x24a5aba8,0x2a2f2913,0xab0e278b,
0xa61fab64,0x2b64a61f,0x261f2b64,0xab64261f,0x2a2fa913,0x2b0e278b,0x9e462bf6,0xaba824a5,0x2aa72872,0x223e2bd9,0xa8722aa7,0xabd9223e,0xa4a52ba8,0xa9132a2f,0xab0e278b,0xabf61e46,
0x2c000000,0x2c000000,0x2c000000,0x2c000000,0x2ba8a4a5,0x2a2fa913,0x278bab0e,0x1e46abf6,0x2aa7a872,0x223eabd9,0xa872aaa7,0xabd9a23e,0x2913aa2f,0xa78bab0e,0xabf61e46,0xa4a52ba8,
0x261fab64,0xab64a61f,0xa61f2b64,0x2b64261f,0x1e46abf6,0xaba824a5,0x29132a2f,0x278bab0e,0xa23eabd9,0xa8722aa7,0x2bd9a23e,0xaaa7a872,0xa78bab0e,0x1e462bf6,0x24a5aba8,0xa9132a2f,
0xa9a8a9a8,0x29a829a8,0xa9a8a9a8,0x29a829a8,0xab0ea78b,0x2bf61e46,0xaba824a5,0x2a2fa913,0xabd9a23e,0x2aa7a872,0xa23e2bd9,0xa872aaa7,0xabf61e46,0x24a5aba8,0x2a2f2913,0xab0e278b,
0xab64261f,0xa61fab64,0x2b64a61f,0x261f2b64,0xaa2f2913,0xab0ea78b,0x1e46abf6,0x2ba8a4a5,0xa8722aa7,0xabd9223e,0xaaa7a872,0xa23eabd9,0xa4a52ba8,0xa9132a2f,0xab0e278b,0xabf61e46,
0x2c000000,0x2c000000,0x2c000000,0x2c000000,0xa4a5aba8,0xa913aa2f,0xab0ea78b,0xabf69e46,0xaaa72872,0xa23e2bd9,0x28722aa7,0x2bd9223e,0x2a2f2913,0x2b0ea78b,0x9e46abf6,0xaba8a4a5,
0x261fab64,0xab64a61f,0xa61f2b64,0x2b64261f,0xabf69e46,0x24a52ba8,0x2a2fa913,0xab0ea78b,0x223e2bd9,0x2872aaa7,0xabd9223e,0x2aa72872,0x2b0ea78b,0xabf61e46,0x2ba824a5,0xaa2fa913,
0xa9a8a9a8,0x29a829a8,0xa9a8a9a8,0x29a829a8,0xa78b2b0e,0x1e46abf6,0x24a52ba8,0xa913aa2f,0x2bd9223e,0xaaa72872,0x223eabd9,0x28722aa7,0x9e46abf6,0x2ba824a5,0xa9132a2f,0xa78bab0e,
0xab64261f,0xa61fab64,0x2b64a61f,0x261f2b64,0x29132a2f,0xa78b2b0e,0xabf69e46,0xa4a5aba8,0x2872aaa7,0x2bd9a23e,0x2aa72872,0x223e2bd9,0xaba8a4a5,0xaa2fa913,0xa78bab0e,0x9e46abf6,
0x2c000000,0x2c000000,0x2c000000,0x2c000000,0xaba824a5,0xaa2f2913,0xa78b2b0e,0x9e462bf6,0x2aa7a872,0x223eabd9,0xa872aaa7,0xabd9a23e,0xa9132a2f,0x278b2b0e,0x2bf69e46,0x24a5aba8,
0x261fab64,0xab64a61f,0xa61f2b64,0x2b64261f,0x9e462bf6,0x2ba8a4a5,0xa913aa2f,0xa78b2b0e,0xa23eabd9,0xa8722aa7,0x2bd9a23e,0xaaa7a872,0x278b2b0e,0x9e46abf6,0xa4a52ba8,0x2913aa2f,
0xa9a8a9a8,0x29a829a8,0xa9a8a9a8,0x29a829a8,0x2b0e278b,0xabf69e46,0x2ba8a4a5,0xaa2f2913,0xabd9a23e,0x2aa7a872,0xa23e2bd9,0xa872aaa7,0x2bf69e46,0xa4a52ba8,0xaa2fa913,0x2b0ea78b,
0xab64261f,0xa61fab64,0x2b64a61f,0x261f2b64,0x2a2fa913,0x2b0e278b,0x9e462bf6,0xaba824a5,0xa8722aa7,0xabd9223e,0xaaa7a872,0xa23eabd9,0x24a5aba8,0x2913aa2f,0x2b0ea78b,0x2bf69e46,
0x2c000000,0x2c000000,0x2c000000,0x2c000000,0x24a52ba8,0x29132a2f,0x2b0e278b,0x2bf61e46,0xaaa72872,0xa23e2bd9,0x28722aa7,0x2bd9223e,0xaa2fa913,0xab0e278b,0x1e462bf6,0x2ba824a5,
0x261fab64,0xab64a61f,0xa61f2b64,0x2b64261f,0x2bf61e46,0xa4a5aba8,0xaa2f2913,0x2b0e278b,0x223e2bd9,0x2872aaa7,0xabd9223e,0x2aa72872,0xab0e278b,0x2bf69e46,0xaba8a4a5,0x2a2f2913,
0xa9a8a9a8,0x29a829a8,0xa9a8a9a8,0x29a829a8,0x278bab0e,0x9e462bf6,0xa4a5aba8,0x29132a2f,0x2bd9223e,0xaaa72872,0x223eabd9,0x28722aa7,0x1e462bf6,0xaba8a4a5,0x2913aa2f,0x278b2b0e,
0xab64261f,0xa61fab64,0x2b64a61f,0x261f2b64,0xa913aa2f,0x278bab0e,0x2bf61e46,0x24a52ba8,0x2872aaa7,0x2bd9a23e,0x2aa72872,0x223e2bd9,0x2ba824a5,0x2a2f2913,0x278b2b0e,0x1e462bf6,
0x80002c00,0x80002c00,0x80002c00,0x80002c00,0x80002c00,0x261f2b64,0x29a829a8,0x2b64261f,0x80002c00,0x29a829a8,0x2c000000,0x29a8a9a8,0x80002c00,0x2b64261f,0x29a8a9a8,0xa61fab64,
0x80002c00,0x2c000000,0x8000ac00,0xac000000,0x80002c00,0x2b64a61f,0xa9a8a9a8,0xa61f2b64,0x80002c00,0x29a8a9a8,0xac000000,0x29a829a8,0x80002c00,0x261fab64,0xa9a829a8,0x2b64a61f,
0x80002c00,0x8000ac00,0x80002c00,0x8000ac00,0x80002c00,0xa61fab64,0x29a829a8,0xab64a61f,0x80002c00,0xa9a8a9a8,0x2c000000,0xa9a829a8,0x80002c00,0xab64a61f,0x29a8a9a8,0x261f2b64,
0x80002c00,0xac000000,0x8000ac00,0x2c000000,0x80002c00,0xab64261f,0xa9a8a9a8,0x261fab64,0x80002c00,0xa9a829a8,0xac000000,0xa9a8a9a8,0x80002c00,0xa61f2b64,0xa9a829a8,0xab64261f,
0x80002c00,0x80002c00,0x80002c00,0x80002c00,0x2c000000,0x2b64a61f,0x29a8a9a8,0x261fab64,0x8000ac00,0xa9a8a9a8,0xac000000,0xa9a829a8,0xac000000,0xa61f2b64,0x29a829a8,0x2b64a61f,
0x80002c00,0x2c000000,0x8000ac00,0xac000000,0x2c000000,0xa61fab64,0xa9a829a8,0x2b64261f,0x8000ac00,0xa9a829a8,0x2c000000,0xa9a8a9a8,0xac000000,0x2b64261f,0xa9a8a9a8,0x261f2b64,
0x80002c00,0x8000ac00,0x80002c00,0x8000ac00,0x2c000000,0xab64261f,0x29a8a9a8,0xa61f2b64,0x8000ac00,0x29a829a8,0xac000000,0x29a8a9a8,0xac000000,0x261fab64,0x29a829a8,0xab64261f,
0x80002c00,0xac000000,0x8000ac00,0x2c000000,0x2c000000,0x261f2b64,0xa9a829a8,0xab64a61f,0x8000ac00,0x29a8a9a8,0x2c000000,0x29a829a8,0xac000000,0xab64a61f,0xa9a8a9a8,0xa61fab64,
0x80002c00,0x80002c00,0x80002c00,0x80002c00,0x8000ac00,0xa61fab64,0xa9a8a9a8,0xab64a61f,0x80002c00,0x29a829a8,0x2c000000,0x29a8a9a8,0x8000ac00,0xab64a61f,0xa9a829a8,0x261f2b64,
0x80002c00,0x2c000000,0x8000ac00,0xac000000,0x8000ac00,0xab64261f,0x29a829a8,0x261fab64,0x80002c00,0x29a8a9a8,0xac000000,0x29a829a8,0x8000ac00,0xa61f2b64,0x29a8a9a8,0xab64261f,
0x80002c00,0x8000ac00,0x80002c00,0x8000ac00,0x8000ac00,0x261f2b64,0xa9a8a9a8,0x2b64261f,0x80002c00,0xa9a8a9a8,0x2c000000,0xa9a829a8,0x8000ac00,0x2b64261f,0xa9a829a8,0xa61fab64,
0x80002c00,0xac000000,0x8000ac00,0x2c000000,0x8000ac00,0x2b64a61f,0x29a829a8,0xa61f2b64,0x80002c00,0xa9a829a8,0xac000000,0xa9a8a9a8,0x8000ac00,0x261fab64,0x29a8a9a8,0x2b64a61f,
0x80002c00,0x80002c00,0x80002c00,0x80002c00,0xac000000,0xab64261f,0xa9a829a8,0xa61f2b64,0x8000ac00,0xa9a8a9a8,0xac000000,0xa9a829a8,0x2c000000,0x261fab64,0xa9a8a9a8,0xab64261f,
0x80002c00,0x2c000000,0x8000ac00,0xac000000,0xac000000,0x261f2b64,0x29a8a9a8,0xab64a61f,0x8000ac00,0xa9a829a8,0x2c000000,0xa9a8a9a8,0x2c000000,0xab64a61f,0x29a829a8,0xa61fab64,
0x80002c00,0x8000ac00,0x80002c00,0x8000ac00,0xac000000,0x2b64a61f,0xa9a829a8,0x261fab64,0x8000ac00,0x29a829a8,0xac000000,0x29a8a9a8,0x2c000000,0xa61f2b64,0xa9a8a9a8,0x2b64a61f,
0x80002c00,0xac000000,0x8000ac00,0x2c000000,0xac000000,0xa61fab64,0x29a8a9a8,0x2b64261f,0x8000ac00,0x29a8a9a8,0x2c000000,0x29a829a8,0x2c000000,0x2b64261f,0x29a829a8,0x261f2b64,
0x2c000000,0x2c000000,0x2c000000,0x2c000000,0x2c000000,0x2b64a61f,0x29a8a9a8,0x261fab64,0x2c000000,0x29a8a9a8,0xac00,0xa9a8a9a8,0x2c000000,0x261fab64,0xa9a8a9a8,0xab64261f,
0x2c000000,0xac00,0xac000000,0x2c00,0x2c000000,0xa61fab64,0xa9a829a8,0x2b64261f,0x2c000000,0xa9a8a9a8,0x2c00,0x29a8a9a8,0x2c000000,0xab64a61f,0x29a829a8,0xa61fab64,
0x2c000000,0xac000000,0x2c000000,0xac000000,0x2c000000,0xab64261f,0x29a8a9a8,0xa61f2b64,0x2c000000,0xa9a829a8,0xac00,0x29a829a8,0x2c000000,0xa61f2b64,0xa9a8a9a8,0x2b64a61f,
0x2c000000,0x2c00,0xac000000,0xac00,0x2c000000,0x261f2b64,0xa9a829a8,0xab64a61f,0x2c000000,0x29a829a8,0x2c00,0xa9a829a8,0x2c000000,0x2b64261f,0x29a829a8,0x261f2b64,
0x2c000000,0x2c000000,0x2c000000,0x2c000000,0xac00,0xa61fab64,0xa9a8a9a8,0xab64a61f,0xac000000,0xa9a829a8,0x2c00,0x29a829a8,0x2c00,0x2b64261f,0x29a8a9a8,0xa61fab64,
0x2c000000,0xac00,0xac000000,0x2c00,0xac00,0xab64261f,0x29a829a8,0x261fab64,0xac000000,0x29a829a8,0xac00,0xa9a829a8,0x2c00,0x261fab64,0xa9a829a8,0x2b64a61f,
0x2c000000,0xac000000,0x2c000000,0xac000000,0xac00,0x261f2b64,0xa9a8a9a8,0x2b64261f,0xac000000,0x29a8a9a8,0x2c00,0xa9a8a9a8,0x2c00,0xab64a61f,0x29a8a9a8,0x261f2b64,
0x2c000000,0x2c00,0xac000000,0xac00,0xac00,0x2b64a61f,0x29a829a8,0xa61f2b64,0xac000000,0xa9a8a9a8,0xac00,0x29a8a9a8,0x2c00,0xa61f2b64,0xa9a829a8,0xab64261f,
0x2c000000,0x2c000000,0x2c000000,0x2c000000,0xac000000,0xab64261f,0xa9a829a8,0xa61f2b64,0x2c000000,0x29a8a9a8,0xac00,0xa9a8a9a8,0xac000000,0xa61f2b64,0x29a829a8,0x2b64a61f,
0x2c000000,0xac00,0xac000000,0x2c00,0xac000000,0x261f2b64,0x29a8a9a8,0xab64a61f,0x2c000000,0xa9a8a9a8,0x2c00,0x29a8a9a8,0xac000000,0x2b64261f,0xa9a8a9a8,0x261f2b64,
0x2c000000,0xac000000,0x2c000000,0xac000000,0xac000000,0x2b64a61f,0xa9a829a8,0x261fab64,0x2c000000,0xa9a829a8,0xac00,0x29a829a8,0xac000000,0x261fab64,0x29a829a8,0xab64261f,
0x2c000000,0x2c00,0xac000000,0xac00,0xac000000,0xa61fab64,0x29a8a9a8,0x2b64261f,0x2c000000,0x29a829a8,0x2c00,0xa9a829a8,0xac000000,0xab64a61f,0xa9a8a9a8,0xa61fab64,
0x2c000000,0x2c000000,0x2c000000,0x2c000000,0x2c00,0x261f2b64,0x29a829a8,0x2b64261f,0xac000000,0xa9a829a8,0x2c00,0x29a829a8,0xac00,0xab64a61f,0xa9a829a8,0x261f2b64,
0x2c000000,0xac00,0xac000000,0x2c00,0x2c00,0x2b64a61f,0xa9a8a9a8,0xa61f2b64,0xac000000,0x29a829a8,0xac00,0xa9a829a8,0xac00,0xa61f2b64,0x29a8a9a8,0xab64261f,
0x2c000000,0xac000000,0x2c000000,0xac000000,0x2c00,0xa61fab64,0x29a829a8,0xab64a61f,0xac000000,0x29a8a9a8,0x2c00,0xa9a8a9a8,0xac00,0x2b64261f,0xa9a829a8,0xa61fab64,
0x2c000000,0x2c00,0xac000000,0xac00,0x2c00,0xab64261f,0xa9a8a9a8,0x261fab64,0xac000000,0xa9a8a9a8,0xac00,0x29a8a9a8,0xac00,0x261fab64,0x29a8a9a8,0x2b64a61f,
0x3f800000,0x0,0x3f7ff4e6,0xbc96c9b6,0x3f800000,0x0,0x3f7fc38f,0xbd2fe007,0x3f800000,0x0,0x3f7f6ac7,0xbd8a200a,0x3f800000,0x0,0x3f7eea9d,0xbdbc3ac3,
0x3f800000,0x0,0x3f7e4323,0xbdee3876,0x3f800000,0x0,0x3f7d7474,0xbe1008b7,0x3f800000,0x0,0x3f7c7eb0,0xbe28defc,0x3f800000,0x0,0x3f7b61fc,0xbe419b37,
0x3f800000,0x0,0x3f7a1e84,0xbe5a3997,0x3f800000,0x0,0x3f78b47b,0xbe72b651,0x3f800000,0x0,0x3f772417,0xbe8586ce,0x3f800000,0x0,0x3f756d97,0xbe919ddd,
0x3f800000,0x0,0x3f73913f,0xbe9d9e78,0x3f800000,0x0,0x3f718f57,0xbea986c4,0x3f800000,0x0,0x3f6f6830,0xbeb554ec,0x3f800000,0x0,0x3f6d1c1d,0xbec1071e,
0x3f7f4e6d,0xbd96a905,0x3f7eea9d,0xbdbc3ac3,0x3f7c3b28,0xbe2f10a2,0x3f7a1e84,0xbe5a3997,0x3f76ba07,0xbe888e93,0x3f718f57,0xbea986c4,0x3f6ed89e,0xbeb8442a,0x3f655e0b,0xbee363fa,
0x3f64aa59,0xbee63375,0x3f55b993,0xbf0cead0,0x3f584853,0xbf08f59b,0x3f42de29,0xbf26050a,0x3f49d112,0xbf1d7fd1,0x3f2d1469,0xbf3ca003,0x3f396842,0xbf3085bb,0x3f14b039,0xbf5064af,
0x3f273656,0xbf41d870,0x3ef41f07,0xbf6106f2,0x3f13682a,0xbf514d3d,0x3ebb31a0,0xbf6e46be,0x3efc5d27,0xbf5ebe05,0x3e7ee6e1,0xbf77f110,0x3ecf7bca,0xbf6a09a7,0x3e039502,0xbf7de0b1,
0x3ea09ae5,0xbf731447,0x3bc90f88,0xbf7ffec4,0x3e605c13,0xbf79c79d,0xbdee3876,0xbf7e4323,0x3dfab273,0xbf7e1324,0xbe72b651,0xbf78b47b,0x3cc90ab0,0xbf7fec43,0xbeb554ec,0xbf6f6830,
0x3f7d3aac,0xbe164083,0x3f7c7eb0,0xbe28defc,0x3f710908,0xbeac7cd4,0x3f6d1c1d,0xbec1071e,0x3f5b941a,0xbf039c3d,0x3f5233c6,0xbf121eb0,0x3f3daef9,0xbf2beb4a,0x3f2d1469,0xbf3ca003,
0x3f187fc0,0xbf4d9f02,0x3eff17b2,0xbf5df6be,0x3edae880,0xbf676bd8,0x3e97a117,0xbf748422,0x3e78cfcc,0xbf7853f8,0x3da3308c,0xbf7f2f9d,0x3d48fb30,0xbf7fb10f,0xbe1008b7,0xbf7d7474,
0xbe164083,0xbf7d3aac,0xbeb554ec,0xbf6f6830,0xbeac7cd4,0xbf710908,0xbf0cead0,0xbf55b993,0xbf039c3d,0xbf5b941a,0xbf385216,0xbf31a81d,0xbf2beb4a,0xbf3daef9,0xbf5ac450,0xbf04f484,
0xbf4d9f02,0xbf187fc0,0xbf7294f8,0xbea395c5,0xbf676bd8,0xbedae880,0xbf7e9bc9,0xbdd53db9,0xbf7853f8,0xbe78cfcc,0xbf7e4323,0x3dee3876,0xbf7fb10f,0xbd48fb30,0xbf718f57,0x3ea986c4,
0x3f79c79d,0xbe605c13,0x3f78b47b,0xbe72b651,0x3f5ebe05,0xbefc5d27,0x3f591e6a,0xbf07a136,0x3f3085bb,0xbf396842,0x3f239da9,0xbf44e3f5,0x3ee63375,0xbf64aa59,0x3ebb31a0,0xbf6e46be,
0x3e2f10a2,0xbf7c3b28,0x3d621469,0xbf7f9c18,0xbdfab273,0xbf7e1324,0xbe8586ce,0xbf772417,0xbecf7bca,0xbf6a09a7,0xbf0cead0,0xbf55b993,0xbf273656,0xbf41d870,0xbf48d8b3,0xbf1ebc12,
0xbf584853,0xbf08f59b,0xbf708066,0xbeaf713a,0xbf76ba07,0xbe888e93,0xbf7fe129,0xbcfb49ba,0xbf7fec43,0x3cc90ab0,0xbf756d97,0x3e919ddd,0xbf731447,0x3ea09ae5,0xbf5233c6,0x3f121eb0,
0xbf514d3d,0x3f13682a,0xbf19c200,0x3f4cae79,0xbf1d7fd1,0x3f49d112,0xbea395c5,0x3f7294f8,0xbeb8442a,0x3f6ed89e,0xbbc90f88,0x3f7ffec4,0xbd96a905,0x3f7f4e6d,0x3e9d9e78,0x3f73913f,
0x3f7fd397,0xbd16c32c,0x3f7f9c18,0xbd621469,0x3f7f0e58,0xbdafb680,0x3f7de0b1,0xbe039502,0x3f7dabcc,0xbe09cf86,0x3f7ac516,0xbe4dee60,0x3f7baccd,0xbe3b6ecf,0x3f764d97,0xbe8b9507,
0x3f791298,0xbe6c9a7f,0x3f708066,0xbeaf713a,0x3f75dec6,0xbe8e9a22,0x3f696591,0xbed25a09,0x3f721352,0xbea68f12,0x3f6106f2,0xbef41f07,0x3f6db293,0xbebe1d4a,0x3f577026,0xbf0a48ad,
0x3f68bf3c,0xbed53641,0x3f4cae79,0xbf19c200,0x3f633c5a,0xbeebcbbb,0x3f40d0da,0xbf286605,0x3f5d2d53,0xbf00e7e4,0x3f33e7bc,0xbf36206c,0x3f5695e5,0xbf0b9a6b,0x3f26050a,0xbf42de29,
0x3f4f7a1f,0xbf15f6d9,0x3f173c07,0xbf4e8d90,0x3f47de65,0xbf1ff6cb,0x3f07a136,0xbf591e6a,0x3f3fc767,0xbf299415,0x3eee9479,0xbf628210,0x3f373a23,0xbf32c8c9,0x3ecc9b8b,0xbf6aab7b,
0x3f7e70b0,0xbde1bc2e,0x3f7de0b1,0xbe039502,0x3f778bc5,0xbe827dc0,0x3f748422,0xbe97a117,0x3f6b4b0c,0xbec9b953,0x3f63f473,0xbee900b7,0x3f59f26a,0xbf064b82,0x3f4cae79,0xbf19c200,
0x3f43e200,0xbf24d225,0x3f2f61a5,0xbf3a7ca4,0x3f299415,0xbf3fc767,0x3f0cead0,0xbf55b993,0x3f0b9a6b,0xbf5695e5,0x3ecc9b8b,0xbf6aab7b,0x3ed53641,0xbf68bf3c,0x3e72b651,0xbf78b47b,
0x3e8e9a22,0xbf75dec6,0x3d8a200a,0xbf7f6ac7,0x3e09cf86,0xbf7dabcc,0xbdd53db9,0xbf7e9bc9,0xbc490e90,0xbf7ffb11,0xbe8b9507,0xbf764d97,0xbe22abb6,0xbf7cbfc9,0xbeddbe79,0xbf66becc,
0xbe9aa086,0xbf740bdd,0xbf14b039,0xbf5064af,0xbee0924f,0xbf660f88,0xbf36206c,0xbf33e7bc,0xbf10d3cd,0xbf531849,0xbf5233c6,0xbf121eb0,0xbf2e3bde,0xbf3b8f3b,0xbf6816a8,0xbed8106b,
0x3f7baccd,0xbe3b6ecf,0x3f7ac516,0xbe4dee60,0x3f68bf3c,0xbed53641,0x3f63f473,0xbee900b7,0x3f47de65,0xbf1ff6cb,0x3f3ca003,0xbf2d1469,0x3f1b02c6,0xbf4bbbf8,0x3f07a136,0xbf591e6a,
0x3ec9b953,0xbf6b4b0c,0x3e919ddd,0xbf756d97,0x3e22abb6,0xbf7cbfc9,0x3c96c9b6,0xbf7ff4e6,0xbdafb680,0xbf7f0e58,0xbe7ee6e1,0xbf77f110,0xbea68f12,0xbf721352,0xbeff17b2,0xbf5df6be,
0xbf0b9a6b,0xbf5695e5,0xbf36206c,0xbf33e7bc,0xbf3b8f3b,0xbf2e3bde,0xbf5f8327,0xbef9a02d,0xbf604621,0xbef6e0cb,0xbf78b47b,0xbe72b651,0xbf778bc5,0xbe827dc0,0xbf7fe129,0x3cfb49ba,
0xbf7ffb11,0xbc490e90,0xbf748422,0x3e97a117,0xbf791298,0x3e6c9a7f,0xbf577026,0x3f0a48ad,0xbf633c5a,0x3eebcbbb,0xbf2ac082,0x3f3ebc1b,0xbf3fc767,0x3f299415,0xbee363fa,0x3f655e0b,
0x3f778bc5,0xbe827dc0,0x3f764d97,0xbe8b9507,0x3f531849,0xbf10d3cd,0x3f4cae79,0xbf19c200,0x3f15f6d9,0xbf4f7a1f,0x3f07a136,0xbf591e6a,0x3e8e9a22,0xbf75dec6,0x3e419b37,0xbf7b61fc,
0xbd7b2b74,0xbf7f84ab,0xbe354098,0xbf7bf531,0xbec9b953,0xbf6b4b0c,0xbf04f484,0xbf5ac450,0xbf2e3bde,0xbf3b8f3b,0xbf4ac77f,0xbf1c420c,0xbf633c5a,0xbeebcbbb,0xbf756d97,0xbe919ddd,
0xbf7dabcc,0xbe09cf86,0xbf7f2f9d,0x3da3308c,0xbf7a7302,0x3e541501,0xbf66becc,0x3eddbe79,0xbf59f26a,0x3f064b82,0xbf2f61a5,0x3f3a7ca4,0xbf1ff6cb,0x3f47de65,0xbec1071e,0x3f6d1c1d,
0xbea68f12,0x3f721352,0xbc96c9b6,0x3f7ff4e6,0x3c490e90,0x3f7ffb11,0x3eaf713a,0x3f708066,0x3eb263ef,0x3f6ff573,0x3f286605,0x3f40d0da,0x3f24d225,0x3f43e200,0x3f628210,0x3eee9479,
0x3f800000,0x0,0x3f800000,0x0,0x3f7ff4e6,0xbc96c9b6,0x3f7fc38f,0xbd2fe007,0x3f7fd397,0xbd16c32c,0x3f7f0e58,0xbdafb680,0x3f7f9c18,0xbd621469,0x3f7de0b1,0xbe039502,
0x3f7f4e6d,0xbd96a905,0x3f7c3b28,0xbe2f10a2,0x3f7eea9d,0xbdbc3ac3,0x3f7a1e84,0xbe5a3997,0x3f7e70b0,0xbde1bc2e,0x3f778bc5,0xbe827dc0,0x3f7de0b1,0xbe039502,0x3f748422,0xbe97a117,
0x3f7d3aac,0xbe164083,0x3f710908,0xbeac7cd4,0x3f7c7eb0,0xbe28defc,0x3f6d1c1d,0xbec1071e,0x3f7baccd,0xbe3b6ecf,0x3f68bf3c,0xbed53641,0x3f7ac516,0xbe4dee60,0x3f63f473,0xbee900b7,
0x3f79c79d,0xbe605c13,0x3f5ebe05,0xbefc5d27,0x3f78b47b,0xbe72b651,0x3f591e6a,0xbf07a136,0x3f778bc5,0xbe827dc0,0x3f531849,0xbf10d3cd,0x3f764d97,0xbe8b9507,0x3f4cae79,0xbf19c200,
0x3f800000,0x0,0x3f800000,0x0,0x3f7e4323,0xbdee3876,0x3f7d7474,0xbe1008b7,0x3f791298,0xbe6c9a7f,0x3f75dec6,0xbe8e9a22,0x3f708066,0xbeaf713a,0x3f696591,0xbed25a09,
0x3f64aa59,0xbee63375,0x3f584853,0xbf08f59b,0x3f55b993,0xbf0cead0,0x3f42de29,0xbf26050a,0x3f43e200,0xbf24d225,0x3f299415,0xbf3fc767,0x3f2f61a5,0xbf3a7ca4,0x3f0cead0,0xbf55b993,
0x3f187fc0,0xbf4d9f02,0x3edae880,0xbf676bd8,0x3eff17b2,0xbf5df6be,0x3e97a117,0xbf748422,0x3ec9b953,0xbf6b4b0c,0x3e22abb6,0xbf7cbfc9,0x3e919ddd,0xbf756d97,0x3c96c9b6,0xbf7ff4e6,
0x3e2f10a2,0xbf7c3b28,0xbdfab273,0xbf7e1324,0x3d621469,0xbf7f9c18,0xbe8586ce,0xbf772417,0xbd7b2b74,0xbf7f84ab,0xbec9b953,0xbf6b4b0c,0xbe354098,0xbf7bf531,0xbf04f484,0xbf5ac450,
0x3f800000,0x0,0x3f800000,0x0,0x3f7a1e84,0xbe5a3997,0x3f78b47b,0xbe72b651,0x3f68bf3c,0xbed53641,0x3f633c5a,0xbeebcbbb,0x3f4cae79,0xbf19c200,0x3f40d0da,0xbf286605,
0x3f273656,0xbf41d870,0x3f13682a,0xbf514d3d,0x3ef41f07,0xbf6106f2,0x3ebb31a0,0xbf6e46be,0x3e8e9a22,0xbf75dec6,0x3e09cf86,0xbf7dabcc,0x3d8a200a,0xbf7f6ac7,0xbdd53db9,0xbf7e9bc9,
0xbe164083,0xbf7d3aac,0xbeac7cd4,0xbf710908,0xbeb554ec,0xbf6f6830,0xbf0cead0,0xbf55b993,0xbf0b9a6b,0xbf5695e5,0xbf3b8f3b,0xbf2e3bde,0xbf36206c,0xbf33e7bc,0xbf5f8327,0xbef9a02d,
0xbf584853,0xbf08f59b,0xbf76ba07,0xbe888e93,0xbf708066,0xbeaf713a,0xbf7fe129,0xbcfb49ba,0xbf7dabcc,0xbe09cf86,0xbf7a7302,0x3e541501,0xbf7f2f9d,0x3da3308c,0xbf66becc,0x3eddbe79,
0x3f800000,0x0,0x3f800000,0x0,0x3f73913f,0xbe9d9e78,0x3f718f57,0xbea986c4,0x3f4f7a1f,0xbf15f6d9,0x3f47de65,0xbf1ff6cb,0x3f173c07,0xbf4e8d90,0x3f07a136,0xbf591e6a,
0x3ea09ae5,0xbf731447,0x3e605c13,0xbf79c79d,0x3bc90f88,0xbf7ffec4,0xbdee3876,0xbf7e4323,0xbe9aa086,0xbf740bdd,0xbee0924f,0xbf660f88,0xbf14b039,0xbf5064af,0xbf36206c,0xbf33e7bc,
0xbf4d9f02,0xbf187fc0,0xbf676bd8,0xbedae880,0xbf7294f8,0xbea395c5,0xbf7e9bc9,0xbdd53db9,0xbf7ffb11,0xbc490e90,0xbf791298,0x3e6c9a7f,0xbf748422,0x3e97a117,0xbf577026,0x3f0a48ad,
0xbf514d3d,0x3f13682a,0xbf1d7fd1,0x3f49d112,0xbf19c200,0x3f4cae79,0xbea395c5,0x3f7294f8,0xbea68f12,0x3f721352,0x3c490e90,0x3f7ffb11,0xbc96c9b6,0x3f7ff4e6,0x3eaf713a,0x3f708066,
0x3f800000,0x0,0x3f800000,0x0,0x3f7f6ac7,0xbd8a200a,0x3f7eea9d,0xbdbc3ac3,0x3f7dabcc,0xbe09cf86,0x3f7baccd,0xbe3b6ecf,0x3f7ac516,0xbe4dee60,0x3f764d97,0xbe8b9507,
0x3f76ba07,0xbe888e93,0x3f6ed89e,0xbeb8442a,0x3f718f57,0xbea986c4,0x3f655e0b,0xbee363fa,0x3f6b4b0c,0xbec9b953,0x3f59f26a,0xbf064b82,0x3f63f473,0xbee900b7,0x3f4cae79,0xbf19c200,
0x3f5b941a,0xbf039c3d,0x3f3daef9,0xbf2beb4a,0x3f5233c6,0xbf121eb0,0x3f2d1469,0xbf3ca003,0x3f47de65,0xbf1ff6cb,0x3f1b02c6,0xbf4bbbf8,0x3f3ca003,0xbf2d1469,0x3f07a136,0xbf591e6a,
0x3f3085bb,0xbf396842,0x3ee63375,0xbf64aa59,0x3f239da9,0xbf44e3f5,0x3ebb31a0,0xbf6e46be,0x3f15f6d9,0xbf4f7a1f,0x3e8e9a22,0xbf75dec6,0x3f07a136,0xbf591e6a,0x3e419b37,0xbf7b61fc,
0x3f800000,0x0,0x3f800000,0x0,0x3f7c7eb0,0xbe28defc,0x3f7b61fc,0xbe419b37,0x3f721352,0xbea68f12,0x3f6db293,0xbebe1d4a,0x3f6106f2,0xbef41f07,0x3f577026,0xbf0a48ad,
0x3f49d112,0xbf1d7fd1,0x3f396842,0xbf3085bb,0x3f2d1469,0xbf3ca003,0x3f14b039,0xbf5064af,0x3f0b9a6b,0xbf5695e5,0x3ed53641,0xbf68bf3c,0x3ecc9b8b,0xbf6aab7b,0x3e72b651,0xbf78b47b,
0x3e78cfcc,0xbf7853f8,0x3d48fb30,0xbf7fb10f,0x3da3308c,0xbf7f2f9d,0xbe1008b7,0xbf7d7474,0xbdafb680,0xbf7f0e58,0xbea68f12,0xbf721352,0xbe7ee6e1,0xbf77f110,0xbeff17b2,0xbf5df6be,
0xbecf7bca,0xbf6a09a7,0xbf273656,0xbf41d870,0xbf0cead0,0xbf55b993,0xbf48d8b3,0xbf1ebc12,0xbf2e3bde,0xbf3b8f3b,0xbf633c5a,0xbeebcbbb,0xbf4ac77f,0xbf1c420c,0xbf756d97,0xbe919ddd,
0x3f800000,0x0,0x3f800000,0x0,0x3f772417,0xbe8586ce,0x3f756d97,0xbe919ddd,0x3f5d2d53,0xbf00e7e4,0x3f5695e5,0xbf0b9a6b,0x3f33e7bc,0xbf36206c,0x3f26050a,0xbf42de29,
0x3efc5d27,0xbf5ebe05,0x3ecf7bca,0xbf6a09a7,0x3e7ee6e1,0xbf77f110,0x3e039502,0xbf7de0b1,0xbc490e90,0xbf7ffb11,0xbe22abb6,0xbf7cbfc9,0xbe8b9507,0xbf764d97,0xbeddbe79,0xbf66becc,
0xbf039c3d,0xbf5b941a,0xbf2beb4a,0xbf3daef9,0xbf385216,0xbf31a81d,0xbf5ac450,0xbf04f484,0xbf604621,0xbef6e0cb,0xbf778bc5,0xbe827dc0,0xbf78b47b,0xbe72b651,0xbf7fe129,0x3cfb49ba,
0xbf7fec43,0x3cc90ab0,0xbf731447,0x3ea09ae5,0xbf756d97,0x3e919ddd,0xbf5233c6,0x3f121eb0,0xbf59f26a,0x3f064b82,0xbf1ff6cb,0x3f47de65,0xbf2f61a5,0x3f3a7ca4,0xbec1071e,0x3f6d1c1d,
0x3f800000,0x0,0x3f800000,0x0,0x3f6f6830,0xbeb554ec,0x3f6d1c1d,0xbec1071e,0x3f3fc767,0xbf299415,0x3f373a23,0xbf32c8c9,0x3eee9479,0xbf628210,0x3ecc9b8b,0xbf6aab7b,
0x3dfab273,0xbf7e1324,0x3cc90ab0,0xbf7fec43,0xbe72b651,0xbf78b47b,0xbeb554ec,0xbf6f6830,0xbf10d3cd,0xbf531849,0xbf2e3bde,0xbf3b8f3b,0xbf5233c6,0xbf121eb0,0xbf6816a8,0xbed8106b,
0xbf7853f8,0xbe78cfcc,0xbf7fb10f,0xbd48fb30,0xbf7e4323,0x3dee3876,0xbf718f57,0x3ea986c4,0xbf633c5a,0x3eebcbbb,0xbf3fc767,0x3f299415,0xbf2ac082,0x3f3ebc1b,0xbee363fa,0x3f655e0b,
0xbeb8442a,0x3f6ed89e,0xbd96a905,0x3f7f4e6d,0xbbc90f88,0x3f7ffec4,0x3e9d9e78,0x3f73913f,0x3eb263ef,0x3f6ff573,0x3f24d225,0x3f43e200,0x3f286605,0x3f40d0da,0x3f628210,0x3eee9479,
0x80002c00,0x25642b88,0x29132a2f,0x2add281d,0x80002c00,0x295f29ed,0x2bf61e46,0x2a6da8c4,0x80002c00,0x2b3b26d7,0x2a2fa913,0xa3c6abc3,0x80002c00,0x2bfe1a48,0x1e46abf6,0xabeaa0b2,
0x80002c00,0x2b88a564,0xa913aa2f,0xa81d2add,0x80002c00,0x29eda95f,0xabf69e46,0x28c42a6d,0x80002c00,0x26d7ab3b,0xaa2f2913,0x2bc3a3c6,0x80002c00,0x1a48abfe,0x9e462bf6,0x20b2abea,
0x80002c00,0xa564ab88,0x29132a2f,0xaadda81d,0x80002c00,0xa95fa9ed,0x2bf61e46,0xaa6d28c4,0x80002c00,0xab3ba6d7,0x2a2fa913,0x23c62bc3,0x80002c00,0xabfe9a48,0x1e46abf6,0x2bea20b2,
0x80002c00,0xab882564,0xa913aa2f,0x281daadd,0x80002c00,0xa9ed295f,0xabf69e46,0xa8c4aa6d,0x80002c00,0xa6d72b3b,0xaa2f2913,0xabc323c6,0x80002c00,0x9a482bfe,0x9e462bf6,0xa0b22bea,
0x2bd9223e,0x2beaa0b2,0x2b0ea78b,0x295fa9ed,0x223eabd9,0xa81daadd,0xaba8a4a5,0xab3b26d7,0xabd9a23e,0xa8c42a6d,0x278b2b0e,0x2bfe9a48,0xa23e2bd9,0x2bc323c6,0x24a5aba8,0xab88a564,
0x2bd9223e,0xa0b2abea,0xab0e278b,0x29ed295f,0x223eabd9,0xaadd281d,0x2ba824a5,0xa6d7ab3b,0xabd9a23e,0x2a6d28c4,0xa78bab0e,0x1a482bfe,0xa23e2bd9,0x23c6abc3,0xa4a52ba8,0x2564ab88,
0x2bd9223e,0xabea20b2,0x2b0ea78b,0xa95f29ed,0x223eabd9,0x281d2add,0xaba8a4a5,0x2b3ba6d7,0xabd9a23e,0x28c4aa6d,0x278b2b0e,0xabfe1a48,0xa23e2bd9,0xabc3a3c6,0x24a5aba8,0x2b882564,
0x2bd9223e,0x20b22bea,0xab0e278b,0xa9eda95f,0x223eabd9,0x2adda81d,0x2ba824a5,0x26d72b3b,0xabd9a23e,0xaa6da8c4,0xa78bab0e,0x9a48abfe,0xa23e2bd9,0xa3c62bc3,0xa4a52ba8,0xa5642b88,
0x261fab64,0x1a48abfe,0xa4a5aba8,0xa8c4aa6d,0xa61f2b64,0x25642b88,0x2b0e278b,0x2bc3a3c6,0x261fab64,0xa95fa9ed,0xaba824a5,0xa0b22bea,0xa61f2b64,0x2b3b26d7,0x278bab0e,0xaadda81d,
0x261fab64,0xabfe9a48,0x24a52ba8,0x2a6da8c4,0xa61f2b64,0x2b88a564,0xab0ea78b,0x23c62bc3,0x261fab64,0xa9ed295f,0x2ba8a4a5,0xabeaa0b2,0xa61f2b64,0x26d7ab3b,0xa78b2b0e,0x281daadd,
0x261fab64,0x9a482bfe,0xa4a5aba8,0x28c42a6d,0xa61f2b64,0xa564ab88,0x2b0e278b,0xabc323c6,0x261fab64,0x295f29ed,0xaba824a5,0x20b2abea,0xa61f2b64,0xab3ba6d7,0x278bab0e,0x2add281d,
0x261fab64,0x2bfe1a48,0x24a52ba8,0xaa6d28c4,0xa61f2b64,0xab882564,0xab0ea78b,0xa3c6abc3,0x261fab64,0x29eda95f,0x2ba8a4a5,0x2bea20b2,0xa61f2b64,0xa6d72b3b,0xa78b2b0e,0xa81d2add,
0xaaa7a872,0xabc3a3c6,0xabf61e46,0xab3b26d7,0x2872aaa7,0xa0b2abea,0xaa2fa913,0xabfe1a48,0x2aa72872,0x2adda81d,0x9e46abf6,0xab88a564,0xa8722aa7,0x2a6d28c4,0x2913aa2f,0xa9eda95f,
0xaaa7a872,0xa3c62bc3,0x2bf69e46,0xa6d7ab3b,0x2872aaa7,0xabea20b2,0x2a2f2913,0x9a48abfe,0x2aa72872,0xa81daadd,0x1e462bf6,0x2564ab88,0xa8722aa7,0x28c4aa6d,0xa9132a2f,0x295fa9ed,
0xaaa7a872,0x2bc323c6,0xabf61e46,0x2b3ba6d7,0x2872aaa7,0x20b22bea,0xaa2fa913,0x2bfe9a48,0x2aa72872,0xaadd281d,0x9e46abf6,0x2b882564,0xa8722aa7,0xaa6da8c4,0x2913aa2f,0x29ed295f,
0xaaa7a872,0x23c6abc3,0x2bf69e46,0x26d72b3b,0x2872aaa7,0x2beaa0b2,0x2a2f2913,0x1a482bfe,0x2aa72872,0x281d2add,0x1e462bf6,0xa5642b88,0xa8722aa7,0xa8c42a6d,0xa9132a2f,0xa95f29ed,
0x2c000000,0x2b88a564,0x2a2fa913,0x281daadd,0x2c000000,0x29eda95f,0x1e46abf6,0xa8c4aa6d,0x2c000000,0x26d7ab3b,0xa913aa2f,0xabc323c6,0x2c000000,0x1a48abfe,0xabf69e46,0xa0b22bea,
0x2c000000,0xa564ab88,0xaa2f2913,0x2add281d,0x2c000000,0xa95fa9ed,0x9e462bf6,0x2a6da8c4,0x2c000000,0xab3ba6d7,0x29132a2f,0xa3c6abc3,0x2c000000,0xabfe9a48,0x2bf61e46,0xabeaa0b2,
0x2c000000,0xab882564,0x2a2fa913,0xa81d2add,0x2c000000,0xa9ed295f,0x1e46abf6,0x28c42a6d,0x2c000000,0xa6d72b3b,0xa913aa2f,0x2bc3a3c6,0x2c000000,0x9a482bfe,0xabf69e46,0x20b2abea,
0x2c000000,0x25642b88,0xaa2f2913,0xaadda81d,0x2c000000,0x295f29ed,0x9e462bf6,0xaa6d28c4,0x2c000000,0x2b3b26d7,0x29132a2f,0x23c62bc3,0x2c000000,0x2bfe1a48,0x2bf61e46,0x2bea20b2,
0x223eabd9,0xa0b2abea,0xa78bab0e,0xa9eda95f,0xabd9a23e,0xaadd281d,0xa4a52ba8,0x26d72b3b,0xa23e2bd9,0x2a6d28c4,0x2b0ea78b,0x9a48abfe,0x2bd9223e,0x23c6abc3,0xaba8a4a5,0xa5642b88,
0x223eabd9,0xabea20b2,0x278b2b0e,0x295fa9ed,0xabd9a23e,0x281d2add,0x24a5aba8,0xab3b26d7,0xa23e2bd9,0x28c4aa6d,0xab0e278b,0x2bfe9a48,0x2bd9223e,0xabc3a3c6,0x2ba824a5,0xab88a564,
0x223eabd9,0x20b22bea,0xa78bab0e,0x29ed295f,0xabd9a23e,0x2adda81d,0xa4a52ba8,0xa6d7ab3b,0xa23e2bd9,0xaa6da8c4,0x2b0ea78b,0x1a482bfe,0x2bd9223e,0xa3c62bc3,0xaba8a4a5,0x2564ab88,
0x223eabd9,0x2beaa0b2,0x278b2b0e,0xa95f29ed,0xabd9a23e,0xa81daadd,0x24a5aba8,0x2b3ba6d7,0xa23e2bd9,0xa8c42a6d,0xab0e278b,0xabfe1a48,0x2bd9223e,0x2bc323c6,0x2ba824a5,0x2b882564,
0xab64a61f,0xabfe9a48,0xaba824a5,0xaa6d28c4,0x2b64261f,0x2b88a564,0x278bab0e,0xa3c6abc3,0xab64a61f,0xa9ed295f,0x24a52ba8,0x2bea20b2,0x2b64261f,0x26d7ab3b,0xab0ea78b,0xa81d2add,
0xab64a61f,0x9a482bfe,0x2ba8a4a5,0xa8c4aa6d,0x2b64261f,0xa564ab88,0xa78b2b0e,0x2bc3a3c6,0xab64a61f,0x295f29ed,0xa4a5aba8,0xa0b22bea,0x2b64261f,0xab3ba6d7,0x2b0e278b,0xaadda81d,
0xab64a61f,0x2bfe1a48,0xaba824a5,0x2a6da8c4,0x2b64261f,0xab882564,0x278bab0e,0x23c62bc3,0xab64a61f,0x29eda95f,0x24a52ba8,0xabeaa0b2,0x2b64261f,0xa6d72b3b,0xab0ea78b,0x281daadd,
0xab64a61f,0x1a48abfe,0x2ba8a4a5,0x28c42a6d,0x2b64261f,0x25642b88,0xa78b2b0e,0xabc323c6,0xab64a61f,0xa95fa9ed,0xa4a5aba8,0x20b2abea,0x2b64261f,0x2b3b26d7,0x2b0e278b,0x2add281d,
0xa8722aa7,0xa3c62bc3,0x1e462bf6,0x26d72b3b,0xaaa7a872,0xabea20b2,0xa9132a2f,0x1a482bfe,0x2872aaa7,0xa81daadd,0xabf61e46,0xa5642b88,0x2aa72872,0x28c4aa6d,0xaa2fa913,0xa95f29ed,
0xa8722aa7,0x2bc323c6,0x9e46abf6,0xab3b26d7,0xaaa7a872,0x20b22bea,0x2913aa2f,0xabfe1a48,0x2872aaa7,0xaadd281d,0x2bf69e46,0xab88a564,0x2aa72872,0xaa6da8c4,0x2a2f2913,0xa9eda95f,
0xa8722aa7,0x23c6abc3,0x1e462bf6,0xa6d7ab3b,0xaaa7a872,0x2beaa0b2,0xa9132a2f,0x9a48abfe,0x2872aaa7,0x281d2add,0xabf61e46,0x2564ab88,0x2aa72872,0xa8c42a6d,0xaa2fa913,0x295fa9ed,
0xa8722aa7,0xabc3a3c6,0x9e46abf6,0x2b3ba6d7,0xaaa7a872,0xa0b2abea,0x2913aa2f,0x2bfe9a48,0x2872aaa7,0x2adda81d,0x2bf69e46,0x2b882564,0x2aa72872,0x2a6d28c4,0x2a2f2913,0x29ed295f,
0x80002c00,0x80002c00,0x80002c00,0x80002c00,0x25642b88,0x295f29ed,0x2b3b26d7,0x2bfe1a48,0x29132a2f,0x2bf61e46,0x2a2fa913,0x1e46abf6,0x2add281d,0x2a6da8c4,0xa3c6abc3,0xabeaa0b2,
0x2bd9223e,0x223eabd9,0xabd9a23e,0xa23e2bd9,0x2beaa0b2,0xa81daadd,0xa8c42a6d,0x2bc323c6,0x2b0ea78b,0xaba8a4a5,0x278b2b0e,0x24a5aba8,0x295fa9ed,0xab3b26d7,0x2bfe9a48,0xab88a564,
0x261fab64,0xa61f2b64,0x261fab64,0xa61f2b64,0x1a48abfe,0x25642b88,0xa95fa9ed,0x2b3b26d7,0xa4a5aba8,0x2b0e278b,0xaba824a5,0x278bab0e,0xa8c4aa6d,0x2bc3a3c6,0xa0b22bea,0xaadda81d,
0xaaa7a872,0x2872aaa7,0x2aa72872,0xa8722aa7,0xabc3a3c6,0xa0b2abea,0x2adda81d,0x2a6d28c4,0xabf61e46,0xaa2fa913,0x9e46abf6,0x2913aa2f,0xab3b26d7,0xabfe1a48,0xab88a564,0xa9eda95f,
0x80002c00,0x80002c00,0x80002c00,0x80002c00,0x2b88a564,0x29eda95f,0x26d7ab3b,0x1a48abfe,0xa913aa2f,0xabf69e46,0xaa2f2913,0x9e462bf6,0xa81d2add,0x28c42a6d,0x2bc3a3c6,0x20b2abea,
0x2bd9223e,0x223eabd9,0xabd9a23e,0xa23e2bd9,0xa0b2abea,0xaadd281d,0x2a6d28c4,0x23c6abc3,0xab0e278b,0x2ba824a5,0xa78bab0e,0xa4a52ba8,0x29ed295f,0xa6d7ab3b,0x1a482bfe,0x2564ab88,
0x261fab64,0xa61f2b64,0x261fab64,0xa61f2b64,0xabfe9a48,0x2b88a564,0xa9ed295f,0x26d7ab3b,0x24a52ba8,0xab0ea78b,0x2ba8a4a5,0xa78b2b0e,0x2a6da8c4,0x23c62bc3,0xabeaa0b2,0x281daadd,
0xaaa7a872,0x2872aaa7,0x2aa72872,0xa8722aa7,0xa3c62bc3,0xabea20b2,0xa81daadd,0x28c4aa6d,0x2bf69e46,0x2a2f2913,0x1e462bf6,0xa9132a2f,0xa6d7ab3b,0x9a48abfe,0x2564ab88,0x295fa9ed,
0x80002c00,0x80002c00,0x80002c00,0x80002c00,0xa564ab88,0xa95fa9ed,0xab3ba6d7,0xabfe9a48,0x29132a2f,0x2bf61e46,0x2a2fa913,0x1e46abf6,0xaadda81d,0xaa6d28c4,0x23c62bc3,0x2bea20b2,
0x2bd9223e,0x223eabd9,0xabd9a23e,0xa23e2bd9,0xabea20b2,0x281d2add,0x28c4aa6d,0xabc3a3c6,0x2b0ea78b,0xaba8a4a5,0x278b2b0e,0x24a5aba8,0xa95f29ed,0x2b3ba6d7,0xabfe1a48,0x2b882564,
0x261fab64,0xa61f2b64,0x261fab64,0xa61f2b64,0x9a482bfe,0xa564ab88,0x295f29ed,0xab3ba6d7,0xa4a5aba8,0x2b0e278b,0xaba824a5,0x278bab0e,0x28c42a6d,0xabc323c6,0x20b2abea,0x2add281d,
0xaaa7a872,0x2872aaa7,0x2aa72872,0xa8722aa7,0x2bc323c6,0x20b22bea,0xaadd281d,0xaa6da8c4,0xabf61e46,0xaa2fa913,0x9e46abf6,0x2913aa2f,0x2b3ba6d7,0x2bfe9a48,0x2b882564,0x29ed295f,
0x80002c00,0x80002c00,0x80002c00,0x80002c00,0xab882564,0xa9ed295f,0xa6d72b3b,0x9a482bfe,0xa913aa2f,0xabf69e46,0xaa2f2913,0x9e462bf6,0x281daadd,0xa8c4aa6d,0xabc323c6,0xa0b22bea,
0x2bd9223e,0x223eabd9,0xabd9a23e,0xa23e2bd9,0x20b22bea,0x2adda81d,0xaa6da8c4,0xa3c62bc3,0xab0e278b,0x2ba824a5,0xa78bab0e,0xa4a52ba8,0xa9eda95f,0x26d72b3b,0x9a48abfe,0xa5642b88,
0x261fab64,0xa61f2b64,0x261fab64,0xa61f2b64,0x2bfe1a48,0xab882564,0x29eda95f,0xa6d72b3b,0x24a52ba8,0xab0ea78b,0x2ba8a4a5,0xa78b2b0e,0xaa6d28c4,0xa3c6abc3,0x2bea20b2,0xa81d2add,
0xaaa7a872,0x2872aaa7,0x2aa72872,0xa8722aa7,0x23c6abc3,0x2beaa0b2,0x281d2add,0xa8c42a6d,0x2bf69e46,0x2a2f2913,0x1e462bf6,0xa9132a2f,0x26d72b3b,0x1a482bfe,0xa5642b88,0xa95f29ed,
0x2c000000,0x2c000000,0x2c000000,0x2c000000,0x2b88a564,0x29eda95f,0x26d7ab3b,0x1a48abfe,0x2a2fa913,0x1e46abf6,0xa913aa2f,0xabf69e46,0x281daadd,0xa8c4aa6d,0xabc323c6,0xa0b22bea,
0x223eabd9,0xabd9a23e,0xa23e2bd9,0x2bd9223e,0xa0b2abea,0xaadd281d,0x2a6d28c4,0x23c6abc3,0xa78bab0e,0xa4a52ba8,0x2b0ea78b,0xaba8a4a5,0xa9eda95f,0x26d72b3b,0x9a48abfe,0xa5642b88,
0xab64a61f,0x2b64261f,0xab64a61f,0x2b64261f,0xabfe9a48,0x2b88a564,0xa9ed295f,0x26d7ab3b,0xaba824a5,0x278bab0e,0x24a52ba8,0xab0ea78b,0xaa6d28c4,0xa3c6abc3,0x2bea20b2,0xa81d2add,
0xa8722aa7,0xaaa7a872,0x2872aaa7,0x2aa72872,0xa3c62bc3,0xabea20b2,0xa81daadd,0x28c4aa6d,0x1e462bf6,0xa9132a2f,0xabf61e46,0xaa2fa913,0x26d72b3b,0x1a482bfe,0xa5642b88,0xa95f29ed,
0x2c000000,0x2c000000,0x2c000000,0x2c000000,0xa564ab88,0xa95fa9ed,0xab3ba6d7,0xabfe9a48,0xaa2f2913,0x9e462bf6,0x29132a2f,0x2bf61e46,0x2add281d,0x2a6da8c4,0xa3c6abc3,0xabeaa0b2,
0x223eabd9,0xabd9a23e,0xa23e2bd9,0x2bd9223e,0xabea20b2,0x281d2add,0x28c4aa6d,0xabc3a3c6,0x278b2b0e,0x24a5aba8,0xab0e278b,0x2ba824a5,0x295fa9ed,0xab3b26d7,0x2bfe9a48,0xab88a564,
0xab64a61f,0x2b64261f,0xab64a61f,0x2b64261f,0x9a482bfe,0xa564ab88,0x295f29ed,0xab3ba6d7,0x2ba8a4a5,0xa78b2b0e,0xa4a5aba8,0x2b0e278b,0xa8c4aa6d,0x2bc3a3c6,0xa0b22bea,0xaadda81d,
0xa8722aa7,0xaaa7a872,0x2872aaa7,0x2aa72872,0x2bc323c6,0x20b22bea,0xaadd281d,0xaa6da8c4,0x9e46abf6,0x2913aa2f,0x2bf69e46,0x2a2f2913,0xab3b26d7,0xabfe1a48,0xab88a564,0xa9eda95f,
0x2c000000,0x2c000000,0x2c000000,0x2c000000,0xab882564,0xa9ed295f,0xa6d72b3b,0x9a482bfe,0x2a2fa913,0x1e46abf6,0xa913aa2f,0xabf69e46,0xa81d2add,0x28c42a6d,0x2bc3a3c6,0x20b2abea,
0x223eabd9,0xabd9a23e,0xa23e2bd9,0x2bd9223e,0x20b22bea,0x2adda81d,0xaa6da8c4,0xa3c62bc3,0xa78bab0e,0xa4a52ba8,0x2b0ea78b,0xaba8a4a5,0x29ed295f,0xa6d7ab3b,0x1a482bfe,0x2564ab88,
0xab64a61f,0x2b64261f,0xab64a61f,0x2b64261f,0x2bfe1a48,0xab882564,0x29eda95f,0xa6d72b3b,0xaba824a5,0x278bab0e,0x24a52ba8,0xab0ea78b,0x2a6da8c4,0x23c62bc3,0xabeaa0b2,0x281daadd,
0xa8722aa7,0xaaa7a872,0x2872aaa7,0x2aa72872,0x23c6abc3,0x2beaa0b2,0x281d2add,0xa8c42a6d,0x1e462bf6,0xa9132a2f,0xabf61e46,0xaa2fa913,0xa6d7ab3b,0x9a48abfe,0x2564ab88,0x295fa9ed,
0x2c000000,0x2c000000,0x2c000000,0x2c000000,0x25642b88,0x295f29ed,0x2b3b26d7,0x2bfe1a48,0xaa2f2913,0x9e462bf6,0x29132a2f,0x2bf61e46,0xaadda81d,0xaa6d28c4,0x23c62bc3,0x2bea20b2,
0x223eabd9,0xabd9a23e,0xa23e2bd9,0x2bd9223e,0x2beaa0b2,0xa81daadd,0xa8c42a6d,0x2bc323c6,0x278b2b0e,0x24a5aba8,0xab0e278b,0x2ba824a5,0xa95f29ed,0x2b3ba6d7,0xabfe1a48,0x2b882564,
0xab64a61f,0x2b64261f,0xab64a61f,0x2b64261f,0x1a48abfe,0x25642b88,0xa95fa9ed,0x2b3b26d7,0x2ba8a4a5,0xa78b2b0e,0xa4a5aba8,0x2b0e278b,0x28c42a6d,0xabc323c6,0x20b2abea,0x2add281d,
0xa8722aa7,0xaaa7a872,0x2872aaa7,0x2aa72872,0xabc3a3c6,0xa0b2abea,0x2adda81d,0x2a6d28c4,0x9e46abf6,0x2913aa2f,0x2bf69e46,0x2a2f2913,0x2b3ba6d7,0x2bfe9a48,0x2b882564,0x29ed295f,
0x80002c00,0x80002c00,0x80002c00,0x80002c00,0x80002c00,0x261f2b64,0x29a829a8,0x2b64261f,0x80002c00,0x29a829a8,0x2c000000,0x29a8a9a8,0x80002c00,0x2b64261f,0x29a8a9a8,0xa61fab64,
0x80002c00,0x2c000000,0x8000ac00,0xac000000,0x80002c00,0x2b64a61f,0xa9a8a9a8,0xa61f2b64,0x80002c00,0x29a8a9a8,0xac000000,0x29a829a8,0x80002c00,0x261fab64,0xa9a829a8,0x2b64a61f,
0x80002c00,0x8000ac00,0x80002c00,0x8000ac00,0x80002c00,0xa61fab64,0x29a829a8,0xab64a61f,0x80002c00,0xa9a8a9a8,0x2c000000,0xa9a829a8,0x80002c00,0xab64a61f,0x29a8a9a8,0x261f2b64,
0x80002c00,0xac000000,0x8000ac00,0x2c000000,0x80002c00,0xab64261f,0xa9a8a9a8,0x261fab64,0x80002c00,0xa9a829a8,0xac000000,0xa9a8a9a8,0x80002c00,0xa61f2b64,0xa9a829a8,0xab64261f,
0x80002c00,0x80002c00,0x80002c00,0x80002c00,0x2c000000,0x2b64a61f,0x29a8a9a8,0x261fab64,0x8000ac00,0xa9a8a9a8,0xac000000,0xa9a829a8,0xac000000,0xa61f2b64,0x29a829a8,0x2b64a61f,
0x80002c00,0x2c000000,0x8000ac00,0xac000000,0x2c000000,0xa61fab64,0xa9a829a8,0x2b64261f,0x8000ac00,0xa9a829a8,0x2c000000,0xa9a8a9a8,0xac000000,0x2b64261f,0xa9a8a9a8,0x261f2b64,
0x80002c00,0x8000ac00,0x80002c00,0x8000ac00,0x2c000000,0xab64261f,0x29a8a9a8,0xa61f2b64,0x8000ac00,0x29a829a8,0xac000000,0x29a8a9a8,0xac000000,0x261fab64,0x29a829a8,0xab64261f,
0x80002c00,0xac000000,0x8000ac00,0x2c000000,0x2c000000,0x261f2b64,0xa9a829a8,0xab64a61f,0x8000ac00,0x29a8a9a8,0x2c000000,0x29a829a8,0xac000000,0xab64a61f,0xa9a8a9a8,0xa61fab64,
0x80002c00,0x80002c00,0x80002c00,0x80002c00,0x8000ac00,0xa61fab64,0xa9a8a9a8,0xab64a61f,0x80002c00,0x29a829a8,0x2c000000,0x29a8a9a8,0x8000ac00,0xab64a61f,0xa9a829a8,0x261f2b64,
0x80002c00,0x2c000000,0x8000ac00,0xac000000,0x8000ac00,0xab64261f,0x29a829a8,0x261fab64,0x80002c00,0x29a8a9a8,0xac000000,0x29a829a8,0x8000ac00,0xa61f2b64,0x29a8a9a8,0xab64261f,
0x80002c00,0x8000ac00,0x80002c00,0x8000ac00,0x8000ac00,0x261f2b64,0xa9a8a9a8,0x2b64261f,0x80002c00,0xa9a8a9a8,0x2c000000,0xa9a829a8,0x8000ac00,0x2b64261f,0xa9a829a8,0xa61fab64,
0x80002c00,0xac000000,0x8000ac00,0x2c000000,0x8000ac00,0x2b64a61f,0x29a829a8,0xa61f2b64,0x80002c00,0xa9a829a8,0xac000000,0xa9a8a9a8,0x8000ac00,0x261fab64,0x29a8a9a8,0x2b64a61f,
0x80002c00,0x80002c00,0x80002c00,0x80002c00,0xac000000,0xab64261f,0xa9a829a8,0xa61f2b64,0x8000ac00,0xa9a8a9a8,0xac000000,0xa9a829a8,0x2c000000,0x261fab64,0xa9a8a9a8,0xab64261f,
0x80002c00,0x2c000000,0x8000ac00,0xac000000,0xac000000,0x261f2b64,0x29a8a9a8,0xab64a61f,0x8000ac00,0xa9a829a8,0x2c000000,0xa9a8a9a8,0x2c000000,0xab64a61f,0x29a829a8,0xa61fab64,
0x80002c00,0x8000ac00,0x80002c00,0x8000ac00,0xac000000,0x2b64a61f,0xa9a829a8,0x261fab64,0x8000ac00,0x29a829a8,0xac000000,0x29a8a9a8,0x2c000000,0xa61f2b64,0xa9a8a9a8,0x2b64a61f,
0x80002c00,0xac000000,0x8000ac00,0x2c000000,0xac000000,0xa61fab64,0x29a8a9a8,0x2b64261f,0x8000ac00,0x29a8a9a8,0x2c000000,0x29a829a8,0x2c000000,0x2b64261f,0x29a829a8,0x261f2b64,
0x2c000000,0x2c000000,0x2c000000,0x2c000000,0x2c000000,0x2b64a61f,0x29a8a9a8,0x261fab64,0x2c000000,0x29a8a9a8,0xac00,0xa9a8a9a8,0x2c000000,0x261fab64,0xa9a8a9a8,0xab64261f,
0x2c000000,0xac00,0xac000000,0x2c00,0x2c000000,0xa61fab64,0xa9a829a8,0x2b64261f,0x2c000000,0xa9a8a9a8,0x2c00,0x29a8a9a8,0x2c000000,0xab64a61f,0x29a829a8,0xa61fab64,
0x2c000000,0xac000000,0x2c000000,0xac000000,0x2c000000,0xab64261f,0x29a8a9a8,0xa61f2b64,0x2c000000,0xa9a829a8,0xac00,0x29a829a8,0x2c000000,0xa61f2b64,0xa9a8a9a8,0x2b64a61f,
0x2c000000,0x2c00,0xac000000,0xac00,0x2c000000,0x261f2b64,0xa9a829a8,0xab64a61f,0x2c000000,0x29a829a8,0x2c00,0xa9a829a8,0x2c000000,0x2b64261f,0x29a829a8,0x261f2b64,
0x2c000000,0x2c000000,0x2c000000,0x2c000000,0xac00,0xa61fab64,0xa9a8a9a8,0xab64a61f,0xac000000,0xa9a829a8,0x2c00,0x29a829a8,0x2c00,0x2b64261f,0x29a8a9a8,0xa61fab64,
0x2c000000,0xac00,0xac000000,0x2c00,0xac00,0xab64261f,0x29a829a8,0x261fab64,0xac000000,0x29a829a8,0xac00,0xa9a829a8,0x2c00,0x261fab64,0xa9a829a8,0x2b64a61f,
0x2c000000,0xac000000,0x2c000000,0xac000000,0xac00,0x261f2b64,0xa9a8a9a8,0x2b64261f,0xac000000,0x29a8a9a8,0x2c00,0xa9a8a9a8,0x2c00,0xab64a61f,0x29a8a9a8,0x261f2b64,
0x2c000000,0x2c00,0xac000000,0xac00,0xac00,0x2b64a61f,0x29a829a8,0xa61f2b64,0xac000000,0xa9a8a9a8,0xac00,0x29a8a9a8,0x2c00,0xa61f2b64,0xa9a829a8,0xab64261f,
0x2c000000,0x2c000000,0x2c000000,0x2c000000,0xac000000,0xab64261f,0xa9a829a8,0xa61f2b64,0x2c000000,0x29a8a9a8,0xac00,0xa9a8a9a8,0xac000000,0xa61f2b64,0x29a829a8,0x2b64a61f,
0x2c000000,0xac00,0xac000000,0x2c00,0xac000000,0x261f2b64,0x29a8a9a8,0xab64a61f,0x2c000000,0xa9a8a9a8,0x2c00,0x29a8a9a8,0xac000000,0x2b64261f,0xa9a8a9a8,0x261f2b64,
0x2c000000,0xac000000,0x2c000000,0xac000000,0xac000000,0x2b64a61f,0xa9a829a8,0x261fab64,0x2c000000,0xa9a829a8,0xac00,0x29a829a8,0xac000000,0x261fab64,0x29a829a8,0xab64261f,
0x2c000000,0x2c00,0xac000000,0xac00,0xac000000,0xa61fab64,0x29a8a9a8,0x2b64261f,0x2c000000,0x29a829a8,0x2c00,0xa9a829a8,0xac000000,0xab64a61f,0xa9a8a9a8,0xa61fab64,
0x2c000000,0x2c000000,0x2c000000,0x2c000000,0x2c00,0x261f2b64,0x29a829a8,0x2b64261f,0xac000000,0xa9a829a8,0x2c00,0x29a829a8,0xac00,0xab64a61f,0xa9a829a8,0x261f2b64,
0x2c000000,0xac00,0xac000000,0x2c00,0x2c00,0x2b64a61f,0xa9a8a9a8,0xa61f2b64,0xac000000,0x29a829a8,0xac00,0xa9a829a8,0xac00,0xa61f2b64,0x29a8a9a8,0xab64261f,
0x2c000000,0xac000000,0x2c000000,0xac000000,0x2c00,0xa61fab64,0x29a829a8,0xab64a61f,0xac000000,0x29a8a9a8,0x2c00,0xa9a8a9a8,0xac00,0x2b64261f,0xa9a829a8,0xa61fab64,
0x2c000000,0x2c00,0xac000000,0xac00,0x2c00,0xab64261f,0xa9a8a9a8,0x261fab64,0xac000000,0xa9a8a9a8,0xac00,0x29a8a9a8,0xac00,0x261fab64,0x29a8a9a8,0x2b64a61f,
0x3f800000,0x0,0x3f7ff0e3,0xbcafea69,0x3f800000,0x0,0x3f7fba9e,0xbd3c6dd5,0x3f800000,0x0,0x3f7f5ce9,0xbd9064b4,0x3f800000,0x0,0x3f7ed7d4,0xbdc27c39,
0x3f800000,0x0,0x3f7e2b72,0xbdf475c0,0x3f800000,0x0,0x3f7d57de,0xbe1324ca,0x3f800000,0x0,0x3f7c5d3a,0xbe2bf804,0x3f800000,0x0,0x3f7b3bab,0xbe44b0b9,
0x3f800000,0x0,0x3f79f35e,0xbe5d4b1a,0x3f800000,0x0,0x3f788486,0xbe75c35a,0x3f800000,0x0,0x3f76ef5b,0xbe870ada,0x3f800000,0x0,0x3f75341d,0xbe931f35,
0x3f800000,0x0,0x3f73530e,0xbe9f1cdf,0x3f800000,0x0,0x3f714c7a,0xbeab0201,0x3f800000,0x0,0x3f6f20b0,0xbeb6ccc3,0x3f800000,0x0,0x3f6cd007,0xbec27b55,
0x3f7f0e58,0xbdafb680,0x3f7e868b,0xbddb7d37,0x3f7baccd,0xbe3b6ecf,0x3f7940c0,0xbe698bba,0x3f75dec6,0xbe8e9a22,0x3f703b37,0xbeb0eacb,0x3f6db293,0xbebe1d4a,0x3f6398ac,0xbeea6681,
0x3f633c5a,0xbeebcbbb,0x3f5389c7,0xbf102dd5,0x3f5695e5,0xbf0b9a6b,0x3f404c5c,0xbf28fd41,0x3f47de65,0xbf1ff6cb,0x3f2a2a80,0xbf3f41fc,0x3f373a23,0xbf32c8c9,0x3f11796b,0xbf52a649,
0x3f24d225,0xbf43e200,0x3eed3063,0xbf62df7b,0x3f10d3cd,0xbf531849,0x3eb3dca5,0xbf6faf1b,0x3ef6e0cb,0xbf604621,0x3e6fa8b2,0xbf78e3d6,0x3ec9b953,0xbf6b4b0c,0x3de7fa9a,0xbf7e5a38,
0x3e9aa086,0xbf740bdd,0xbc16cb58,0xbf7ffd39,0x3e541501,0xbf7a7302,0xbe06b26e,0xbf7dc68c,0x3de1bc2e,0xbf7e70b0,0xbe80f8c0,0xbf77beb7,0x3c490e90,0xbf7ffb11,0xbebca7af,0xbf6dfcf2,
0x3f7c3b28,0xbe2f10a2,0x3f7b3bab,0xbe44b0b9,0x3f6ed89e,0xbeb8442a,0x3f6a5ad9,0xbece0bea,0x3f584853,0xbf08f59b,0x3f4e1689,0xbf17de12,0x3f396842,0xbf3085bb,0x3f27ce61,0xbf4154e1,
0x3f13682a,0xbf514d3d,0x3ef2bd43,0xbf61668a,0x3ecf7bca,0xbf6a09a7,0x3e8a11f7,0xbf76841b,0x3e605c13,0xbf79c79d,0x3d55880e,0xbf7fa6e3,0x3cc90ab0,0xbf7fec43,0xbe2bf804,0xbf7c5d3a,
0xbe2f10a2,0xbf7c3b28,0xbec27b55,0xbf6cd007,0xbeb8442a,0xbf6ed89e,0xbf12c39a,0xbf51c0c2,0xbf08f59b,0xbf584853,0xbf3d27b8,0xbf2c800f,0xbf3085bb,0xbf396842,0xbf5e5aa6,0xbefdbabb,
0xbf514d3d,0xbf13682a,0xbf74bf62,0xbe9620d2,0xbf6a09a7,0xbecf7bca,0xbf7f3f54,0xbd9cecf9,0xbf79c79d,0xbe605c13,0xbf7d57de,0x3e1324ca,0xbf7fec43,0xbcc90ab0,0xbf6f20b0,0x3eb6ccc3,
0x3f778bc5,0xbe827dc0,0x3f76167a,0xbe8d17c0,0x3f59f26a,0xbf064b82,0x3f5389c7,0xbf102dd5,0x3f299415,0xbf3fc767,0x3f1ba299,0xbf4b41fa,0x3ed53641,0xbf68bf3c,0x3ea80b1f,0xbf71d19f,
0x3e09cf86,0xbf7dabcc,0x3c7b514b,0xbf7ff84a,0xbe22abb6,0xbf7cbfc9,0xbe9920fe,0xbf74484b,0xbee0924f,0xbf660f88,0xbf1553b7,0xbf4fefa8,0xbf2e3bde,0xbf3b8f3b,0xbf4f0417,0xbf16999f,
0xbf5d2d53,0xbf00e7e4,0xbf73ced9,0xbe9c1faf,0xbf791298,0xbe6c9a7f,0xbf7ffd39,0x3c16cb58,0xbf7f84ab,0x3d7b2b74,0xbf725470,0x3ea5129f,0xbf6ff573,0x3eb263ef,0xbf4c3578,0x3f1a6293,
0xbf4bbbf8,0x3f1b02c6,0xbf11796b,0x3f52a649,0xbf15f6d9,0x3f4f7a1f,0xbe901c2c,0x3f75a67b,0xbea68f12,0x3f721352,0x3d0a342f,0x3f7fdaaf,0xbd16c32c,0x3f7fd397,0x3eb0eacb,0x3f703b37,
0x3f7fc38f,0xbd2fe007,0x3f7f7808,0xbd83db0a,0x3f7eea9d,0xbdbc3ac3,0x3f7d906e,0xbe0cec4a,0x3f7d7474,0xbe1008b7,0x3f7a4910,0xbe57278f,0x3f7b61fc,0xbe419b37,0x3f75a67b,0xbe901c2c,
0x3f78b47b,0xbe72b651,0x3f6faf1b,0xbeb3dca5,0x3f756d97,0xbe919ddd,0x3f686b39,0xbed6a399,0x3f718f57,0xbea986c4,0x3f5fe4e9,0xbef840c8,0x3f6d1c1d,0xbec1071e,0x3f5627fe,0xbf0c42c9,
0x3f6816a8,0xbed8106b,0x3f4b41fa,0xbf1ba299,0x3f628210,0xbeee9479,0x3f3f41fc,0xbf2a2a80,0x3f5c61c7,0xbf0242b1,0x3f3238aa,0xbf37c655,0x3f55b993,0xbf0cead0,0x3f24381a,0xbf446337,
0x3f4e8d90,0xbf173c07,0x3f1553b7,0xbf4fefa8,0x3f46e22a,0xbf212ff9,0x3f05a02c,0xbf5a5ba0,0x3f3ebc1b,0xbf2ac082,0x3eea6681,0xbf6398ac,0x3f36206c,0xbf33e7bc,0x3ec8477c,0xbf6b99fb,
0x3f7de0b1,0xbe039502,0x3f7d1cdd,0xbe195be0,0x3f764d97,0xbe8b9507,0x3f72d4eb,0xbea21887,0x3f696591,0xbed25a09,0x3f61668a,0xbef2bd43,0x3f577026,0xbf0a48ad,0x3f495521,0xbf1e1e22,
0x3f40d0da,0xbf286605,0x3f2b561b,0xbf3e35c5,0x3f26050a,0xbf42de29,0x3f084b92,0xbf58b3a1,0x3f07a136,0xbf591e6a,0x3ec27b55,0xbf6cd007,0x3ecc9b8b,0xbf6aab7b,0x3e5d4b1a,0xbf79f35e,
0x3e8586ce,0xbf772417,0x3d3c6dd5,0xbf7fba9e,0x3dee3876,0xbf7e4323,0xbe007745,0xbf7dfa38,0xbcfb49ba,0xbf7fe129,0xbe9620d2,0xbf74bf62,0xbe354098,0xbf7bf531,0xbee79a5d,0xbf644fac,
0xbea395c5,0xbf7294f8,0xbf19210f,0xbf4d26fd,0xbee900b7,0xbf63f473,0xbf39f2ac,0xbf2ff3e6,0xbf14b039,0xbf5064af,0xbf554aa4,0xbf0d9281,0xbf31a81d,0xbf385216,0xbf6a5ad9,0xbece0bea,
0x3f7a1e84,0xbe5a3997,0x3f78e3d6,0xbe6fa8b2,0x3f655e0b,0xbee363fa,0x3f5fe4e9,0xbef840c8,0x3f42de29,0xbf26050a,0x3f36ad7f,0xbf33587a,0x3f14b039,0xbf5064af,0x3f003a06,0xbf5d924d,
0x3ebb31a0,0xbf6e46be,0x3e80f8c0,0xbf77beb7,0x3e039502,0xbf7de0b1,0xbc7b514b,0xbf7ff84a,0xbdee3876,0xbf7e4323,0xbe901c2c,0xbf75a67b,0xbeb554ec,0xbf6f6830,0xbf06f686,0xbf5988ad,
0xbf121eb0,0xbf5233c6,0xbf3c17d9,0xbf2da859,0xbf40d0da,0xbf286605,0xbf6398ac,0xbeea6681,0xbf63f473,0xbee900b7,0xbf7a9c59,0xbe5101f1,0xbf796e4e,0xbe667c66,0xbf7f7808,0x3d83db0a,
0xbf7ff4e6,0x3c96c9b6,0xbf71d19f,0x3ea80b1f,0xbf772417,0x3e8586ce,0xbf52a649,0x3f11796b,0xbf5f8327,0x3ef9a02d,0xbf24381a,0x3f446337,0xbf3a7ca4,0x3f2f61a5,0xbed3c867,0x3f6912ae,
0x3f748422,0xbe97a117,0x3f72d4eb,0xbea21887,0x3f4cae79,0xbf19c200,0x3f456439,0xbf2302d3,0x3f0cead0,0xbf55b993,0x3efafef7,0xbf5f20db,0x3e72b651,0xbf78b47b,0x3e1324ca,0xbf7d57de,
0xbdd53db9,0xbf7e9bc9,0xbe636c83,0xbf799b43,0xbeddbe79,0xbf66becc,0xbf0ee0db,0xbf546b3b,0xbf36206c,0xbf33e7bc,0xbf51c0c2,0xbf12c39a,0xbf6816a8,0xbed8106b,0xbf788486,0xbe75c35a,
0xbf7eea9d,0xbdbc3ac3,0xbf7dfa38,0x3e007745,0xbf77f110,0x3e7ee6e1,0xbf61668a,0x3ef2bd43,0xbf53fac3,0x3f0f8784,0xbf269de3,0x3f425b89,0xbf173c07,0x3f4e8d90,0xbeab0201,0x3f714c7a,
0xbe919ddd,0x3f756d97,0x3ce22a7a,0x3f7fe705,0x3d621469,0x3f7f9c18,0x3ec5625c,0x3f6c3624,0x3ec6d529,0x3f6be858,0x3f311722,0x3f38dd65,0x3f2d1469,0x3f3ca003,0x3f67c187,0x3ed97cb9,
0x3f800000,0x0,0x3f800000,0x0,0x3f7ff0e3,0xbcafea69,0x3f7fba9e,0xbd3c6dd5,0x3f7fc38f,0xbd2fe007,0x3f7eea9d,0xbdbc3ac3,0x3f7f7808,0xbd83db0a,0x3f7d906e,0xbe0cec4a,
0x3f7f0e58,0xbdafb680,0x3f7baccd,0xbe3b6ecf,0x3f7e868b,0xbddb7d37,0x3f7940c0,0xbe698bba,0x3f7de0b1,0xbe039502,0x3f764d97,0xbe8b9507,0x3f7d1cdd,0xbe195be0,0x3f72d4eb,0xbea21887,
0x3f7c3b28,0xbe2f10a2,0x3f6ed89e,0xbeb8442a,0x3f7b3bab,0xbe44b0b9,0x3f6a5ad9,0xbece0bea,0x3f7a1e84,0xbe5a3997,0x3f655e0b,0xbee363fa,0x3f78e3d6,0xbe6fa8b2,0x3f5fe4e9,0xbef840c8,
0x3f778bc5,0xbe827dc0,0x3f59f26a,0xbf064b82,0x3f76167a,0xbe8d17c0,0x3f5389c7,0xbf102dd5,0x3f748422,0xbe97a117,0x3f4cae79,0xbf19c200,0x3f72d4eb,0xbea21887,0x3f456439,0xbf2302d3,
0x3f800000,0x0,0x3f800000,0x0,0x3f7e2b72,0xbdf475c0,0x3f7d57de,0xbe1324ca,0x3f78b47b,0xbe72b651,0x3f756d97,0xbe919ddd,0x3f6faf1b,0xbeb3dca5,0x3f686b39,0xbed6a399,
0x3f633c5a,0xbeebcbbb,0x3f5695e5,0xbf0b9a6b,0x3f5389c7,0xbf102dd5,0x3f404c5c,0xbf28fd41,0x3f40d0da,0xbf286605,0x3f26050a,0xbf42de29,0x3f2b561b,0xbf3e35c5,0x3f084b92,0xbf58b3a1,
0x3f13682a,0xbf514d3d,0x3ecf7bca,0xbf6a09a7,0x3ef2bd43,0xbf61668a,0x3e8a11f7,0xbf76841b,0x3ebb31a0,0xbf6e46be,0x3e039502,0xbf7de0b1,0x3e80f8c0,0xbf77beb7,0xbc7b514b,0xbf7ff84a,
0x3e09cf86,0xbf7dabcc,0xbe22abb6,0xbf7cbfc9,0x3c7b514b,0xbf7ff84a,0xbe9920fe,0xbf74484b,0xbdd53db9,0xbf7e9bc9,0xbeddbe79,0xbf66becc,0xbe636c83,0xbf799b43,0xbf0ee0db,0xbf546b3b,
0x3f800000,0x0,0x3f800000,0x0,0x3f79f35e,0xbe5d4b1a,0x3f788486,0xbe75c35a,0x3f6816a8,0xbed8106b,0x3f628210,0xbeee9479,0x3f4b41fa,0xbf1ba299,0x3f3f41fc,0xbf2a2a80,
0x3f24d225,0xbf43e200,0x3f10d3cd,0xbf531849,0x3eed3063,0xbf62df7b,0x3eb3dca5,0xbf6faf1b,0x3e8586ce,0xbf772417,0x3dee3876,0xbf7e4323,0x3d3c6dd5,0xbf7fba9e,0xbe007745,0xbf7dfa38,
0xbe2f10a2,0xbf7c3b28,0xbeb8442a,0xbf6ed89e,0xbec27b55,0xbf6cd007,0xbf12c39a,0xbf51c0c2,0xbf121eb0,0xbf5233c6,0xbf40d0da,0xbf286605,0xbf3c17d9,0xbf2da859,0xbf6398ac,0xbeea6681,
0xbf5d2d53,0xbf00e7e4,0xbf791298,0xbe6c9a7f,0xbf73ced9,0xbe9c1faf,0xbf7ffd39,0x3c16cb58,0xbf7eea9d,0xbdbc3ac3,0xbf77f110,0x3e7ee6e1,0xbf7dfa38,0x3e007745,0xbf61668a,0x3ef2bd43,
0x3f800000,0x0,0x3f800000,0x0,0x3f73530e,0xbe9f1cdf,0x3f714c7a,0xbeab0201,0x3f4e8d90,0xbf173c07,0x3f46e22a,0xbf212ff9,0x3f1553b7,0xbf4fefa8,0x3f05a02c,0xbf5a5ba0,
0x3e9aa086,0xbf740bdd,0x3e541501,0xbf7a7302,0xbc16cb58,0xbf7ffd39,0xbe06b26e,0xbf7dc68c,0xbea395c5,0xbf7294f8,0xbee900b7,0xbf63f473,0xbf19210f,0xbf4d26fd,0xbf39f2ac,0xbf2ff3e6,
0xbf514d3d,0xbf13682a,0xbf6a09a7,0xbecf7bca,0xbf74bf62,0xbe9620d2,0xbf7f3f54,0xbd9cecf9,0xbf7ff4e6,0x3c96c9b6,0xbf772417,0x3e8586ce,0xbf71d19f,0x3ea80b1f,0xbf52a649,0x3f11796b,
0xbf4bbbf8,0x3f1b02c6,0xbf15f6d9,0x3f4f7a1f,0xbf11796b,0x3f52a649,0xbe901c2c,0x3f75a67b,0xbe919ddd,0x3f756d97,0x3d621469,0x3f7f9c18,0x3ce22a7a,0x3f7fe705,0x3ec5625c,0x3f6c3624,
0x3f800000,0x0,0x3f800000,0x0,0x3f7f5ce9,0xbd9064b4,0x3f7ed7d4,0xbdc27c39,0x3f7d7474,0xbe1008b7,0x3f7b61fc,0xbe419b37,0x3f7a4910,0xbe57278f,0x3f75a67b,0xbe901c2c,
0x3f75dec6,0xbe8e9a22,0x3f6db293,0xbebe1d4a,0x3f703b37,0xbeb0eacb,0x3f6398ac,0xbeea6681,0x3f696591,0xbed25a09,0x3f577026,0xbf0a48ad,0x3f61668a,0xbef2bd43,0x3f495521,0xbf1e1e22,
0x3f584853,0xbf08f59b,0x3f396842,0xbf3085bb,0x3f4e1689,0xbf17de12,0x3f27ce61,0xbf4154e1,0x3f42de29,0xbf26050a,0x3f14b039,0xbf5064af,0x3f36ad7f,0xbf33587a,0x3f003a06,0xbf5d924d,
0x3f299415,0xbf3fc767,0x3ed53641,0xbf68bf3c,0x3f1ba299,0xbf4b41fa,0x3ea80b1f,0xbf71d19f,0x3f0cead0,0xbf55b993,0x3e72b651,0xbf78b47b,0x3efafef7,0xbf5f20db,0x3e1324ca,0xbf7d57de,
0x3f800000,0x0,0x3f800000,0x0,0x3f7c5d3a,0xbe2bf804,0x3f7b3bab,0xbe44b0b9,0x3f718f57,0xbea986c4,0x3f6d1c1d,0xbec1071e,0x3f5fe4e9,0xbef840c8,0x3f5627fe,0xbf0c42c9,
0x3f47de65,0xbf1ff6cb,0x3f373a23,0xbf32c8c9,0x3f2a2a80,0xbf3f41fc,0x3f11796b,0xbf52a649,0x3f07a136,0xbf591e6a,0x3ecc9b8b,0xbf6aab7b,0x3ec27b55,0xbf6cd007,0x3e5d4b1a,0xbf79f35e,
0x3e605c13,0xbf79c79d,0x3cc90ab0,0xbf7fec43,0x3d55880e,0xbf7fa6e3,0xbe2bf804,0xbf7c5d3a,0xbdee3876,0xbf7e4323,0xbeb554ec,0xbf6f6830,0xbe901c2c,0xbf75a67b,0xbf06f686,0xbf5988ad,
0xbee0924f,0xbf660f88,0xbf2e3bde,0xbf3b8f3b,0xbf1553b7,0xbf4fefa8,0xbf4f0417,0xbf16999f,0xbf36206c,0xbf33e7bc,0xbf6816a8,0xbed8106b,0xbf51c0c2,0xbf12c39a,0xbf788486,0xbe75c35a,
0x3f800000,0x0,0x3f800000,0x0,0x3f76ef5b,0xbe870ada,0x3f75341d,0xbe931f35,0x3f5c61c7,0xbf0242b1,0x3f55b993,0xbf0cead0,0x3f3238aa,0xbf37c655,0x3f24381a,0xbf446337,
0x3ef6e0cb,0xbf604621,0x3ec9b953,0xbf6b4b0c,0x3e6fa8b2,0xbf78e3d6,0x3de7fa9a,0xbf7e5a38,0xbcfb49ba,0xbf7fe129,0xbe354098,0xbf7bf531,0xbe9620d2,0xbf74bf62,0xbee79a5d,0xbf644fac,
0xbf08f59b,0xbf584853,0xbf3085bb,0xbf396842,0xbf3d27b8,0xbf2c800f,0xbf5e5aa6,0xbefdbabb,0xbf63f473,0xbee900b7,0xbf796e4e,0xbe667c66,0xbf7a9c59,0xbe5101f1,0xbf7f7808,0x3d83db0a,
0xbf7f84ab,0x3d7b2b74,0xbf6ff573,0x3eb263ef,0xbf725470,0x3ea5129f,0xbf4c3578,0x3f1a6293,0xbf53fac3,0x3f0f8784,0xbf173c07,0x3f4e8d90,0xbf269de3,0x3f425b89,0xbeab0201,0x3f714c7a,
0x3f800000,0x0,0x3f800000,0x0,0x3f6f20b0,0xbeb6ccc3,0x3f6cd007,0xbec27b55,0x3f3ebc1b,0xbf2ac082,0x3f36206c,0xbf33e7bc,0x3eea6681,0xbf6398ac,0x3ec8477c,0xbf6b99fb,
0x3de1bc2e,0xbf7e70b0,0x3c490e90,0xbf7ffb11,0xbe80f8c0,0xbf77beb7,0xbebca7af,0xbf6dfcf2,0xbf14b039,0xbf5064af,0xbf31a81d,0xbf385216,0xbf554aa4,0xbf0d9281,0xbf6a5ad9,0xbece0bea,
0xbf79c79d,0xbe605c13,0xbf7fec43,0xbcc90ab0,0xbf7d57de,0x3e1324ca,0xbf6f20b0,0x3eb6ccc3,0xbf5f8327,0x3ef9a02d,0xbf3a7ca4,0x3f2f61a5,0xbf24381a,0x3f446337,0xbed3c867,0x3f6912ae,
0xbea68f12,0x3f721352,0xbd16c32c,0x3f7fd397,0x3d0a342f,0x3f7fdaaf,0x3eb0eacb,0x3f703b37,0x3ec6d529,0x3f6be858,0x3f2d1469,0x3f3ca003,0x3f311722,0x3f38dd65,0x3f67c187,0x3ed97cb9,
};
__device__ const uint32_t __attribute__((aligned(16))) TAB3[2560] = {
0x80002c00,0x80002c00,0x80002c00,0x80002c00,0x2c000000,0x2c000000,0x2c000000,0x2c000000,0x80002c00,0x8000ac00,0x80002c00,0x8000ac00,0x2c000000,0xac000000,0x2c000000,0xac000000,
0x80002c00,0x80002c00,0x29a829a8,0x29a829a8,0x2c000000,0x2c000000,0x29a8a9a8,0x29a8a9a8,0x80002c00,0x8000ac00,0x29a829a8,0xa9a8a9a8,0x2c000000,0xac000000,0x29a8a9a8,0xa9a829a8,
0x80002c00,0x80002c00,0x2c000000,0x2c000000,0x2c000000,0x2c000000,0xac00,0xac00,0x80002c00,0x8000ac00,0x2c000000,0xac000000,0x2c000000,0xac000000,0xac00,0x2c00,
0x80002c00,0x80002c00,0x29a8a9a8,0x29a8a9a8,0x2c000000,0x2c000000,0xa9a8a9a8,0xa9a8a9a8,0x80002c00,0x8000ac00,0x29a8a9a8,0xa9a829a8,0x2c000000,0xac000000,0xa9a8a9a8,0x29a829a8,
0x80002c00,0x80002c00,0x80002c00,0x80002c00,0x2c000000,0x2c000000,0x2c000000,0x2c000000,0x80002c00,0x8000ac00,0x80002c00,0x8000ac00,0x2c000000,0xac000000,0x2c000000,0xac000000,
0x2c000000,0x2c000000,0x29a8a9a8,0x29a8a9a8,0xac00,0xac00,0xa9a8a9a8,0xa9a8a9a8,0x2c000000,0xac000000,0x29a8a9a8,0xa9a829a8,0xac00,0x2c00,0xa9a8a9a8,0x29a829a8,
0x8000ac00,0x8000ac00,0xac000000,0xac000000,0xac000000,0xac000000,0x2c00,0x2c00,0x8000ac00,0x80002c00,0xac000000,0x2c000000,0xac000000,0x2c000000,0x2c00,0xac00,
0xac000000,0xac000000,0x29a829a8,0x29a829a8,0x2c00,0x2c00,0x29a8a9a8,0x29a8a9a8,0xac000000,0x2c000000,0x29a829a8,0xa9a8a9a8,0x2c00,0xac00,0x29a8a9a8,0xa9a829a8,
0x80002c00,0x80002c00,0x80002c00,0x80002c00,0x2c000000,0x2c000000,0x2c000000,0x2c000000,0x80002c00,0x8000ac00,0x80002c00,0x8000ac00,0x2c000000,0xac000000,0x2c000000,0xac000000,
0x8000ac00,0x8000ac00,0xa9a8a9a8,0xa9a8a9a8,0xac000000,0xac000000,0xa9a829a8,0xa9a829a8,0x8000ac00,0x80002c00,0xa9a8a9a8,0x29a829a8,0xac000000,0x2c000000,0xa9a829a8,0x29a8a9a8,
0x80002c00,0x80002c00,0x2c000000,0x2c000000,0x2c000000,0x2c000000,0xac00,0xac00,0x80002c00,0x8000ac00,0x2c000000,0xac000000,0x2c000000,0xac000000,0xac00,0x2c00,
0x8000ac00,0x8000ac00,0xa9a829a8,0xa9a829a8,0xac000000,0xac000000,0x29a829a8,0x29a829a8,0x8000ac00,0x80002c00,0xa9a829a8,0x29a8a9a8,0xac000000,0x2c000000,0x29a829a8,0xa9a8a9a8,
0x80002c00,0x80002c00,0x80002c00,0x80002c00,0x2c000000,0x2c000000,0x2c000000,0x2c000000,0x80002c00,0x8000ac00,0x80002c00,0x8000ac00,0x2c000000,0xac000000,0x2c000000,0xac000000,
0xac000000,0xac000000,0xa9a829a8,0xa9a829a8,0x2c00,0x2c00,0x29a829a8,0x29a829a8,0xac000000,0x2c000000,0xa9a829a8,0x29a8a9a8,0x2c00,0xac00,0x29a829a8,0xa9a8a9a8,
0x8000ac00,0x8000ac00,0xac000000,0xac000000,0xac000000,0xac000000,0x2c00,0x2c00,0x8000ac00,0x80002c00,0xac000000,0x2c000000,0xac000000,0x2c000000,0x2c00,0xac00,
0x2c000000,0x2c000000,0xa9a8a9a8,0xa9a8a9a8,0xac00,0xac00,0xa9a829a8,0xa9a829a8,0x2c000000,0xac000000,0xa9a8a9a8,0x29a829a8,0xac00,0x2c00,0xa9a829a8,0x29a8a9a8,
0x80002c00,0x80002c00,0x8000ac00,0x8000ac00,0x2c000000,0x2c000000,0xac000000,0xac000000,0x80002c00,0x8000ac00,0x8000ac00,0x80002c00,0x2c000000,0xac000000,0xac000000,0x2c000000,
0x80002c00,0x80002c00,0xa9a8a9a8,0xa9a8a9a8,0x2c000000,0x2c000000,0xa9a829a8,0xa9a829a8,0x80002c00,0x8000ac00,0xa9a8a9a8,0x29a829a8,0x2c000000,0xac000000,0xa9a829a8,0x29a8a9a8,
0x80002c00,0x80002c00,0xac000000,0xac000000,0x2c000000,0x2c000000,0x2c00,0x2c00,0x80002c00,0x8000ac00,0xac000000,0x2c000000,0x2c000000,0xac000000,0x2c00,0xac00,
0x80002c00,0x80002c00,0xa9a829a8,0xa9a829a8,0x2c000000,0x2c000000,0x29a829a8,0x29a829a8,0x80002c00,0x8000ac00,0xa9a829a8,0x29a8a9a8,0x2c000000,0xac000000,0x29a829a8,0xa9a8a9a8,
0x80002c00,0x80002c00,0x8000ac00,0x8000ac00,0x2c000000,0x2c000000,0xac000000,0xac000000,0x80002c00,0x8000ac00,0x8000ac00,0x80002c00,0x2c000000,0xac000000,0xac000000,0x2c000000,
0x2c000000,0x2c000000,0xa9a829a8,0xa9a829a8,0xac00,0xac00,0x29a829a8,0x29a829a8,0x2c000000,0xac000000,0xa9a829a8,0x29a8a9a8,0xac00,0x2c00,0x29a829a8,0xa9a8a9a8,
0x8000ac00,0x8000ac00,0x2c000000,0x2c000000,0xac000000,0xac000000,0xac00,0xac00,0x8000ac00,0x80002c00,0x2c000000,0xac000000,0xac000000,0x2c000000,0xac00,0x2c00,
0xac000000,0xac000000,0xa9a8a9a8,0xa9a8a9a8,0x2c00,0x2c00,0xa9a829a8,0xa9a829a8,0xac000000,0x2c000000,0xa9a8a9a8,0x29a829a8,0x2c00,0xac00,0xa9a829a8,0x29a8a9a8,
0x80002c00,0x80002c00,0x8000ac00,0x8000ac00,0x2c000000,0x2c000000,0xac000000,0xac000000,0x80002c00,0x8000ac00,0x8000ac00,0x80002c00,0x2c000000,0xac000000,0xac000000,0x2c000000,
0x8000ac00,0x8000ac00,0x29a829a8,0x29a829a8,0xac000000,0xac000000,0x29a8a9a8,0x29a8a9a8,0x8000ac00,0x80002c00,0x29a829a8,0xa9a8a9a8,0xac000000,0x2c000000,0x29a8a9a8,0xa9a829a8,
0x80002c00,0x80002c00,0xac000000,0xac000000,0x2c000000,0x2c000000,0x2c00,0x2c00,0x80002c00,0x8000ac00,0xac000000,0x2c000000,0x2c000000,0xac000000,0x2c00,0xac00,
0x8000ac00,0x8000ac00,0x29a8a9a8,0x29a8a9a8,0xac000000,0xac000000,0xa9a8a9a8,0xa9a8a9a8,0x8000ac00,0x80002c00,0x29a8a9a8,0xa9a829a8,0xac000000,0x2c000000,0xa9a8a9a8,0x29a829a8,
0x80002c00,0x80002c00,0x8000ac00,0x8000ac00,0x2c000000,0x2c000000,0xac000000,0xac000000,0x80002c00,0x8000ac00,0x8000ac00,0x80002c00,0x2c000000,0xac000000,0xac000000,0x2c000000,
0xac000000,0xac000000,0x29a8a9a8,0x29a8a9a8,0x2c00,0x2c00,0xa9a8a9a8,0xa9a8a9a8,0xac000000,0x2c000000,0x29a8a9a8,0xa9a829a8,0x2c00,0xac00,0xa9a8a9a8,0x29a829a8,
0x8000ac00,0x8000ac00,0x2c000000,0x2c000000,0xac000000,0xac000000,0xac00,0xac00,0x8000ac00,0x80002c00,0x2c000000,0xac000000,0xac000000,0x2c000000,0xac00,0x2c00,
0x2c000000,0x2c000000,0x29a829a8,0x29a829a8,0xac00,0xac00,0x29a8a9a8,0x29a8a9a8,0x2c000000,0xac000000,0x29a829a8,0xa9a8a9a8,0xac00,0x2c00,0x29a8a9a8,0xa9a829a8,
0x80002c00,0x80002c00,0x80002c00,0x80002c00,0x80002c00,0x261f2b64,0x29a829a8,0x2b64261f,0x80002c00,0x29a829a8,0x2c000000,0x29a8a9a8,0x80002c00,0x2b64261f,0x29a8a9a8,0xa61fab64,
0x80002c00,0x2c000000,0x8000ac00,0xac000000,0x80002c00,0x2b64a61f,0xa9a8a9a8,0xa61f2b64,0x80002c00,0x29a8a9a8,0xac000000,0x29a829a8,0x80002c00,0x261fab64,0xa9a829a8,0x2b64a61f,
0x80002c00,0x8000ac00,0x80002c00,0x8000ac00,0x80002c00,0xa61fab64,0x29a829a8,0xab64a61f,0x80002c00,0xa9a8a9a8,0x2c000000,0xa9a829a8,0x80002c00,0xab64a61f,0x29a8a9a8,0x261f2b64,
0x80002c00,0xac000000,0x8000ac00,0x2c000000,0x80002c00,0xab64261f,0xa9a8a9a8,0x261fab64,0x80002c00,0xa9a829a8,0xac000000,0xa9a8a9a8,0x80002c00,0xa61f2b64,0xa9a829a8,0xab64261f,
0x80002c00,0x80002c00,0x80002c00,0x80002c00,0x2c000000,0x2b64a61f,0x29a8a9a8,0x261fab64,0x8000ac00,0xa9a8a9a8,0xac000000,0xa9a829a8,0xac000000,0xa61f2b64,0x29a829a8,0x2b64a61f,
0x80002c00,0x2c000000,0x8000ac00,0xac000000,0x2c000000,0xa61fab64,0xa9a829a8,0x2b64261f,0x8000ac00,0xa9a829a8,0x2c000000,0xa9a8a9a8,0xac000000,0x2b64261f,0xa9a8a9a8,0x261f2b64,
0x80002c00,0x8000ac00,0x80002c00,0x8000ac00,0x2c000000,0xab64261f,0x29a8a9a8,0xa61f2b64,0x8000ac00,0x29a829a8,0xac000000,0x29a8a9a8,0xac000000,0x261fab64,0x29a829a8,0xab64261f,
0x80002c00,0xac000000,0x8000ac00,0x2c000000,0x2c000000,0x261f2b64,0xa9a829a8,0xab64a61f,0x8000ac00,0x29a8a9a8,0x2c000000,0x29a829a8,0xac000000,0xab64a61f,0xa9a8a9a8,0xa61fab64,
0x80002c00,0x80002c00,0x80002c00,0x80002c00,0x8000ac00,0xa61fab64,0xa9a8a9a8,0xab64a61f,0x80002c00,0x29a829a8,0x2c000000,0x29a8a9a8,0x8000ac00,0xab64a61f,0xa9a829a8,0x261f2b64,
0x80002c00,0x2c000000,0x8000ac00,0xac000000,0x8000ac00,0xab64261f,0x29a829a8,0x261fab64,0x80002c00,0x29a8a9a8,0xac000000,0x29a829a8,0x8000ac00,0xa61f2b64,0x29a8a9a8,0xab64261f,
0x80002c00,0x8000ac00,0x80002c00,0x8000ac00,0x8000ac00,0x261f2b64,0xa9a8a9a8,0x2b64261f,0x80002c00,0xa9a8a9a8,0x2c000000,0xa9a829a8,0x8000ac00,0x2b64261f,0xa9a829a8,0xa61fab64,
0x80002c00,0xac000000,0x8000ac00,0x2c000000,0x8000ac00,0x2b64a61f,0x29a829a8,0xa61f2b64,0x80002c00,0xa9a829a8,0xac000000,0xa9a8a9a8,0x8000ac00,0x261fab64,0x29a8a9a8,0x2b64a61f,
0x80002c00,0x80002c00,0x80002c00,0x80002c00,0xac000000,0xab64261f,0xa9a829a8,0xa61f2b64,0x8000ac00,0xa9a8a9a8,0xac000000,0xa9a829a8,0x2c000000,0x261fab64,0xa9a8a9a8,0xab64261f,
0x80002c00,0x2c000000,0x8000ac00,0xac000000,0xac000000,0x261f2b64,0x29a8a9a8,0xab64a61f,0x8000ac00,0xa9a829a8,0x2c000000,0xa9a8a9a8,0x2c000000,0xab64a61f,0x29a829a8,0xa61fab64,
0x80002c00,0x8000ac00,0x80002c00,0x8000ac00,0xac000000,0x2b64a61f,0xa9a829a8,0x261fab64,0x8000ac00,0x29a829a8,0xac000000,0x29a8a9a8,0x2c000000,0xa61f2b64,0xa9a8a9a8,0x2b64a61f,
0x80002c00,0xac000000,0x8000ac00,0x2c000000,0xac000000,0xa61fab64,0x29a8a9a8,0x2b64261f,0x8000ac00,0x29a8a9a8,0x2c000000,0x29a829a8,0x2c000000,0x2b64261f,0x29a829a8,0x261f2b64,
0x2c000000,0x2c000000,0x2c000000,0x2c000000,0x2c000000,0x2b64a61f,0x29a8a9a8,0x261fab64,0x2c000000,0x29a8a9a8,0xac00,0xa9a8a9a8,0x2c000000,0x261fab64,0xa9a8a9a8,0xab64261f,
0x2c000000,0xac00,0xac000000,0x2c00,0x2c000000,0xa61fab64,0xa9a829a8,0x2b64261f,0x2c000000,0xa9a8a9a8,0x2c00,0x29a8a9a8,0x2c000000,0xab64a61f,0x29a829a8,0xa61fab64,
0x2c000000,0xac000000,0x2c000000,0xac000000,0x2c000000,0xab64261f,0x29a8a9a8,0xa61f2b64,0x2c000000,0xa9a829a8,0xac00,0x29a829a8,0x2c000000,0xa61f2b64,0xa9a8a9a8,0x2b64a61f,
0x2c000000,0x2c00,0xac000000,0xac00,0x2c000000,0x261f2b64,0xa9a829a8,0xab64a61f,0x2c000000,0x29a829a8,0x2c00,0xa9a829a8,0x2c000000,0x2b64261f,0x29a829a8,0x261f2b64,
0x2c000000,0x2c000000,0x2c000000,0x2c000000,0xac00,0xa61fab64,0xa9a8a9a8,0xab64a61f,0xac000000,0xa9a829a8,0x2c00,0x29a829a8,0x2c00,0x2b64261f,0x29a8a9a8,0xa61fab64,
0x2c000000,0xac00,0xac000000,0x2c00,0xac00,0xab64261f,0x29a829a8,0x261fab64,0xac000000,0x29a829a8,0xac00,0xa9a829a8,0x2c00,0x261fab64,0xa9a829a8,0x2b64a61f,
0x2c000000,0xac000000,0x2c000000,0xac000000,0xac00,0x261f2b64,0xa9a8a9a8,0x2b64261f,0xac000000,0x29a8a9a8,0x2c00,0xa9a8a9a8,0x2c00,0xab64a61f,0x29a8a9a8,0x261f2b64,
0x2c000000,0x2c00,0xac000000,0xac00,0xac00,0x2b64a61f,0x29a829a8,0xa61f2b64,0xac000000,0xa9a8a9a8,0xac00,0x29a8a9a8,0x2c00,0xa61f2b64,0xa9a829a8,0xab64261f,
0x2c000000,0x2c000000,0x2c000000,0x2c000000,0xac000000,0xab64261f,0xa9a829a8,0xa61f2b64,0x2c000000,0x29a8a9a8,0xac00,0xa9a8a9a8,0xac000000,0xa61f2b64,0x29a829a8,0x2b64a61f,
0x2c000000,0xac00,0xac000000,0x2c00,0xac000000,0x261f2b64,0x29a8a9a8,0xab64a61f,0x2c000000,0xa9a8a9a8,0x2c00,0x29a8a9a8,0xac000000,0x2b64261f,0xa9a8a9a8,0x261f2b64,
0x2c000000,0xac000000,0x2c000000,0xac000000,0xac000000,0x2b64a61f,0xa9a829a8,0x261fab64,0x2c000000,0xa9a829a8,0xac00,0x29a829a8,0xac000000,0x261fab64,0x29a829a8,0xab64261f,
0x2c000000,0x2c00,0xac000000,0xac00,0xac000000,0xa61fab64,0x29a8a9a8,0x2b64261f,0x2c000000,0x29a829a8,0x2c00,0xa9a829a8,0xac000000,0xab64a61f,0xa9a8a9a8,0xa61fab64,
0x2c000000,0x2c000000,0x2c000000,0x2c000000,0x2c00,0x261f2b64,0x29a829a8,0x2b64261f,0xac000000,0xa9a829a8,0x2c00,0x29a829a8,0xac00,0xab64a61f,0xa9a829a8,0x261f2b64,
0x2c000000,0xac00,0xac000000,0x2c00,0x2c00,0x2b64a61f,0xa9a8a9a8,0xa61f2b64,0xac000000,0x29a829a8,0xac00,0xa9a829a8,0xac00,0xa61f2b64,0x29a8a9a8,0xab64261f,
0x2c000000,0xac000000,0x2c000000,0xac000000,0x2c00,0xa61fab64,0x29a829a8,0xab64a61f,0xac000000,0x29a8a9a8,0x2c00,0xa9a8a9a8,0xac00,0x2b64261f,0xa9a829a8,0xa61fab64,
0x2c000000,0x2c00,0xac000000,0xac00,0x2c00,0xab64261f,0xa9a8a9a8,0x261fab64,0xac000000,0xa9a8a9a8,0xac00,0x29a8a9a8,0xac00,0x261fab64,0x29a8a9a8,0x2b64a61f,
0x80002c00,0x223e2bd9,0x261f2b64,0x28722aa7,0x80002c00,0x28722aa7,0x2b64261f,0x2bd9a23e,0x80002c00,0x2aa72872,0x2b64a61f,0x223eabd9,0x80002c00,0x2bd9223e,0x261fab64,0xaaa7a872,
0x80002c00,0x2bd9a23e,0xa61fab64,0xaaa72872,0x80002c00,0x2aa7a872,0xab64a61f,0x223e2bd9,0x80002c00,0x2872aaa7,0xab64261f,0x2bd9223e,0x80002c00,0x223eabd9,0xa61f2b64,0x2872aaa7,
0x80002c00,0xa23eabd9,0x261f2b64,0xa872aaa7,0x80002c00,0xa872aaa7,0x2b64261f,0xabd9223e,0x80002c00,0xaaa7a872,0x2b64a61f,0xa23e2bd9,0x80002c00,0xabd9a23e,0x261fab64,0x2aa72872,
0x80002c00,0xabd9223e,0xa61fab64,0x2aa7a872,0x80002c00,0xaaa72872,0xab64a61f,0xa23eabd9,0x80002c00,0xa8722aa7,0xab64261f,0xabd9a23e,0x80002c00,0xa23e2bd9,0xa61f2b64,0xa8722aa7,
0x29a829a8,0x2aa72872,0x2b64261f,0x2bd9223e,0x29a8a9a8,0x223eabd9,0xa61fab64,0xaaa7a872,0xa9a8a9a8,0xabd9223e,0xa61f2b64,0x28722aa7,0xa9a829a8,0x28722aa7,0x2b64a61f,0xa23eabd9,
0x29a829a8,0x2872aaa7,0xab64a61f,0xa23e2bd9,0x29a8a9a8,0xabd9a23e,0x261f2b64,0x2872aaa7,0xa9a8a9a8,0x223e2bd9,0x261fab64,0xaaa72872,0xa9a829a8,0x2aa7a872,0xab64261f,0x2bd9a23e,
0x29a829a8,0xaaa7a872,0x2b64261f,0xabd9a23e,0x29a8a9a8,0xa23e2bd9,0xa61fab64,0x2aa72872,0xa9a8a9a8,0x2bd9a23e,0xa61f2b64,0xa872aaa7,0xa9a829a8,0xa872aaa7,0x2b64a61f,0x223e2bd9,
0x29a829a8,0xa8722aa7,0xab64a61f,0x223eabd9,0x29a8a9a8,0x2bd9223e,0x261f2b64,0xa8722aa7,0xa9a8a9a8,0xa23eabd9,0x261fab64,0x2aa7a872,0xa9a829a8,0xaaa72872,0xab64261f,0xabd9223e,
0x2c000000,0x2bd9a23e,0x2b64a61f,0x2aa7a872,0xac000000,0xaaa72872,0xa61f2b64,0x223e2bd9,0x2c000000,0x2872aaa7,0xa61fab64,0xabd9a23e,0xac000000,0xa23e2bd9,0x2b64261f,0x2872aaa7,
0x2c000000,0xa23eabd9,0xab64261f,0x28722aa7,0xac000000,0x28722aa7,0x261fab64,0xabd9223e,0x2c000000,0xaaa7a872,0x261f2b64,0x223eabd9,0xac000000,0x2bd9223e,0xab64a61f,0x2aa72872,
0x2c000000,0xabd9223e,0x2b64a61f,0xaaa72872,0xac000000,0x2aa7a872,0xa61f2b64,0xa23eabd9,0x2c000000,0xa8722aa7,0xa61fab64,0x2bd9223e,0xac000000,0x223eabd9,0x2b64261f,0xa8722aa7,
0x2c000000,0x223e2bd9,0xab64261f,0xa872aaa7,0xac000000,0xa872aaa7,0x261fab64,0x2bd9a23e,0x2c000000,0x2aa72872,0x261f2b64,0xa23e2bd9,0xac000000,0xabd9a23e,0xab64a61f,0xaaa7a872,
0x29a8a9a8,0x2872aaa7,0x261fab64,0x223eabd9,0x29a829a8,0x2bd9223e,0x2b64a61f,0x2872aaa7,0xa9a829a8,0x223e2bd9,0x2b64261f,0x2aa7a872,0xa9a8a9a8,0xaaa72872,0x261f2b64,0x2bd9a23e,
0x29a8a9a8,0xaaa7a872,0xa61f2b64,0x2bd9223e,0x29a829a8,0x223eabd9,0xab64261f,0x2aa72872,0xa9a829a8,0x2bd9a23e,0xab64a61f,0x28722aa7,0xa9a8a9a8,0x28722aa7,0xa61fab64,0x223e2bd9,
0x29a8a9a8,0xa8722aa7,0x261fab64,0xa23e2bd9,0x29a829a8,0xabd9a23e,0x2b64a61f,0xa8722aa7,0xa9a829a8,0xa23eabd9,0x2b64261f,0xaaa72872,0xa9a8a9a8,0x2aa7a872,0x261f2b64,0xabd9223e,
0x29a8a9a8,0x2aa72872,0xa61f2b64,0xabd9a23e,0x29a829a8,0xa23e2bd9,0xab64261f,0xaaa7a872,0xa9a829a8,0xabd9223e,0xab64a61f,0xa872aaa7,0xa9a8a9a8,0xa872aaa7,0xa61fab64,0xa23eabd9,
0x2c000000,0x2bd9a23e,0x2b64a61f,0x2aa7a872,0x2c000000,0x2aa7a872,0x261fab64,0xa23eabd9,0x2c000000,0x2872aaa7,0xa61fab64,0xabd9a23e,0x2c000000,0x223eabd9,0xab64a61f,0xa8722aa7,
0x2c000000,0xa23eabd9,0xab64261f,0x28722aa7,0x2c000000,0xa872aaa7,0xa61f2b64,0x2bd9a23e,0x2c000000,0xaaa7a872,0x261f2b64,0x223eabd9,0x2c000000,0xabd9a23e,0x2b64261f,0xaaa7a872,
0x2c000000,0xabd9223e,0x2b64a61f,0xaaa72872,0x2c000000,0xaaa72872,0x261fab64,0x223e2bd9,0x2c000000,0xa8722aa7,0xa61fab64,0x2bd9223e,0x2c000000,0xa23e2bd9,0xab64a61f,0x2872aaa7,
0x2c000000,0x223e2bd9,0xab64261f,0xa872aaa7,0x2c000000,0x28722aa7,0xa61f2b64,0xabd9223e,0x2c000000,0x2aa72872,0x261f2b64,0xa23e2bd9,0x2c000000,0x2bd9223e,0x2b64261f,0x2aa72872,
0x29a8a9a8,0x2872aaa7,0x261fab64,0x223eabd9,0xa9a8a9a8,0xabd9a23e,0xab64261f,0xa8722aa7,0xa9a829a8,0x223e2bd9,0x2b64261f,0x2aa7a872,0x29a829a8,0x2aa7a872,0xa61fab64,0xabd9223e,
0x29a8a9a8,0xaaa7a872,0xa61f2b64,0x2bd9223e,0xa9a8a9a8,0xa23e2bd9,0x2b64a61f,0xaaa7a872,0xa9a829a8,0x2bd9a23e,0xab64a61f,0x28722aa7,0x29a829a8,0xa872aaa7,0x261f2b64,0xa23eabd9,
0x29a8a9a8,0xa8722aa7,0x261fab64,0xa23e2bd9,0xa9a8a9a8,0x2bd9223e,0xab64261f,0x2872aaa7,0xa9a829a8,0xa23eabd9,0x2b64261f,0xaaa72872,0x29a829a8,0xaaa72872,0xa61fab64,0x2bd9a23e,
0x29a8a9a8,0x2aa72872,0xa61f2b64,0xabd9a23e,0xa9a8a9a8,0x223eabd9,0x2b64a61f,0x2aa72872,0xa9a829a8,0xabd9223e,0xab64a61f,0xa872aaa7,0x29a829a8,0x28722aa7,0x261f2b64,0x223e2bd9,
0xac00,0xa23eabd9,0xa61fab64,0xa872aaa7,0x2c00,0x28722aa7,0x2b64261f,0x2bd9a23e,0xac00,0xaaa7a872,0xab64261f,0xa23e2bd9,0x2c00,0x2bd9223e,0x261fab64,0xaaa7a872,
0xac00,0xabd9223e,0x261f2b64,0x2aa7a872,0x2c00,0x2aa7a872,0xab64a61f,0x223e2bd9,0xac00,0xa8722aa7,0x2b64a61f,0xabd9a23e,0x2c00,0x223eabd9,0xa61f2b64,0x2872aaa7,
0xac00,0x223e2bd9,0xa61fab64,0x28722aa7,0x2c00,0xa872aaa7,0x2b64261f,0xabd9223e,0xac00,0x2aa72872,0xab64261f,0x223eabd9,0x2c00,0xabd9a23e,0x261fab64,0x2aa72872,
0xac00,0x2bd9a23e,0x261f2b64,0xaaa72872,0x2c00,0xaaa72872,0xab64a61f,0xa23eabd9,0xac00,0x2872aaa7,0x2b64a61f,0x2bd9223e,0x2c00,0xa23e2bd9,0xa61f2b64,0xa8722aa7,
0xa9a8a9a8,0xaaa7a872,0xab64a61f,0xabd9a23e,0x29a8a9a8,0x223eabd9,0xa61fab64,0xaaa7a872,0x29a829a8,0x2bd9a23e,0x261fab64,0xa872aaa7,0xa9a829a8,0x28722aa7,0x2b64a61f,0xa23eabd9,
0xa9a8a9a8,0xa8722aa7,0x2b64261f,0x223eabd9,0x29a8a9a8,0xabd9a23e,0x261f2b64,0x2872aaa7,0x29a829a8,0xa23eabd9,0xa61f2b64,0x2aa7a872,0xa9a829a8,0x2aa7a872,0xab64261f,0x2bd9a23e,
0xa9a8a9a8,0x2aa72872,0xab64a61f,0x2bd9223e,0x29a8a9a8,0xa23e2bd9,0xa61fab64,0x2aa72872,0x29a829a8,0xabd9223e,0x261fab64,0x28722aa7,0xa9a829a8,0xa872aaa7,0x2b64a61f,0x223e2bd9,
0xa9a8a9a8,0x2872aaa7,0x2b64261f,0xa23e2bd9,0x29a8a9a8,0x2bd9223e,0x261f2b64,0xa8722aa7,0x29a829a8,0x223e2bd9,0xa61f2b64,0xaaa72872,0xa9a829a8,0xaaa72872,0xab64261f,0xabd9223e,
0x3f800000,0x0,0x3f800000,0x0,0x3f800000,0x0,0x3f7fec43,0xbcc90ab0,0x3f800000,0x0,0x3f7fb10f,0xbd48fb30,0x3f800000,0x0,0x3f7f4e6d,0xbd96a905,
0x3f800000,0x0,0x3f7ec46d,0xbdc8bd36,0x3f800000,0x0,0x3f7e1324,0xbdfab273,0x3f800000,0x0,0x3f7d3aac,0xbe164083,0x3f800000,0x0,0x3f7c3b28,0xbe2f10a2,
0x3f800000,0x0,0x3f7b14be,0xbe47c5c2,0x3f800000,0x0,0x3f79c79d,0xbe605c13,0x3f800000,0x0,0x3f7853f8,0xbe78cfcc,0x3f800000,0x0,0x3f76ba07,0xbe888e93,
0x3f800000,0x0,0x3f74fa0b,0xbe94a031,0x3f800000,0x0,0x3f731447,0xbea09ae5,0x3f800000,0x0,0x3f710908,0xbeac7cd4,0x3f800000,0x0,0x3f6ed89e,0xbeb8442a,
0x3f800000,0x0,0x3f800000,0x0,0x3f7ec46d,0xbdc8bd36,0x3f7e1324,0xbdfab273,0x3f7b14be,0xbe47c5c2,0x3f7853f8,0xbe78cfcc,0x3f74fa0b,0xbe94a031,0x3f6ed89e,0xbeb8442a,
0x3f6c835e,0xbec3ef15,0x3f61c598,0xbef15aea,0x3f61c598,0xbef15aea,0x3f514d3d,0xbf13682a,0x3f54db31,0xbf0e39da,0x3f3daef9,0xbf2beb4a,0x3f45e403,0xbf226799,0x3f273656,0xbf41d870,
0x3f3504f3,0xbf3504f3,0x3f0e39da,0xbf54db31,0x3f226799,0xbf45e403,0x3ee63375,0xbf64aa59,0x3f0e39da,0xbf54db31,0x3eac7cd4,0xbf710908,0x3ef15aea,0xbf61c598,0x3e605c13,0xbf79c79d,
0x3ec3ef15,0xbf6c835e,0x3dc8bd36,0xbf7ec46d,0x3e94a031,0xbf74fa0b,0xbcc90ab0,0xbf7fec43,0x3e47c5c2,0xbf7b14be,0xbe164083,0xbf7d3aac,0x3dc8bd36,0xbf7ec46d,0xbe888e93,0xbf76ba07,
0x3f800000,0x0,0x3f800000,0x0,0x3f7b14be,0xbe47c5c2,0x3f79c79d,0xbe605c13,0x3f6c835e,0xbec3ef15,0x3f676bd8,0xbedae880,0x3f54db31,0xbf0e39da,0x3f49d112,0xbf1d7fd1,
0x3f3504f3,0xbf3504f3,0x3f226799,0xbf45e403,0x3f0e39da,0xbf54db31,0x3ee63375,0xbf64aa59,0x3ec3ef15,0xbf6c835e,0x3e78cfcc,0xbf7853f8,0x3e47c5c2,0xbf7b14be,0x3cc90ab0,0xbf7fec43,
0x0,0xbf800000,0xbe47c5c2,0xbf7b14be,0xbe47c5c2,0xbf7b14be,0xbecf7bca,0xbf6a09a7,0xbec3ef15,0xbf6c835e,0xbf187fc0,0xbf4d9f02,0xbf0e39da,0xbf54db31,0xbf41d870,0xbf273656,
0xbf3504f3,0xbf3504f3,0xbf61c598,0xbef15aea,0xbf54db31,0xbf0e39da,0xbf76ba07,0xbe888e93,0xbf6c835e,0xbec3ef15,0xbf7fb10f,0xbd48fb30,0xbf7b14be,0xbe47c5c2,0xbf7c3b28,0x3e2f10a2,
0x3f800000,0x0,0x3f800000,0x0,0x3f74fa0b,0xbe94a031,0x3f731447,0xbea09ae5,0x3f54db31,0xbf0e39da,0x3f4d9f02,0xbf187fc0,0x3f226799,0xbf45e403,0x3f13682a,0xbf514d3d,
0x3ec3ef15,0xbf6c835e,0x3e94a031,0xbf74fa0b,0x3dc8bd36,0xbf7ec46d,0xbcc90ab0,0xbf7fec43,0xbe47c5c2,0xbf7b14be,0xbeac7cd4,0xbf710908,0xbef15aea,0xbf61c598,0xbf1d7fd1,0xbf49d112,
0xbf3504f3,0xbf3504f3,0xbf54db31,0xbf0e39da,0xbf61c598,0xbef15aea,0xbf76ba07,0xbe888e93,0xbf7b14be,0xbe47c5c2,0xbf7fb10f,0x3d48fb30,0xbf7ec46d,0x3dc8bd36,0xbf6ed89e,0x3eb8442a,
0xbf6c835e,0x3ec3ef15,0xbf45e403,0x3f226799,0xbf45e403,0x3f226799,0xbf08f59b,0x3f584853,0xbf0e39da,0x3f54db31,0xbe78cfcc,0x3f7853f8,0xbe94a031,0x3f74fa0b,0x3d96a905,0x3f7f4e6d,
0x3f800000,0x0,0x3f800000,0x0,0x3f7fb10f,0xbd48fb30,0x3f7f4e6d,0xbd96a905,0x3f7ec46d,0xbdc8bd36,0x3f7d3aac,0xbe164083,0x3f7d3aac,0xbe164083,0x3f79c79d,0xbe605c13,
0x3f7b14be,0xbe47c5c2,0x3f74fa0b,0xbe94a031,0x3f7853f8,0xbe78cfcc,0x3f6ed89e,0xbeb8442a,0x3f74fa0b,0xbe94a031,0x3f676bd8,0xbedae880,0x3f710908,0xbeac7cd4,0x3f5ebe05,0xbefc5d27,
0x3f6c835e,0xbec3ef15,0x3f54db31,0xbf0e39da,0x3f676bd8,0xbedae880,0x3f49d112,0xbf1d7fd1,0x3f61c598,0xbef15aea,0x3f3daef9,0xbf2beb4a,0x3f5b941a,0xbf039c3d,0x3f3085bb,0xbf396842,
0x3f54db31,0xbf0e39da,0x3f226799,0xbf45e403,0x3f4d9f02,0xbf187fc0,0x3f13682a,0xbf514d3d,0x3f45e403,0xbf226799,0x3f039c3d,0xbf5b941a,0x3f3daef9,0xbf2beb4a,0x3ee63375,0xbf64aa59,
0x3f800000,0x0,0x3f800000,0x0,0x3f7d3aac,0xbe164083,0x3f7c3b28,0xbe2f10a2,0x3f74fa0b,0xbe94a031,0x3f710908,0xbeac7cd4,0x3f676bd8,0xbedae880,0x3f5ebe05,0xbefc5d27,
0x3f54db31,0xbf0e39da,0x3f45e403,0xbf226799,0x3f3daef9,0xbf2beb4a,0x3f273656,0xbf41d870,0x3f226799,0xbf45e403,0x3f039c3d,0xbf5b941a,0x3f039c3d,0xbf5b941a,0x3eb8442a,0xbf6ed89e,
0x3ec3ef15,0xbf6c835e,0x3e47c5c2,0xbf7b14be,0x3e78cfcc,0xbf7853f8,0x3cc90ab0,0xbf7fec43,0x3dc8bd36,0xbf7ec46d,0xbe164083,0xbf7d3aac,0xbd48fb30,0xbf7fb10f,0xbea09ae5,0xbf731447,
0xbe47c5c2,0xbf7b14be,0xbef15aea,0xbf61c598,0xbeac7cd4,0xbf710908,0xbf1d7fd1,0xbf49d112,0xbef15aea,0xbf61c598,0xbf3daef9,0xbf2beb4a,0xbf187fc0,0xbf4d9f02,0xbf584853,0xbf08f59b,
0x3f800000,0x0,0x3f800000,0x0,0x3f7853f8,0xbe78cfcc,0x3f76ba07,0xbe888e93,0x3f61c598,0xbef15aea,0x3f5b941a,0xbf039c3d,0x3f3daef9,0xbf2beb4a,0x3f3085bb,0xbf396842,
0x3f0e39da,0xbf54db31,0x3ef15aea,0xbf61c598,0x3eac7cd4,0xbf710908,0x3e605c13,0xbf79c79d,0x3dc8bd36,0xbf7ec46d,0xbd48fb30,0xbf7fb10f,0xbe164083,0xbf7d3aac,0xbea09ae5,0xbf731447,
0xbec3ef15,0xbf6c835e,0xbf0e39da,0xbf54db31,0xbf187fc0,0xbf4d9f02,0xbf41d870,0xbf273656,0xbf45e403,0xbf226799,0xbf676bd8,0xbedae880,0xbf676bd8,0xbedae880,0xbf7c3b28,0xbe2f10a2,
0xbf7b14be,0xbe47c5c2,0xbf7ec46d,0x3dc8bd36,0xbf7fb10f,0x3d48fb30,0xbf6ed89e,0x3eb8442a,0xbf74fa0b,0x3e94a031,0xbf4d9f02,0x3f187fc0,0xbf5b941a,0x3f039c3d,0xbf1d7fd1,0x3f49d112,
0x3f800000,0x0,0x3f800000,0x0,0x3f710908,0xbeac7cd4,0x3f6ed89e,0xbeb8442a,0x3f45e403,0xbf226799,0x3f3daef9,0xbf2beb4a,0x3f039c3d,0xbf5b941a,0x3ee63375,0xbf64aa59,
0x3e47c5c2,0xbf7b14be,0x3dc8bd36,0xbf7ec46d,0xbe164083,0xbf7d3aac,0xbe888e93,0xbf76ba07,0xbef15aea,0xbf61c598,0xbf187fc0,0xbf4d9f02,0xbf3daef9,0xbf2beb4a,0xbf584853,0xbf08f59b,
0xbf6c835e,0xbec3ef15,0xbf7b14be,0xbe47c5c2,0xbf7fb10f,0xbd48fb30,0xbf7c3b28,0x3e2f10a2,0xbf74fa0b,0x3e94a031,0xbf5b941a,0x3f039c3d,0xbf4d9f02,0x3f187fc0,0xbf1d7fd1,0x3f49d112,
0xbf0e39da,0x3f54db31,0xbe94a031,0x3f74fa0b,0xbe78cfcc,0x3f7853f8,0x3d96a905,0x3f7f4e6d,0x3dc8bd36,0x3f7ec46d,0x3edae880,0x3f676bd8,0x3edae880,0x3f676bd8,0x3f396842,0x3f3085bb,
0x3f800000,0x0,0x3f7ffb11,0xbc490e90,0x3f800000,0x0,0x3f7fd397,0xbd16c32c,0x3f800000,0x0,0x3f7f84ab,0xbd7b2b74,0x3f800000,0x0,0x3f7f0e58,0xbdafb680,
0x3f800000,0x0,0x3f7e70b0,0xbde1bc2e,0x3f800000,0x0,0x3f7dabcc,0xbe09cf86,0x3f800000,0x0,0x3f7cbfc9,0xbe22abb6,0x3f800000,0x0,0x3f7baccd,0xbe3b6ecf,
0x3f800000,0x0,0x3f7a7302,0xbe541501,0x3f800000,0x0,0x3f791298,0xbe6c9a7f,0x3f800000,0x0,0x3f778bc5,0xbe827dc0,0x3f800000,0x0,0x3f75dec6,0xbe8e9a22,
0x3f800000,0x0,0x3f740bdd,0xbe9aa086,0x3f800000,0x0,0x3f721352,0xbea68f12,0x3f800000,0x0,0x3f6ff573,0xbeb263ef,0x3f800000,0x0,0x3f6db293,0xbebe1d4a,
0x3f7fb10f,0xbd48fb30,0x3f7f84ab,0xbd7b2b74,0x3f7d3aac,0xbe164083,0x3f7baccd,0xbe3b6ecf,0x3f7853f8,0xbe78cfcc,0x3f740bdd,0xbe9aa086,0x3f710908,0xbeac7cd4,0x3f68bf3c,0xbed53641,
0x3f676bd8,0xbedae880,0x3f59f26a,0xbf064b82,0x3f5b941a,0xbf039c3d,0x3f47de65,0xbf1ff6cb,0x3f4d9f02,0xbf187fc0,0x3f32c8c9,0xbf373a23,0x3f3daef9,0xbf2beb4a,0x3f1b02c6,0xbf4bbbf8,
0x3f2beb4a,0xbf3daef9,0x3f00e7e4,0xbf5d2d53,0x3f187fc0,0xbf4d9f02,0x3ec9b953,0xbf6b4b0c,0x3f039c3d,0xbf5b941a,0x3e8e9a22,0xbf75dec6,0x3edae880,0xbf676bd8,0x3e22abb6,0xbf7cbfc9,
0x3eac7cd4,0xbf710908,0x3d16c32c,0xbf7fd397,0x3e78cfcc,0xbf7853f8,0xbdafb680,0xbf7f0e58,0x3e164083,0xbf7d3aac,0xbe541501,0xbf7a7302,0x3d48fb30,0xbf7fb10f,0xbea68f12,0xbf721352,
0x3f7ec46d,0xbdc8bd36,0x3f7e70b0,0xbde1bc2e,0x3f74fa0b,0xbe94a031,0x3f721352,0xbea68f12,0x3f61c598,0xbef15aea,0x3f59f26a,0xbf064b82,0x3f45e403,0xbf226799,0x3f373a23,0xbf32c8c9,
0x3f226799,0xbf45e403,0x3f0b9a6b,0xbf5695e5,0x3ef15aea,0xbf61c598,0x3eb263ef,0xbf6ff573,0x3e94a031,0xbf74fa0b,0x3e09cf86,0xbf7dabcc,0x3dc8bd36,0xbf7ec46d,0xbdafb680,0xbf7f0e58,
0xbdc8bd36,0xbf7ec46d,0xbe9aa086,0xbf740bdd,0xbe94a031,0xbf74fa0b,0xbf00e7e4,0xbf5d2d53,0xbef15aea,0xbf61c598,0xbf2e3bde,0xbf3b8f3b,0xbf226799,0xbf45e403,0xbf531849,0xbf10d3cd,
0xbf45e403,0xbf226799,0xbf6db293,0xbebe1d4a,0xbf61c598,0xbef15aea,0xbf7cbfc9,0xbe22abb6,0xbf74fa0b,0xbe94a031,0xbf7f84ab,0x3d7b2b74,0xbf7ec46d,0xbdc8bd36,0xbf75dec6,0x3e8e9a22,
0x3f7d3aac,0xbe164083,0x3f7cbfc9,0xbe22abb6,0x3f676bd8,0xbedae880,0x3f633c5a,0xbeebcbbb,0x3f3daef9,0xbf2beb4a,0x3f32c8c9,0xbf373a23,0x3f039c3d,0xbf5b941a,0x3ee0924f,0xbf660f88,
0x3e78cfcc,0xbf7853f8,0x3e09cf86,0xbf7dabcc,0xbd48fb30,0xbf7fb10f,0xbe3b6ecf,0xbf7baccd,0xbeac7cd4,0xbf710908,0xbef6e0cb,0xbf604621,0xbf187fc0,0xbf4d9f02,0xbf3b8f3b,0xbf2e3bde,
0xbf4d9f02,0xbf187fc0,0xbf68bf3c,0xbed53641,0xbf710908,0xbeac7cd4,0xbf7e70b0,0xbde1bc2e,0xbf7fb10f,0xbd48fb30,0xbf7a7302,0x3e541501,0xbf7853f8,0x3e78cfcc,0xbf5d2d53,0x3f00e7e4,
0xbf5b941a,0x3f039c3d,0xbf299415,0x3f3fc767,0xbf2beb4a,0x3f3daef9,0xbec9b953,0x3f6b4b0c,0xbedae880,0x3f676bd8,0xbdafb680,0x3f7f0e58,0xbe164083,0x3f7d3aac,0x3e6c9a7f,0x3f791298,
0x3f7fec43,0xbcc90ab0,0x3f7fd397,0xbd16c32c,0x3f7f4e6d,0xbd96a905,0x3f7e70b0,0xbde1bc2e,0x3f7e1324,0xbdfab273,0x3f7baccd,0xbe3b6ecf,0x3f7c3b28,0xbe2f10a2,0x3f778bc5,0xbe827dc0,
0x3f79c79d,0xbe605c13,0x3f721352,0xbea68f12,0x3f76ba07,0xbe888e93,0x3f6b4b0c,0xbec9b953,0x3f731447,0xbea09ae5,0x3f633c5a,0xbeebcbbb,0x3f6ed89e,0xbeb8442a,0x3f59f26a,0xbf064b82,
0x3f6a09a7,0xbecf7bca,0x3f4f7a1f,0xbf15f6d9,0x3f64aa59,0xbee63375,0x3f43e200,0xbf24d225,0x3f5ebe05,0xbefc5d27,0x3f373a23,0xbf32c8c9,0x3f584853,0xbf08f59b,0x3f299415,0xbf3fc767,
0x3f514d3d,0xbf13682a,0x3f1b02c6,0xbf4bbbf8,0x3f49d112,0xbf1d7fd1,0x3f0b9a6b,0xbf5695e5,0x3f41d870,0xbf273656,0x3ef6e0cb,0xbf604621,0x3f396842,0xbf3085bb,0x3ed53641,0xbf68bf3c,
0x3f7f4e6d,0xbd96a905,0x3f7f0e58,0xbdafb680,0x3f79c79d,0xbe605c13,0x3f778bc5,0xbe827dc0,0x3f6ed89e,0xbeb8442a,0x3f68bf3c,0xbed53641,0x3f5ebe05,0xbefc5d27,0x3f531849,0xbf10d3cd,
0x3f49d112,0xbf1d7fd1,0x3f373a23,0xbf32c8c9,0x3f3085bb,0xbf396842,0x3f15f6d9,0xbf4f7a1f,0x3f13682a,0xbf514d3d,0x3ee0924f,0xbf660f88,0x3ee63375,0xbf64aa59,0x3e8e9a22,0xbf75dec6,
0x3ea09ae5,0xbf731447,0x3de1bc2e,0xbf7e70b0,0x3e2f10a2,0xbf7c3b28,0xbd7b2b74,0xbf7f84ab,0x3cc90ab0,0xbf7fec43,0xbe6c9a7f,0xbf791298,0xbdfab273,0xbf7e1324,0xbec9b953,0xbf6b4b0c,
0xbe888e93,0xbf76ba07,0xbf0b9a6b,0xbf5695e5,0xbecf7bca,0xbf6a09a7,0xbf2e3bde,0xbf3b8f3b,0xbf08f59b,0xbf584853,0xbf4bbbf8,0xbf1b02c6,0xbf273656,0xbf41d870,0xbf633c5a,0xbeebcbbb,
0x3f7e1324,0xbdfab273,0x3f7dabcc,0xbe09cf86,0x3f6ed89e,0xbeb8442a,0x3f6b4b0c,0xbec9b953,0x3f514d3d,0xbf13682a,0x3f47de65,0xbf1ff6cb,0x3f273656,0xbf41d870,0x3f15f6d9,0xbf4f7a1f,
0x3ee63375,0xbf64aa59,0x3eb263ef,0xbf6ff573,0x3e605c13,0xbf79c79d,0x3dafb680,0xbf7f0e58,0xbcc90ab0,0xbf7fec43,0xbe3b6ecf,0xbf7baccd,0xbe888e93,0xbf76ba07,0xbee0924f,0xbf660f88,
0xbefc5d27,0xbf5ebe05,0xbf299415,0xbf3fc767,0xbf3085bb,0xbf396842,0xbf5695e5,0xbf0b9a6b,0xbf584853,0xbf08f59b,0xbf740bdd,0xbe9aa086,0xbf731447,0xbea09ae5,0xbf7fd397,0xbd16c32c,
0xbf7f4e6d,0xbd96a905,0xbf791298,0x3e6c9a7f,0xbf7c3b28,0x3e2f10a2,0xbf604621,0x3ef6e0cb,0xbf6a09a7,0x3ecf7bca,0xbf373a23,0x3f32c8c9,0xbf49d112,0x3f1d7fd1,0xbf00e7e4,0x3f5d2d53,
0x3f7c3b28,0xbe2f10a2,0x3f7baccd,0xbe3b6ecf,0x3f5ebe05,0xbefc5d27,0x3f59f26a,0xbf064b82,0x3f273656,0xbf41d870,0x3f1b02c6,0xbf4bbbf8,0x3eb8442a,0xbf6ed89e,0x3e8e9a22,0xbf75dec6,
0x3cc90ab0,0xbf7fec43,0xbdafb680,0xbf7f0e58,0xbea09ae5,0xbf731447,0xbee0924f,0xbf660f88,0xbf1d7fd1,0xbf49d112,0xbf3b8f3b,0xbf2e3bde,0xbf584853,0xbf08f59b,0xbf6db293,0xbebe1d4a,
0xbf79c79d,0xbe605c13,0xbf7ffb11,0xbc490e90,0xbf7e1324,0x3dfab273,0xbf6ff573,0x3eb263ef,0xbf64aa59,0x3ee63375,0xbf3fc767,0x3f299415,0xbf3085bb,0x3f396842,0xbeebcbbb,0x3f633c5a,
0xbecf7bca,0x3f6a09a7,0xbde1bc2e,0x3f7e70b0,0xbd96a905,0x3f7f4e6d,0x3e827dc0,0x3f778bc5,0x3e888e93,0x3f76ba07,0x3f15f6d9,0x3f4f7a1f,0x3f13682a,0x3f514d3d,0x3f5695e5,0x3f0b9a6b,
};
__device__ const uint32_t __attribute__((aligned(16))) TABOM[514] = {
0x3f800000,0x0,0x3f7ffec4,0xbbc90f88,0x3f7ffb11,0xbc490e90,0x3f7ff4e6,0xbc96c9b6,0x3f7fec43,0xbcc90ab0,0x3f7fe129,0xbcfb49ba,0x3f7fd397,0xbd16c32c,0x3f7fc38f,0xbd2fe007,
0x3f7fb10f,0xbd48fb30,0x3f7f9c18,0xbd621469,0x3f7f84ab,0xbd7b2b74,0x3f7f6ac7,0xbd8a200a,0x3f7f4e6d,0xbd96a905,0x3f7f2f9d,0xbda3308c,0x3f7f0e58,0xbdafb680,0x3f7eea9d,0xbdbc3ac3,
0x3f7ec46d,0xbdc8bd36,0x3f7e9bc9,0xbdd53db9,0x3f7e70b0,0xbde1bc2e,0x3f7e4323,0xbdee3876,0x3f7e1324,0xbdfab273,0x3f7de0b1,0xbe039502,0x3f7dabcc,0xbe09cf86,0x3f7d7474,0xbe1008b7,
0x3f7d3aac,0xbe164083,0x3f7cfe73,0xbe1c76de,0x3f7cbfc9,0xbe22abb6,0x3f7c7eb0,0xbe28defc,0x3f7c3b28,0xbe2f10a2,0x3f7bf531,0xbe354098,0x3f7baccd,0xbe3b6ecf,0x3f7b61fc,0xbe419b37,
0x3f7b14be,0xbe47c5c2,0x3f7ac516,0xbe4dee60,0x3f7a7302,0xbe541501,0x3f7a1e84,0xbe5a3997,0x3f79c79d,0xbe605c13,0x3f796e4e,0xbe667c66,0x3f791298,0xbe6c9a7f,0x3f78b47b,0xbe72b651,
0x3f7853f8,0xbe78cfcc,0x3f77f110,0xbe7ee6e1,0x3f778bc5,0xbe827dc0,0x3f772417,0xbe8586ce,0x3f76ba07,0xbe888e93,0x3f764d97,0xbe8b9507,0x3f75dec6,0xbe8e9a22,0x3f756d97,0xbe919ddd,
0x3f74fa0b,0xbe94a031,0x3f748422,0xbe97a117,0x3f740bdd,0xbe9aa086,0x3f73913f,0xbe9d9e78,0x3f731447,0xbea09ae5,0x3f7294f8,0xbea395c5,0x3f721352,0xbea68f12,0x3f718f57,0xbea986c4,
0x3f710908,0xbeac7cd4,0x3f708066,0xbeaf713a,0x3f6ff573,0xbeb263ef,0x3f6f6830,0xbeb554ec,0x3f6ed89e,0xbeb8442a,0x3f6e46be,0xbebb31a0,0x3f6db293,0xbebe1d4a,0x3f6d1c1d,0xbec1071e,
0x3f6c835e,0xbec3ef15,0x3f6be858,0xbec6d529,0x3f6b4b0c,0xbec9b953,0x3f6aab7b,0xbecc9b8b,0x3f6a09a7,0xbecf7bca,0x3f696591,0xbed25a09,0x3f68bf3c,0xbed53641,0x3f6816a8,0xbed8106b,
0x3f676bd8,0xbedae880,0x3f66becc,0xbeddbe79,0x3f660f88,0xbee0924f,0x3f655e0b,0xbee363fa,0x3f64aa59,0xbee63375,0x3f63f473,0xbee900b7,0x3f633c5a,0xbeebcbbb,0x3f628210,0xbeee9479,
0x3f61c598,0xbef15aea,0x3f6106f2,0xbef41f07,0x3f604621,0xbef6e0cb,0x3f5f8327,0xbef9a02d,0x3f5ebe05,0xbefc5d27,0x3f5df6be,0xbeff17b2,0x3f5d2d53,0xbf00e7e4,0x3f5c61c7,0xbf0242b1,
0x3f5b941a,0xbf039c3d,0x3f5ac450,0xbf04f484,0x3f59f26a,0xbf064b82,0x3f591e6a,0xbf07a136,0x3f584853,0xbf08f59b,0x3f577026,0xbf0a48ad,0x3f5695e5,0xbf0b9a6b,0x3f55b993,0xbf0cead0,
0x3f54db31,0xbf0e39da,0x3f53fac3,0xbf0f8784,0x3f531849,0xbf10d3cd,0x3f5233c6,0xbf121eb0,0x3f514d3d,0xbf13682a,0x3f5064af,0xbf14b039,0x3f4f7a1f,0xbf15f6d9,0x3f4e8d90,0xbf173c07,
0x3f4d9f02,0xbf187fc0,0x3f4cae79,0xbf19c200,0x3f4bbbf8,0xbf1b02c6,0x3f4ac77f,0xbf1c420c,0x3f49d112,0xbf1d7fd1,0x3f48d8b3,0xbf1ebc12,0x3f47de65,0xbf1ff6cb,0x3f46e22a,0xbf212ff9,
0x3f45e403,0xbf226799,0x3f44e3f5,0xbf239da9,0x3f43e200,0xbf24d225,0x3f42de29,0xbf26050a,0x3f41d870,0xbf273656,0x3f40d0da,0xbf286605,0x3f3fc767,0xbf299415,0x3f3ebc1b,0xbf2ac082,
0x3f3daef9,0xbf2beb4a,0x3f3ca003,0xbf2d1469,0x3f3b8f3b,0xbf2e3bde,0x3f3a7ca4,0xbf2f61a5,0x3f396842,0xbf3085bb,0x3f385216,0xbf31a81d,0x3f373a23,0xbf32c8c9,0x3f36206c,0xbf33e7bc,
0x3f3504f3,0xbf3504f3,0x3f33e7bc,0xbf36206c,0x3f32c8c9,0xbf373a23,0x3f31a81d,0xbf385216,0x3f3085bb,0xbf396842,0x3f2f61a5,0xbf3a7ca4,0x3f2e3bde,0xbf3b8f3b,0x3f2d1469,0xbf3ca003,
0x3f2beb4a,0xbf3daef9,0x3f2ac082,0xbf3ebc1b,0x3f299415,0xbf3fc767,0x3f286605,0xbf40d0da,0x3f273656,0xbf41d870,0x3f26050a,0xbf42de29,0x3f24d225,0xbf43e200,0x3f239da9,0xbf44e3f5,
0x3f226799,0xbf45e403,0x3f212ff9,0xbf46e22a,0x3f1ff6cb,0xbf47de65,0x3f1ebc12,0xbf48d8b3,0x3f1d7fd1,0xbf49d112,0x3f1c420c,0xbf4ac77f,0x3f1b02c6,0xbf4bbbf8,0x3f19c200,0xbf4cae79,
0x3f187fc0,0xbf4d9f02,0x3f173c07,0xbf4e8d90,0x3f15f6d9,0xbf4f7a1f,0x3f14b039,0xbf5064af,0x3f13682a,0xbf514d3d,0x3f121eb0,0xbf5233c6,0x3f10d3cd,0xbf531849,0x3f0f8784,0xbf53fac3,
0x3f0e39da,0xbf54db31,0x3f0cead0,0xbf55b993,0x3f0b9a6b,0xbf5695e5,0x3f0a48ad,0xbf577026,0x3f08f59b,0xbf584853,0x3f07a136,0xbf591e6a,0x3f064b82,0xbf59f26a,0x3f04f484,0xbf5ac450,
0x3f039c3d,0xbf5b941a,0x3f0242b1,0xbf5c61c7,0x3f00e7e4,0xbf5d2d53,0x3eff17b2,0xbf5df6be,0x3efc5d27,0xbf5ebe05,0x3ef9a02d,0xbf5f8327,0x3ef6e0cb,0xbf604621,0x3ef41f07,0xbf6106f2,
0x3ef15aea,0xbf61c598,0x3eee9479,0xbf628210,0x3eebcbbb,0xbf633c5a,0x3ee900b7,0xbf63f473,0x3ee63375,0xbf64aa59,0x3ee363fa,0xbf655e0b,0x3ee0924f,0xbf660f88,0x3eddbe79,0xbf66becc,
0x3edae880,0xbf676bd8,0x3ed8106b,0xbf6816a8,0x3ed53641,0xbf68bf3c,0x3ed25a09,0xbf696591,0x3ecf7bca,0xbf6a09a7,0x3ecc9b8b,0xbf6aab7b,0x3ec9b953,0xbf6b4b0c,0x3ec6d529,0xbf6be858,
0x3ec3ef15,0xbf6c835e,0x3ec1071e,0xbf6d1c1d,0x3ebe1d4a,0xbf6db293,0x3ebb31a0,0xbf6e46be,0x3eb8442a,0xbf6ed89e,0x3eb554ec,0xbf6f6830,0x3eb263ef,0xbf6ff573,0x3eaf713a,0xbf708066,
0x3eac7cd4,0xbf710908,0x3ea986c4,0xbf718f57,0x3ea68f12,0xbf721352,0x3ea395c5,0xbf7294f8,0x3ea09ae5,0xbf731447,0x3e9d9e78,0xbf73913f,0x3e9aa086,0xbf740bdd,0x3e97a117,0xbf748422,
0x3e94a031,0xbf74fa0b,0x3e919ddd,0xbf756d97,0x3e8e9a22,0xbf75dec6,0x3e8b9507,0xbf764d97,0x3e888e93,0xbf76ba07,0x3e8586ce,0xbf772417,0x3e827dc0,0xbf778bc5,0x3e7ee6e1,0xbf77f110,
0x3e78cfcc,0xbf7853f8,0x3e72b651,0xbf78b47b,0x3e6c9a7f,0xbf791298,0x3e667c66,0xbf796e4e,0x3e605c13,0xbf79c79d,0x3e5a3997,0xbf7a1e84,0x3e541501,0xbf7a7302,0x3e4dee60,0xbf7ac516,
0x3e47c5c2,0xbf7b14be,0x3e419b37,0xbf7b61fc,0x3e3b6ecf,0xbf7baccd,0x3e354098,0xbf7bf531,0x3e2f10a2,0xbf7c3b28,0x3e28defc,0xbf7c7eb0,0x3e22abb6,0xbf7cbfc9,0x3e1c76de,0xbf7cfe73,
0x3e164083,0xbf7d3aac,0x3e1008b7,0xbf7d7474,0x3e09cf86,0xbf7dabcc,0x3e039502,0xbf7de0b1,0x3dfab273,0xbf7e1324,0x3dee3876,0xbf7e4323,0x3de1bc2e,0xbf7e70b0,0x3dd53db9,0xbf7e9bc9,
0x3dc8bd36,0xbf7ec46d,0x3dbc3ac3,0xbf7eea9d,0x3dafb680,0xbf7f0e58,0x3da3308c,0xbf7f2f9d,0x3d96a905,0xbf7f4e6d,0x3d8a200a,0xbf7f6ac7,0x3d7b2b74,0xbf7f84ab,0x3d621469,0xbf7f9c18,
0x3d48fb30,0xbf7fb10f,0x3d2fe007,0xbf7fc38f,0x3d16c32c,0xbf7fd397,0x3cfb49ba,0xbf7fe129,0x3cc90ab0,0xbf7fec43,0x3c96c9b6,0xbf7ff4e6,0x3c490e90,0xbf7ffb11,0x3bc90f88,0xbf7ffec4,
0x0,0xbf800000,
};

#define DEV __device__ __forceinline__
#define NTL(p) __builtin_nontemporal_load(p)
#define TID ((int)__builtin_amdgcn_workitem_id_x())
#define BID ((int)__builtin_amdgcn_workgroup_id_x())

DEV void lds_barrier() {
  __builtin_amdgcn_fence(__ATOMIC_RELEASE, "workgroup", "local");
  __builtin_amdgcn_s_barrier();
  __builtin_amdgcn_fence(__ATOMIC_ACQUIRE, "workgroup", "local");
}
DEV float2 cmul(float2 a, float2 b) { return make_float2(a.x * b.x - a.y * b.y, a.x * b.y + a.y * b.x); }
DEV float bf_lo(uint32_t v) { return __uint_as_float(v << 16); }
DEV float bf_hi(uint32_t v) { return __uint_as_float(v & 0xffff0000u); }
DEV float bf2f(uint16_t v) { return __uint_as_float(((uint32_t)v) << 16); }
typedef __bf16 bf2 __attribute__((ext_vector_type(2)));
typedef float f2v __attribute__((ext_vector_type(2)));
DEV uint32_t pk_bf(float lo, float hi) {
  return __builtin_bit_cast(uint32_t, __builtin_convertvector((f2v){lo, hi}, bf2));
}
DEV float rbf(float f) { return __uint_as_float(pk_bf(f, f) << 16); }
DEV uint32_t pk_h(float lo, float hi) {
  h2 r; r.x = (_Float16)lo; r.y = (_Float16)hi;
  return __builtin_bit_cast(uint32_t, r);
}
DEV uint32_t pkmul_h(uint32_t a, uint32_t b) {
  return __builtin_bit_cast(uint32_t, __builtin_bit_cast(h2, a) * __builtin_bit_cast(h2, b));
}
DEV uint32_t pkmax_u16(uint32_t a, uint32_t b) {
  return __builtin_bit_cast(uint32_t, __builtin_elementwise_max(__builtin_bit_cast(us2, a), __builtin_bit_cast(us2, b)));
}
DEV uint32_t umax(uint32_t a, uint32_t b) { return a > b ? a : b; }
#define DPP(v, ctrl, rm) ((uint32_t)__builtin_amdgcn_update_dpp(0, (int)(v), ctrl, rm, 0xf, false))
DEV uint32_t wave_umax(uint32_t v) {
  v = umax(v, DPP(v, 0x111, 0xf)); v = umax(v, DPP(v, 0x112, 0xf));
  v = umax(v, DPP(v, 0x114, 0xf)); v = umax(v, DPP(v, 0x118, 0xf));
  v = umax(v, DPP(v, 0x142, 0xa)); v = umax(v, DPP(v, 0x143, 0xc));
  return (uint32_t)__builtin_amdgcn_readlane((int)v, 63);
}
DEV uint32_t wave_pkmax(uint32_t v) {
  v = pkmax_u16(v, DPP(v, 0x111, 0xf)); v = pkmax_u16(v, DPP(v, 0x112, 0xf));
  v = pkmax_u16(v, DPP(v, 0x114, 0xf)); v = pkmax_u16(v, DPP(v, 0x118, 0xf));
  v = pkmax_u16(v, DPP(v, 0x142, 0xa)); v = pkmax_u16(v, DPP(v, 0x143, 0xc));
  return (uint32_t)__builtin_amdgcn_readlane((int)v, 63);
}
DEV float wave_max(float v) { return __uint_as_float(wave_umax(__float_as_uint(v))); }
DEV float amax2(float a, float b) { return __builtin_amdgcn_fmed3f(__builtin_fabsf(a), __builtin_fabsf(b), __builtin_inff()); }
DEV float exp2i(int e) {
  e = e < -126 ? -126 : (e > 127 ? 127 : e);
  return __uint_as_float((uint32_t)(e + 127) << 23);
}
DEV float pow2_scale(float mx) {
  uint32_t e = (__float_as_uint(mx) >> 23) & 0xff;
  if (e == 0 || e == 255) return 1.0f;
  return exp2i(14 - ((int)e - 127));
}
DEV f4 mfma_h(u4 a, u4 b, f4 c) {
  return __builtin_amdgcn_mfma_f32_16x16x32_f16(__builtin_bit_cast(h8, a), __builtin_bit_cast(h8, b), c, 0, 0, 0);
}
DEV f4 mfma_b(u4 a, u4 b, f4 c) {
  return __builtin_amdgcn_mfma_f32_16x16x32_bf16(__builtin_bit_cast(b8, a), __builtin_bit_cast(b8, b), c, 0, 0, 0);
}
typedef __amdgpu_buffer_rsrc_t rsrc_t;
DEV rsrc_t mk_rsrc(void* p) { return __builtin_amdgcn_make_buffer_rsrc(p, 0, 0x7fffffff, 0x00020000); }
DEV void st16(rsrc_t r, uint32_t byte_off, u4 v) { __builtin_amdgcn_raw_buffer_store_b128(v, r, byte_off, 0, 16); }

constexpr uint32_t OFF_Z1 = 0;
constexpr uint32_t OFF_Z2 = 4u << 20;
constexpr uint32_t OFF_UT = 8u << 20;
constexpr uint32_t OFF_OT = 12u << 20;
constexpr uint32_t OFF_KR = 16u << 20;
constexpr int KRH = 1792;
constexpr int KRP = 2 * KRH;
constexpr uint32_t OFF_EX = 20u << 20;
constexpr uint32_t OFF_EW = OFF_EX + 1024;
constexpr uint32_t OFF_WT = 21u << 20;
constexpr int WTB = 258 * 256;

constexpr int SMEM1 = 53504;
constexpr int SMEM2 = 49152 + 160 + 2048 + 64;
constexpr int SMEM3 = 31744 + 64 * 256;

DEV u4 tw_pack(f4 re, f4 im, f4 t01, f4 t23) {
  u4 o;
  o[0] = pk_h(re[0] * t01[0] - im[0] * t01[1], re[0] * t01[1] + im[0] * t01[0]);
  o[1] = pk_h(re[1] * t01[2] - im[1] * t01[3], re[1] * t01[3] + im[1] * t01[2]);
  o[2] = pk_h(re[2] * t23[0] - im[2] * t23[1], re[2] * t23[1] + im[2] * t23[0]);
  o[3] = pk_h(re[3] * t23[2] - im[3] * t23[3], re[3] * t23[3] + im[3] * t23[2]);
  return o;
}
DEV u4 lds16(const char* p) { return *reinterpret_cast<const u4*>(p); }
DEV f4 ldsf4(const char* p) { return *reinterpret_cast<const f4*>(p); }
constexpr f4 ZERO4 = {0.f, 0.f, 0.f, 0.f};

DEV int t1_off(int kp) { return kp * 64 + ((kp >> 2) + (kp >> 4)) * 16; }

DEV void a1_block(int b, char* smem, const uint32_t* __restrict__ x, const uint16_t* __restrict__ wr,
                  const uint16_t* __restrict__ wi, const uint32_t* __restrict__ u, char* ws) {
  const int t = TID, w = __builtin_amdgcn_readfirstlane(t >> 6), lane = t & 63, idx = lane & 15, g = lane >> 4;
  const rsrc_t rs = mk_rsrc(ws);
  char* tab = smem;
  char* T = smem + 14336;
  int* slot = reinterpret_cast<int*>(smem + 14336 + 17664);
  u2 xv[4];
  {
    const u2* xr = reinterpret_cast<const u2*>(x + (size_t)(b + 256 * w) * 512);
#pragma unroll
    for (int j = 0; j < 4; ++j) xv[j] = NTL(&xr[16 * (4 * g + j) + idx]);
  }
  u4 c0 = reinterpret_cast<const u4*>(TAB1)[t];
  const int ti1 = min(512 + t, 895);
  u4 c1 = reinterpret_cast<const u4*>(TAB1)[ti1];
  const int rq = lane >> 3, cp = (lane & 7) + 8 * w;
  uint32_t uv[8];
#pragma unroll
  for (int i = 0; i < 8; ++i) uv[i] = u[(64 * (b >> 3) + 8 * rq + i) * 512 + 64 * (b & 7) + cp];
  const int wma = b & 7, wkp = (b >> 3) & 7, wq = b >> 6, par = wma & 1;
  const int wr8 = rq, wside = w >> 2, wj = 32 * wq + (cp & 31);
  const int m0 = wma + 8 * (2 * wkp + (wr8 >> 2)) + 512 * (wr8 & 3);
  const int wc = wside ? 510 - 2 * wj + par : 2 * wj + par;
  uint32_t wa[4], wb[4];
#pragma unroll
  for (int i = 0; i < 4; ++i) {
    int m = m0 + 128 * i;
    int off = (wside ? ((2048 - m) & 2047) : m) * 513 + wc;
    wa[i] = *reinterpret_cast<const uint32_t*>(wr + off);
    wb[i] = *reinterpret_cast<const uint32_t*>(wi + off);
  }
  uint16_t wxr, wxi;
  {
    const int i = t & 31, m = wma + 8 * (2 * wkp + (i >> 4)) + 128 * (i & 15), mn = (2048 - m) & 2047;
    const int off = (wq == 0) ? m * 513 + (par ? 0 : 256) : (wq == 1) ? mn * 513 + (par ? 256 : 512) : (wq == 2) ? mn * 513 : m * 513 + 512;
    const int o2 = (w == 0) ? off : 0;
    wxr = wr[o2]; wxi = wi[o2];
  }
  __builtin_amdgcn_s_setprio(3);
  reinterpret_cast<u4*>(tab)[t] = c0;
  reinterpret_cast<u4*>(tab)[ti1] = c1;
  uint32_t mx = 0;
#pragma unroll
  for (int j = 0; j < 4; ++j) mx = pkmax_u16(mx, pkmax_u16(xv[j].x & 0x7fff7fffu, xv[j].y & 0x7fff7fffu));
  mx = wave_umax(umax(mx & 0xffffu, mx >> 16));
  const int ex = (int)(mx >> 7);
  const int es = (ex == 0 || ex == 255) ? 0 : min(136 - ex, 126);
  const float sa = exp2i(es);
  lds_barrier();
  const char* tl = tab + lane * 16;
  u4 e[2];
  {
    const u4 f0 = lds16(tl), f1 = lds16(tl + 1024), f2 = lds16(tl + 2048), f3 = lds16(tl + 3072);
#pragma unroll
    for (int n3 = 0; n3 < 2; ++n3) {
      u4 d = {xv[0][n3], xv[1][n3], xv[2][n3], xv[3][n3]};
      f4 re = mfma_b(d, f1, ZERO4), im = mfma_b(d, f3, ZERO4);
      re = mfma_b(d, f0, re); im = mfma_b(d, f2, im);
      f4 t01 = ldsf4(tl + (10 + 2 * n3) * 1024) * sa, t23 = ldsf4(tl + (11 + 2 * n3) * 1024) * sa;
      e[n3] = tw_pack(re, im, t01, t23);
    }
  }
  {
    uint32_t mh = 0;
    u4 o[2];
#pragma unroll
    for (int n3 = 0; n3 < 2; ++n3) {
      f4 re = mfma_h(e[n3], lds16(tl + (4 + 2 * n3) * 1024), ZERO4);
      f4 im = mfma_h(e[n3], lds16(tl + (5 + 2 * n3) * 1024), ZERO4);
#pragma unroll
      for (int r = 0; r < 4; ++r) { o[n3][r] = pk_h(re[r], im[r]); mh = pkmax_u16(mh, o[n3][r] & 0x7fff7fffu); }
    }
#pragma unroll
    for (int r = 0; r < 4; ++r)
      *reinterpret_cast<u2*>(T + t1_off(4 * g + r + 16 * idx) + w * 8) = (u2){o[0][r], o[1][r]};
    const uint32_t mb = wave_umax(umax(mh & 0xffffu, mh >> 16));
    const int em = max((int)(mb >> 10), 1) - 15;
    if (lane == 0) { slot[w] = (mb == 0) ? -100000 : (em - es); slot[8 + w] = es; }
  }
  {
    const uint32_t wx = (uint32_t)wxr | ((uint32_t)wxi << 16);
    uint32_t um = 0, wm = wx & 0x7fff7fffu;
#pragma unroll
    for (int i = 0; i < 8; ++i) um = pkmax_u16(um, uv[i] & 0x7fff7fffu);
#pragma unroll
    for (int i = 0; i < 4; ++i) wm = pkmax_u16(wm, pkmax_u16(wa[i] & 0x7fff7fffu, wb[i] & 0x7fff7fffu));
    um = wave_pkmax(umax(um & 0xffffu, um >> 16) | (umax(wm & 0xffffu, wm >> 16) << 16));
    if (lane == 0) slot[16 + w] = (int)um;
    u4 lo, hi;
#pragma unroll
    for (int i = 0; i < 4; ++i) {
      lo[i] = (uv[2 * i] & 0xffffu) | (uv[2 * i + 1] << 16);
      hi[i] = (uv[2 * i] >> 16) | (uv[2 * i + 1] & 0xffff0000u);
    }
    char* bb = smem + 32256 + w * 2048;
    char* bw = bb + (lane & 7) * 256 + ((rq ^ (lane & 7)) << 4);
    const int rl = lane >> 3, rc = ((lane & 7) ^ (rl >> 1)) << 4, rc2 = ((lane & 7) ^ (4 + (rl >> 1))) << 4;
    auto wsync = [] {
      __builtin_amdgcn_fence(__ATOMIC_RELEASE, "wavefront");
      __builtin_amdgcn_wave_barrier();
      __builtin_amdgcn_fence(__ATOMIC_ACQUIRE, "wavefront");
    };
    *reinterpret_cast<u4*>(bw) = lo; *reinterpret_cast<u4*>(bw + 128) = hi;
    wsync();
    {
      const u4 v0 = lds16(bb + lane * 16), v1 = lds16(bb + 1024 + lane * 16);
      const uint32_t uo = OFF_UT + (uint32_t)(b >> 3) * 131072 + (uint32_t)(128 * (b & 7) + 16 * w + rl) * 128;
      st16(rs, uo + rc, v0); st16(rs, uo + 1024 + rc2, v1);
    }
#pragma unroll
    for (int i = 0; i < 4; ++i) {
      lo[i] = (wa[i] & 0xffffu) | (wb[i] << 16);
      hi[i] = (wa[i] >> 16) | (wb[i] & 0xffff0000u);
    }
    wsync();
    *reinterpret_cast<u4*>(bw) = lo; *reinterpret_cast<u4*>(bw + 128) = hi;
    wsync();
    const uint32_t wb0 = OFF_WT + (uint32_t)(b & 63) * WTB;
    {
      const u4 v0 = lds16(bb + lane * 16), v1 = lds16(bb + 1024 + lane * 16);
      const int wj0 = 32 * wq + 8 * (w & 3);
      const int kA = wside ? 2 + 2 * wj0 - par + 2 * (rl >> 1) - (rl & 1) : 2 * wj0 + par + rl;
      const uint32_t wo = wb0 + wside * 128 + kA * 256;
      st16(rs, wo + rc, v0); st16(rs, wo + 8 * 256 + rc2, v1);
    }
    if (t < 32) {
      const int wxo = (wq == 0) ? (par ? 0 : 256) * 256 : (wq == 1) ? (par ? 256 : 0) * 256 + 128 : 257 * 256 + (wq - 2) * 128;
      *reinterpret_cast<uint32_t*>(ws + (wb0 + wxo + t * 4)) = wx;
    }
  }
  lds_barrier();
  {
    const int G = w & 3, mt = w >> 2;
    int E = -100000;
#pragma unroll
    for (int i = 0; i < 8; ++i) E = max(E, slot[i]);
    if (E < -1000) E = -200;
    float r0 = exp2i(min(9 - E - slot[8 + 2 * g], 15)), r1 = exp2i(min(9 - E - slot[9 + 2 * g], 15));
    u4 A = lds16(tl + (8 + mt) * 1024);
    uint32_t p0 = pk_h(r0, r0), p1 = pk_h(r1, r1);
    A[0] = pkmul_h(A[0], p0); A[1] = pkmul_h(A[1], p0); A[2] = pkmul_h(A[2], p1); A[3] = pkmul_h(A[3], p1);
    u4 o0, o1;
#pragma unroll
    for (int tt = 0; tt < 4; ++tt) {
      u4 B = lds16(T + t1_off(64 * G + 4 * idx + tt) + g * 16);
      f4 d = mfma_h(A, B, ZERO4);
      o0[tt] = pk_h(d[0], d[1]); o1[tt] = pk_h(d[2], d[3]);
    }
    const uint32_t zo = OFF_Z1 + ((uint32_t)(b * 8 + 4 * mt + g) * 512 + 64 * G + 4 * idx) * 4;
    st16(rs, zo, o0); st16(rs, zo + 1024, o1);
    if (t == 0) {
      uint32_t um = 0;
#pragma unroll
      for (int i = 0; i < 8; ++i) um = pkmax_u16(um, (uint32_t)slot[16 + i]);
      reinterpret_cast<uint32_t*>(ws + OFF_EX)[b] = (uint32_t)(E + 512) | ((((um & 0xffffu) >> 7) - 127 + 512) << 16);
      reinterpret_cast<uint32_t*>(ws + OFF_EW)[b] = umax(um >> 23, 32u) - 127 + 512;
    }
  }
}

DEV float2 zpow(float ar, float ai, float p) {
  float mag = __builtin_amdgcn_exp2f((ar * p) * 1.44269504088896340736f);
  float ang = ai * p;
  const float C_HI = 0.15915494309189533577f;
  const float C_LO = (float)(0.15915494309189533577 - (double)0.15915494309189533577f);
  float hi = ang * C_HI;
  float lo = fmaf(ang, C_HI, -hi) + ang * C_LO;
  float fr = (hi - rintf(hi)) + lo;
  return make_float2(mag * __builtin_amdgcn_cosf(fr), mag * __builtin_amdgcn_sinf(fr));
}
DEV float exp_acc(float x) {
  const float L_HI = 1.44269504088896340736f;
  const float L_LO = (float)(1.44269504088896340736 - (double)1.44269504088896340736f);
  float t = x * L_HI;
  float r = fmaf(x, L_HI, -t) + x * L_LO;
  float e = __builtin_amdgcn_exp2f(t);
  return fmaf(e * 0.69314718056f, r, e);
}
DEV uint32_t cmul_h(uint32_t a, uint32_t b) {
  h2 A = __builtin_bit_cast(h2, a), B = __builtin_bit_cast(h2, b);
  h2 t = (h2){A.x, A.x} * B;
  h2 r = __builtin_elementwise_fma((h2){-A.y, A.y}, (h2){B.y, B.x}, t);
  return __builtin_bit_cast(uint32_t, r);
}
DEV uint32_t pk_c(float2 a) { return pk_h(a.x, a.y); }
DEV float wave_sum(float v) {
#define DPPF(v, ctrl, rm) __uint_as_float(DPP(__float_as_uint(v), ctrl, rm))
  v += DPPF(v, 0x111, 0xf); v += DPPF(v, 0x112, 0xf); v += DPPF(v, 0x114, 0xf); v += DPPF(v, 0x118, 0xf);
  v += DPPF(v, 0x142, 0xa); v += DPPF(v, 0x143, 0xc);
  return __uint_as_float((uint32_t)__builtin_amdgcn_readlane((int)__float_as_uint(v), 63));
}

#define CODE_ADDR(sym, out) do { unsigned long long _pc; int _o; asm volatile("s_getpc_b64 %0\n\ts_mov_b32 %1, " sym "@rel32@lo+4" : "=s"(_pc), "=s"(_o)); out = _pc + (long long)_o; } while (0)
constexpr int PF_K1 = 9856, PF_K2 = 16512, PF_K3 = 2688;
constexpr int PF_BLOCKS = (PF_K1 + PF_K2 + PF_K3 + 14336 + 10240 + 10240 + 2048 + 8191) / 8192;
DEV uint32_t prefetch_lines(int kb, int lane) {
  unsigned long long a1, a2, a3;
  CODE_ADDR("gs_k1", a1); CODE_ADDR("gs_k2", a2); CODE_ADDR("gs_k3", a3);
  int o = ((kb >> 3) * 64 + lane) * 128;
  const char* p = nullptr;
  if (o < PF_K1) p = (const char*)a1 + o;
  else if ((o -= PF_K1) < 14336) p = (const char*)TAB1 + o;
  else if ((o -= 14336) < PF_K2) p = (const char*)a2 + o;
  else if ((o -= PF_K2) < 10240) p = (const char*)TAB2 + (kb & 7) * 10240 + o;
  else if ((o -= 10240) < PF_K3) p = (const char*)a3 + o;
  else if ((o -= PF_K3) < 10240) p = (const char*)TAB3 + o;
  else if ((o -= 10240) < 2048) p = (const char*)TABOM + o;
  uint32_t v = 0;
  if (p) v = *reinterpret_cast<const volatile uint32_t*>(p);
  return v;
}

DEV void kg_block(int kb, char* smem, const uint16_t* __restrict__ lar, const uint16_t* __restrict__ aim,
                  const uint16_t* __restrict__ cre, const uint16_t* __restrict__ cim,
                  const uint16_t* __restrict__ logdt, char* ws) {
  const int t = TID, w = __builtin_amdgcn_readfirstlane(t >> 6), lane = t & 63;
  const int ch = w >> 2, role = ((w >> 1) ^ ch) & 1, n = t & 127;
  const int d = 2 * kb + ch;
  uint32_t pfv = 0;
  if (kb < 8 * PF_BLOCKS && w == 7) pfv = prefetch_lines(kb, lane);
  char* base = smem + ch * (52 * 512);
  float* slot = reinterpret_cast<float*>(smem + 2 * 52 * 512);
  char* pb = base + (n >> 4) * (52 * 64) + (n & 15) * 4;
  char* pb2 = base + (n >> 4) * (52 * 64) + ((n & 15) ^ 8) * 4;
  auto put = [&](int row, uint32_t val) {
    const int r = (row < 20 ? row : row - 20) & 15;
    *reinterpret_cast<uint32_t*>((r & 8 ? pb2 : pb) + row * 64) = val;
  };
  if (role == 0) {
    const uint16_t v_dt = logdt[d], v_lar = lar[d * 128 + n], v_ai = aim[d * 128 + n];
    const uint16_t v_cr = cre[d * 128 + n], v_ci = cim[d * 128 + n];
    const float dt = rbf(exp_acc(bf2f(v_dt)));
    const float ar = -rbf(exp_acc(bf2f(v_lar))) * dt;
    const float ai = bf2f(v_ai) * dt;
    const float S = 16384.0f;
    float2 z1 = zpow(ar, -ai, 1.0f), z256 = zpow(ar, -ai, 256.0f);
    float2 z2 = cmul(z1, z1), z3 = cmul(z2, z1), z4 = cmul(z2, z2), z5 = cmul(z4, z1), z6 = cmul(z3, z3), z7 = cmul(z4, z3);
    const uint32_t h2_ = pk_h(z2.x * S, z2.y * S), h4_ = pk_h(z4.x * S, z4.y * S), h6 = pk_h(z6.x * S, z6.y * S);
    put(0, 0x7400u); put(1, pk_h(z1.x * S, z1.y * S)); put(2, h2_); put(3, pk_h(z3.x * S, z3.y * S));
    put(4, h4_); put(5, pk_h(z5.x * S, z5.y * S)); put(6, h6); put(7, pk_h(z7.x * S, z7.y * S));
    float2 c = z256;
    uint32_t hc = pk_c(c);
    put(8, pk_h(c.x * S, c.y * S)); put(9, cmul_h(h2_, hc)); put(10, cmul_h(h4_, hc)); put(11, cmul_h(h6, hc));
    c = cmul(c, z256);
    put(12, pk_h(c.x * S, c.y * S)); put(13, cmul_h(h4_, pk_c(c)));
    c = cmul(c, z256);
    put(14, pk_h(c.x * S, c.y * S)); put(15, cmul_h(h4_, pk_c(c)));
    c.x *= S; c.y *= S;
#pragma unroll
    for (int i = 0; i < 4; ++i) { c = cmul(c, z256); put(16 + i, pk_c(c)); }
    c = cmul(c, z256);
    float k2048 = wave_sum((bf2f(v_cr) * c.x + bf2f(v_ci) * c.y) * dt);
    if (lane == 0) slot[8 + 2 * ch + (w & 1)] = k2048;
  } else {
    const uint16_t v_dt = logdt[d], v_lar = lar[d * 128 + n], v_ai = aim[d * 128 + n];
    const uint16_t v_c0r = cre[d * 128 + n], v_c0i = cim[d * 128 + n], v_c1r = cre[d * 128 + (n ^ 64)], v_c1i = cim[d * 128 + (n ^ 64)];
    const float dt = rbf(exp_acc(bf2f(v_dt)));
    const float ar = -rbf(exp_acc(bf2f(v_lar))) * dt;
    const float ai = bf2f(v_ai) * dt;
    uint32_t cm = umax(umax(v_c0r & 0x7fffu, v_c0i & 0x7fffu), umax(v_c1r & 0x7fffu, v_c1i & 0x7fffu));
    cm = wave_umax(cm);
    const float sL = pow2_scale(__uint_as_float(cm << 16) * dt) * 0.5f;
    float2 w1 = zpow(ar, ai, 8.0f), w4 = zpow(ar, ai, 32.0f);
    float2 w2 = cmul(w1, w1), w3 = cmul(w2, w1);
    const uint32_t h1 = pk_c(w1), h2_ = pk_c(w2), h3 = pk_c(w3);
    float2 c = make_float2(bf2f(v_c0r) * dt * sL, bf2f(v_c0i) * dt * sL);
#pragma unroll
    for (int i = 0; i < 8; ++i) {
      uint32_t hc = pk_c(c);
      put(20 + 4 * i, hc);
      put(21 + 4 * i, cmul_h(hc, h1));
      put(22 + 4 * i, cmul_h(hc, h2_));
      put(23 + 4 * i, cmul_h(hc, h3));
      if (i < 7) c = cmul(c, w4);
    }
    if (lane == 0) slot[12 + ch] = sL;
  }
  lds_barrier();
  const int mt = (w >> 1) & 1, nt = w & 1;
  const int r = lane & 15, g = lane >> 4;
  f4 acc0 = ZERO4, acc1 = ZERO4;
  {
    const int gs = (g ^ ((r >> 2) & 2)) * 16;
    const char* pa = base + (16 * mt + r) * 64 + gs;
    const char* pq = base + (20 + 16 * nt + r) * 64 + gs;
#pragma unroll
    for (int ks = 0; ks < 8; ks += 2) {
      acc0 = mfma_h(lds16(pa + ks * 3328), lds16(pq + ks * 3328), acc0);
      acc1 = mfma_h(lds16(pa + (ks + 1) * 3328), lds16(pq + (ks + 1) * 3328), acc1);
    }
  }
  const int j = 16 * nt + r;
  int i0, s1, s2;
  if (mt) { i0 = 752 + 95 * (j & 1) - (j >> 1); s1 = -16; s2 = -32; }
  else {
    i0 = g < 2 ? 271 - 4 * g - 8 * j : (g == 2 ? 383 - 2 * j : 575 - j);
    s1 = g < 2 ? -1 : 96;
    s2 = g < 2 ? -2 : (g == 2 ? -1 : -32);
  }
  f4 acc = acc0 + acc1;
  const bool own = (mt == 0 || g == 0);
  float mx = own ? amax2(amax2(acc[0], acc[1]), amax2(acc[2], acc[3])) : 0.f;
  mx = wave_max(mx);
  lds_barrier();
  float* img = reinterpret_cast<float*>(smem) + ch * 896;
  if (own) { img[i0] = acc[0]; img[i0 + s1] = acc[1]; img[i0 + s2] = acc[2]; img[i0 + s1 + s2] = acc[3]; }
  if (lane == 0) slot[w] = mx;
  lds_barrier();
  {
    const int c = t & 127;
    if (role == 1 && c < 112) {
      const unsigned long long M0 = 0xff0ff03fffffffcull, M1 = 0x3fc3fcff0ffull;
      const unsigned vb = (unsigned)((w & 1) ? (M1 >> lane) : ((M0 >> lane) | (lane == 63 ? 2u : 0u)));
      f4 m4 = *reinterpret_cast<const f4*>(slot + 4 * ch);
      const float k2048 = (slot[8 + 2 * ch] + slot[9 + 2 * ch]) * slot[12 + ch];
      const float k256 = img[383], k512 = img[575];
      f4 a = *reinterpret_cast<const f4*>(img + 8 * c), bq = *reinterpret_cast<const f4*>(img + 8 * c + 4);
      float nx = img[8 * c + 8];
      const float sK = pow2_scale(amax2(amax2(amax2(m4.x, m4.y), amax2(m4.z, m4.w)), k2048)) * 0.5f;
      auto fix = [&](int c, float v) { return c == 2 ? v - k256 : (c == 52 ? v - k512 : (c == 86 ? k2048 : v)); };
      a[0] = fix(c, a[0]); nx = fix(c + 1, nx);
      u4 o = (u4){pk_h(a[0] * sK, a[1] * sK), pk_h(a[2] * sK, a[3] * sK), pk_h(bq[0] * sK, bq[1] * sK), pk_h(bq[2] * sK, bq[3] * sK)};
      uint32_t on = pk_h(nx * sK, 0.f);
      if (!(vb & 1)) o = (u4){0u, 0u, 0u, 0u};
      if (!(vb & 2)) on = 0;
      u4 o2 = (u4){__builtin_amdgcn_alignbit(o[1], o[0], 16), __builtin_amdgcn_alignbit(o[2], o[1], 16),
                   __builtin_amdgcn_alignbit(o[3], o[2], 16), __builtin_amdgcn_alignbit(on, o[3], 16)};
      if (c == 110) {
        float ks = (2.0f / 16384.0f) / slot[12 + ch] / sK;
        o = (u4){__float_as_uint(k256 * sK), __float_as_uint(k512 * sK), __float_as_uint(img[752] * sK), __float_as_uint(ks)};
      }
      const rsrc_t rs = mk_rsrc(ws);
      st16(rs, OFF_KR + (uint32_t)d * KRP + c * 16, o);
      st16(rs, OFF_KR + (uint32_t)d * KRP + KRH + c * 16, o2);
    }
  }
  asm volatile("" :: "v"(pfv));
}

DEV void k1_body(const uint32_t* __restrict__ x, const uint16_t* __restrict__ wr,
                                          const uint16_t* __restrict__ wi, const uint32_t* __restrict__ u,
                                          const uint16_t* __restrict__ lar, const uint16_t* __restrict__ aim,
                                          const uint16_t* __restrict__ cre, const uint16_t* __restrict__ cim,
                                          const uint16_t* __restrict__ logdt, char* ws) {
  __shared__ __attribute__((aligned(16))) char smem[SMEM1];
  const int b = BID;
  if (b >= 512) a1_block(b - 512, smem, x, wr, wi, u, ws);
  else kg_block(b, smem, lar, aim, cre, cim, logdt, ws);
}
#define K1ARGS const uint32_t* __restrict__ x, const uint16_t* __restrict__ wr, const uint16_t* __restrict__ wi, \
               const uint32_t* __restrict__ u, const uint16_t* __restrict__ lar, const uint16_t* __restrict__ aim, \
               const uint16_t* __restrict__ cre, const uint16_t* __restrict__ cim, const uint16_t* __restrict__ logdt, char* ws
extern "C" __global__ __launch_bounds__(512, 6) void gs_k1(K1ARGS) { k1_body(x, wr, wi, u, lar, aim, cre, cim, logdt, ws); }

struct __attribute__((packed, aligned(4))) u4a4 { uint32_t a, b, c, d; };

DEV uint32_t max4(u4 q) { return umax(umax(q[0], q[1]), umax(q[2], q[3])); }
DEV uint32_t red_exps(u4 ex, u4 ew) {
  uint32_t z = umax(umax(ex[0] & 0xffffu, ex[1] & 0xffffu), umax(ex[2] & 0xffffu, ex[3] & 0xffffu));
  return wave_pkmax(z | (max4(ew) << 16));
}

#define SCHED_FENCE() __builtin_amdgcn_sched_barrier(0)
DEV void a2_unit(const char* tl, u4& a, u4& b, const f4& sx, const f4& sy, const f4& dx, const f4& dy, float sg, float kp) {
  f4 ar, ai, br, bi;
  const u4 f2r = lds16(tl + 4096), f2i = lds16(tl + 5120);
  {
    const u4 g1r = lds16(tl), g1i = lds16(tl + 1024);
    const f4 ta0 = ldsf4(tl + 6144), ta1 = ldsf4(tl + 7168);
    f4 re = mfma_h(a, g1r, ZERO4), im = mfma_h(a, g1i, ZERO4);
    f4 re2 = mfma_h(b, g1r, ZERO4), im2 = mfma_h(b, g1i, ZERO4);
    u4 e = tw_pack(re, im, ta0, ta1);
    ar = mfma_h(f2r, e, ZERO4); ai = mfma_h(f2i, e, ZERO4);
    e = tw_pack(re2, im2, ta0, ta1);
    br = mfma_h(f2r, e, ZERO4); bi = mfma_h(f2i, e, ZERO4);
  }
  SCHED_FENCE();
  u4 z1, z2;
#pragma unroll
  for (int r = 0; r < 4; ++r) {
    float u1x = sg * ar[r] - kp * bi[r], u1y = sg * ai[r] + kp * br[r];
    float u2x = sg * br[r] - kp * ai[r], u2y = sg * bi[r] + kp * ar[r];
    float z1x = sx[r] * ar[r] - sy[r] * ai[r] + dx[r] * u1x - dy[r] * u1y;
    float z1y = sx[r] * ai[r] + sy[r] * ar[r] + dx[r] * u1y + dy[r] * u1x;
    float z2x = sx[r] * br[r] - sy[r] * bi[r] - dx[r] * u2x + dy[r] * u2y;
    float z2y = sx[r] * bi[r] + sy[r] * br[r] - dx[r] * u2y - dy[r] * u2x;
    z1[r] = pk_h(z1x, -z1y); z2[r] = pk_h(z2x, -z2y);
  }
  SCHED_FENCE();
  {
    const u4 g4r = lds16(tl + 2048), g4i = lds16(tl + 3072);
    const f4 tb0 = ldsf4(tl + 8192), tb1 = ldsf4(tl + 9216);
    f4 re = mfma_h(z1, f2r, ZERO4), im = mfma_h(z1, f2i, ZERO4);
    f4 re2 = mfma_h(z2, f2r, ZERO4), im2 = mfma_h(z2, f2i, ZERO4);
    u4 e = tw_pack(re, im, tb0, tb1);
    re = mfma_h(g4r, e, ZERO4); im = mfma_h(g4i, e, ZERO4);
    e = tw_pack(re2, im2, tb0, tb1);
    re2 = mfma_h(g4r, e, ZERO4); im2 = mfma_h(g4i, e, ZERO4);
#pragma unroll
    for (int r = 0; r < 4; ++r) { a[r] = pk_h(re[r], im[r]); b[r] = pk_h(re2[r], -im2[r]); }
  }
}

DEV void a2_block(int b, char* smem, char* ws) {
  const int t = TID, w = __builtin_amdgcn_readfirstlane(t >> 6), lane = t & 63, idx = lane & 15, g = lane >> 4;
  const int ma = b & 7, kg = b >> 3, mb = (8 - ma) & 7;
  char* tab = smem;
  char* S = smem + 10240;
  const uint32_t* Z1 = reinterpret_cast<const uint32_t*>(ws + OFF_Z1);
  uint32_t* Z2 = reinterpret_cast<uint32_t*>(ws + OFF_Z2);
  const int e = t & 7, n2 = ((t >> 3) & 7) + 8 * ((w >> 1) & 1), plane = w & 1, hh = w >> 2;
  const int zcol = plane ? ((505 - 8 * kg + e) & 511) : 8 * kg + e;
  const int zrow = ((128 * hh + n2) * 8 + (plane ? mb : ma)) * 512;
  const u4* wt = reinterpret_cast<const u4*>(ws + (OFF_WT + (uint32_t)(ma + 8 * (idx >> 1)) * WTB)) + (idx & 1) * 4 + g;
  uint32_t x0 = 0;
  u4 wextra = {0u, 0u, 0u, 0u};
  const bool mid = (kg == 17);
  if (mid) x0 = Z1[zrow + 256 + e * (16 * 8 * 512)];
  if (mid || kg == 0) wextra = wt[(mid ? 256 : 257) * 16 + 8 * (w & 1)];
  uint32_t d[8];
#pragma unroll
  for (int i = 0; i < 8; ++i) d[i] = Z1[zrow + zcol + i * (16 * 8 * 512)];
  const u4* tsrc = reinterpret_cast<const u4*>(TAB2) + ma * 640;
  const u4 c0 = tsrc[t];
  const uint32_t c1 = reinterpret_cast<const uint32_t*>(tsrc + 512)[t];
  const int k = 8 * kg + w;
  const u4 wa = wt[k * 16], wb = wt[k * 16 + 8];
  const float omx = __uint_as_float(TABOM[2 * k]), omy = __uint_as_float(TABOM[2 * k + 1]);
  uint32_t* FT = reinterpret_cast<uint32_t*>(S + 18 * 1296 + 2048);
  if (w == 7) {
    const u4 exq = reinterpret_cast<const u4*>(ws + OFF_EX)[lane], ewq = reinterpret_cast<const u4*>(ws + OFF_EW)[lane];
    const uint32_t er = red_exps(exq, ewq);
    const int EZ = (int)(er & 0xffffu);
#pragma unroll
    for (int i = 0; i < 4; ++i) {
      const int fe = (int)(exq[i] & 0xffffu) - EZ + 15;
      FT[(4 * (lane & 3) + i) * 16 + (lane >> 2)] = (fe > 0 ? (uint32_t)fe << 10 : 0u) * 0x10001u;
    }
    if (lane == 0) FT[256] = er >> 16;
  }
  __builtin_amdgcn_s_setprio(3);
  reinterpret_cast<u4*>(tab)[t] = c0;
  reinterpret_cast<uint32_t*>(tab + 8192)[t] = c1;
  {
    char* sp = S + (plane ? 15 - e : e) * 1296 + (n2 * 20 + 8 * hh) * 4;
    *reinterpret_cast<u4*>(sp) = (u4){d[0], d[1], d[2], d[3]};
    *reinterpret_cast<u4*>(sp + 16) = (u4){d[4], d[5], d[6], d[7]};
    if ((mid || kg == 0) && (w >> 1) == 1) *reinterpret_cast<u4*>(S + 18 * 1296 + (w & 1) * 1024 + lane * 16) = wextra;
    if (mid) *reinterpret_cast<uint32_t*>(S + (16 + plane) * 1296 + (n2 * 20 + 8 * hh + e) * 4) = x0;
  }
  lds_barrier();
  {
    const char* tl = tab + lane * 16;
    char* pa = S + w * 1296 + (idx * 20 + 4 * g) * 4;
    char* pb = pa + 8 * 1296;
    const int EW = (int)FT[256] - 512;
    const u4 ft = lds16(reinterpret_cast<const char*>(FT) + (idx * 16 + 4 * g) * 4);
    const float sf = 0.5f * exp2i(-(EW + 2));
    float sg = omy * sf, kp = omx * sf;
    f4 sx, sy, dx, dy;
    auto setw = [&](const u4& wa, const u4& wb) {
#pragma unroll
      for (int r = 0; r < 4; ++r) {
        float ax = bf_lo(wa[r]), ay = bf_hi(wa[r]), bx = bf_lo(wb[r]), by = bf_hi(wb[r]);
        sx[r] = (ax + bx) * sf; sy[r] = (ay - by) * sf; dx[r] = ax - bx; dy[r] = ay + by;
      }
    };
    setw(wa, wb);
    if (k == 0) {
      const u4 wx0 = lds16(S + 18 * 1296 + lane * 16), wx1 = lds16(S + 18 * 1296 + 1024 + lane * 16);
#pragma unroll
      for (int r = 0; r < 4; ++r) {
        float ax = bf_lo(wx0[r]), ay = -bf_hi(wx0[r]), bx = bf_lo(wx1[r]), by = bf_hi(wx1[r]);
        sx[r] = 0.5f * (sx[r] + (ax + bx) * sf); sy[r] = 0.5f * (sy[r] + (ay + by) * sf);
        dx[r] = 0.5f * (dx[r] + (ax - bx)); dy[r] = 0.5f * (dy[r] + (ay - by));
      }
    }
#pragma unroll 1
    for (int pass = 0;; ++pass) {
      u4 a = lds16(pa), bb = lds16(pb);
#pragma unroll
      for (int r = 0; r < 4; ++r) { a[r] = pkmul_h(a[r], ft[r]); bb[r] = pkmul_h(bb[r], ft[r] | 0x80000000u); }
      a2_unit(tl, a, bb, sx, sy, dx, dy, sg, kp);
      *reinterpret_cast<u4*>(pa) = a;
      *reinterpret_cast<u4*>(pb) = bb;
      if (!mid || w != 0 || pass) break;
      setw(lds16(S + 18 * 1296 + lane * 16), lds16(S + 18 * 1296 + 1024 + lane * 16));
      sg = -sf; kp = 0.0f;
      pa += 16 * 1296; pb = pa + 1296;
    }
  }
  lds_barrier();
  {
    int t2 = TID; asm volatile("" : "+v"(t2));
    const int half = t2 & 1, n2 = (t2 >> 1) & 15, plane = (t2 >> 7) & 1, n1 = 4 * ((t2 >> 5) & 3) + 2 * (t2 >> 8);
    const int zrow = ((16 * n1 + n2) * 8 + (plane ? mb : ma)) * 512;
    const int zoff = zrow + (plane ? 505 - 8 * kg : 8 * kg) + 4 * half;
    const bool wrap = (kg == 0) && plane && half;
    const char* sp = S + (n2 * 20 + n1) * 4 + (plane ? (15 - 4 * half) : 4 * half) * 1296;
    const int st = plane ? -1296 : 1296;
    u2 q0 = *reinterpret_cast<const u2*>(sp), q1 = *reinterpret_cast<const u2*>(sp + st);
    u2 q2 = *reinterpret_cast<const u2*>(sp + 2 * st), q3 = *reinterpret_cast<const u2*>(sp + 3 * st);
    if (!wrap) {
      *reinterpret_cast<u4a4*>(Z2 + zoff) = (u4a4){q0.x, q1.x, q2.x, q3.x};
      *reinterpret_cast<u4a4*>(Z2 + zoff + 16 * 8 * 512) = (u4a4){q0.y, q1.y, q2.y, q3.y};
    } else {
      Z2[zoff] = q0.x; Z2[zoff + 1] = q1.x; Z2[zoff + 2] = q2.x;
      Z2[zoff + 16 * 8 * 512] = q0.y; Z2[zoff + 16 * 8 * 512 + 1] = q1.y; Z2[zoff + 16 * 8 * 512 + 2] = q2.y;
    }
    if (kg == 17 && !half && !plane) {
      u2 q = *reinterpret_cast<const u2*>(S + (n2 * 20 + n1) * 4 + 16 * 1296);
      Z2[zrow + 256] = q.x; Z2[zrow + 256 + 16 * 8 * 512] = q.y;
    }
  }
}

typedef short s4 __attribute__((ext_vector_type(4)));
DEV u2 lds_tr(const char* p) {
  auto q = (__attribute__((address_space(3))) s4*)(p);
  return __builtin_bit_cast(u2, __builtin_amdgcn_ds_read_tr16_b64_v4i16(q));
}
DEV u4 lds_tr2(const char* p, int step) { u2 a = lds_tr(p), b = lds_tr(p + step); return (u4){a.x, a.y, b.x, b.y}; }
DEV u4 ldg_a4(const char* p) { u4a4 q = *reinterpret_cast<const u4a4*>(p); return (u4){q.a, q.b, q.c, q.d}; }
DEV uint32_t pkadd_h(uint32_t a, uint32_t b) {
  return __builtin_bit_cast(uint32_t, __builtin_bit_cast(h2, a) + __builtin_bit_cast(h2, b));
}
DEV uint32_t shr16(uint32_t lo, uint32_t hi) { return __builtin_amdgcn_alignbit(hi, lo, 16); }
DEV uint32_t cvt_u(uint32_t v, float s) { return pk_h(bf_lo(v) * s, bf_hi(v) * s); }

constexpr int CV_CH = 24576 + 80;
constexpr int UA0 = 512 + 32;
constexpr int U20 = 4944 + 512;
constexpr int B30 = 9040 + 1040;
constexpr int B50 = 13664 + 1024;
constexpr int B90 = 17760 + 32;
constexpr int B70 = 22560 + 32;
constexpr int KC0 = 19808;
constexpr int P0O = 0;
constexpr int P1O = 8448 - 1024;
constexpr int P2O = 15616 - 2048;
DEV int p2sw(int c) { return c ^ ((c >> 3) & 7); }

template <int ROLE> DEV void cv_body(int cbi, int w, char* smem, const uint16_t* __restrict__ dskip, char* ws) {
  const int t = TID, lane = t & 63, idx = lane & 15, g = lane >> 4;
  const int d0 = 2 * cbi;
  const int lch = (t >> 3) & 1, q = (t >> 4) * 8 + (t & 7);
  const uint32_t ubase = OFF_UT + (uint32_t)d0 * 128;
  const u4 uin = *reinterpret_cast<const u4*>(ws + (ubase + (uint32_t)(t >> 4) * 131072 + (t & 15) * 16));
  u2 uh = {0u, 0u};
  {
    const bool lo = (lane & ~8) == 0, hi = (lane | 8) == 63;
    const int qn = lo ? q - 1 : ((q + 1) & 255);
    if ((lo && q) || hi) uh = *reinterpret_cast<const u2*>(ws + (ubase + (uint32_t)(qn >> 3) * 131072 + lch * 128 + (qn & 7) * 16 + (lo ? 8 : 0)));
  }
  const u4 exq = reinterpret_cast<const u4*>(ws + OFF_EX)[lane];
  const int ch = w & 1;
  f4 spec0 = ZERO4; uint16_t dsk0 = 0;
  if (ROLE == 0) {
    spec0 = *reinterpret_cast<const f4*>(ws + (OFF_KR + (uint32_t)(d0 + (t & 1)) * KRP + 1760));
    dsk0 = dskip[d0 + (t & 1)];
  }
  const char* kr = ws + OFF_KR + (uint32_t)(d0 + ch) * KRP;
  const uint32_t ko = (uint32_t)(((15 - idx) & 1) * KRH + ((15 - idx) & ~1) * 2);
  u4 T[9];
  u4 kc = {0u, 0u, 0u, 0u};
  const int kch = t >> 8, ksh = (t >> 6) & 3;
  if (lane < 38) kc = ldg_a4(ws + (OFF_KR + (uint32_t)(d0 + kch) * KRP + (ksh & 1) * KRH + (ksh >> 1) * 4 + lane * 16));
  if (ROLE >= 2) {
    const uint32_t k2 = ko + (g >= 2 ? 192 : 0) + 16 * (g & 1);
    if (ROLE == 3) {
#pragma unroll
      for (int o = 0; o < 4; ++o) T[5 + o] = ldg_a4(kr + (k2 + 2 * 688 + 32 * (3 - o)));
    }
#pragma unroll
    for (int s = 0; s < 5; ++s) T[s] = ldg_a4(kr + (k2 + 2 * (ROLE == 2 ? 304 : 496) + 32 * s));
  }
  {
    int off, nz;
    if (ROLE == 0) {
      off = lane < 34 ? UA0 - 544 + 16 * lane : (lane < 36 ? UA0 + 4096 + 256 - 34 * 16 + 16 * lane : (lane == 36 ? B90 - 32 : B70 - 32));
      nz = 38;
    } else if (ROLE == 1) { off = U20 - 512 + 16 * lane; nz = 32; }
    else if (ROLE == 2) { off = B30 - 1040 + 16 * lane; nz = 64; }
    else { off = B50 - 1024 + 16 * lane; nz = 63; }
    if (lane < nz) *reinterpret_cast<u4*>(smem + ch * CV_CH + off) = (u4){0u, 0u, 0u, 0u};
  }
  const int EU = max((int)(wave_umax(umax(umax(exq[0], exq[1]), umax(exq[2], exq[3]))) >> 16) - 512, -110);
  const float su = exp2i(11 - EU);
  {
    char* cb = smem + lch * CV_CH;
    uint32_t e[8];
#pragma unroll
    for (int i = 0; i < 4; ++i) e[2 + i] = cvt_u(uin[i], su);
    *reinterpret_cast<u4*>(cb + UA0 + 16 * q + 16 * (q >> 4)) = (u4){e[2], e[3], e[4], e[5]};
    const bool last = (ROLE == 3) && (q == 255);
    {
      const int lp = ((lane & 7) ? lane - 1 : lane - 9) * 4, ln = ((lane & 7) != 7 ? lane + 1 : lane + 9) * 4;
      const uint32_t h0 = cvt_u(uh.x, su), h1 = cvt_u(uh.y, su);
      const bool lo = (lane & ~8) == 0, hi = (lane | 8) == 63;
      e[0] = (uint32_t)__builtin_amdgcn_ds_bpermute(lp, (int)e[4]); e[1] = (uint32_t)__builtin_amdgcn_ds_bpermute(lp, (int)e[5]);
      e[6] = (uint32_t)__builtin_amdgcn_ds_bpermute(ln, (int)e[2]); e[7] = (uint32_t)__builtin_amdgcn_ds_bpermute(ln, (int)e[3]);
      if (lo) { e[0] = h0; e[1] = h1; }
      if (hi) { e[6] = h0; e[7] = h1; }
    }
    if (ROLE < 3 || w < 7 || last) {
      if (last) { e[0] = 0; e[1] = 0; }
      if (last) { e[2] = 0; e[3] = 0; e[4] = 0; e[5] = 0; }
      const int qq = last ? -1 : q;
      uint32_t o[7];
#pragma unroll
      for (int i = 0; i < 7; ++i) o[i] = shr16(e[i], e[i + 1]);
      u4 v;
      if (!last) *reinterpret_cast<u4*>(cb + U20 + 16 * qq) = (u4){e[1], e[2], e[3], e[4]};
#pragma unroll
      for (int i = 0; i < 4; ++i) v[i] = pkadd_h(e[2 + i], pkadd_h(o[1 + i], o[2 + i]));
      *reinterpret_cast<u4*>(cb + B30 + 16 * qq) = v;
      if (ROLE < 3 || last) {
#pragma unroll
        for (int i = 0; i < 4; ++i) v[i] = pkadd_h(v[i], pkadd_h(e[1 + i], e[3 + i]));
        *reinterpret_cast<u4*>(cb + B50 + 16 * qq) = v;
        if (ROLE < 2 || last) {
#pragma unroll
          for (int i = 0; i < 4; ++i) v[i] = pkadd_h(v[i], pkadd_h(o[i], o[3 + i]));
          *reinterpret_cast<u4*>(cb + B70 + 16 * qq) = v;
#pragma unroll
          for (int i = 0; i < 4; ++i) v[i] = pkadd_h(v[i], pkadd_h(e[i], e[4 + i]));
          if (qq < 126) *reinterpret_cast<u4*>(cb + B90 + 16 * qq) = v;
        }
      }
    }
  }
  if (lane < 38) *reinterpret_cast<u4*>(smem + kch * CV_CH + KC0 + ksh * 704 + lane * 16) = kc;
  if (ROLE == 3) *reinterpret_cast<u4*>(smem + 2 * CV_CH + (t - 384) * 16) = uin;
  lds_barrier();
  uint32_t u01 = 0;
  if (ROLE == 0) u01 = *reinterpret_cast<const uint32_t*>(smem + lch * CV_CH + UA0);
  char* cb = smem + ch * CV_CH;
  const int tq = (lane >> 2) & 3, tp = lane & 3;
  if (ROLE < 2) {
    f4 acc[4] = {ZERO4, ZERO4, ZERO4, ZERO4};
    const char* b1 = cb + UA0 + idx * 272 + g * 16;
    const char* b2 = b1 + (g >= 2 ? 16 : 0);
    {
      const char* tb = cb + KC0 + ((15 - idx) & 3) * 704 + ((15 - idx) & ~3) * 2 + 16 * g;
#pragma unroll
      for (int s = 0; s < 9; ++s) {
        u2 lo = *reinterpret_cast<const u2*>(tb + 64 * s);
        SCHED_FENCE();
        u2 hi = *reinterpret_cast<const u2*>(tb + 64 * s + 8);
        SCHED_FENCE();
        T[s] = (u4){lo.x, lo.y, hi.x, hi.y};
      }
    }
    u4 U[12];
#pragma unroll
    for (int n = 0; n < 12; ++n) {
      if (n == 0) {
#pragma unroll
        for (int z = 0; z < 2; ++z) { const int kap = 16 * ROLE - 256 + 32 * z; U[z] = lds16(b1 + 2 * kap + 16 * (kap >> 7)); }
      }
      if (n + 2 < 12) {
        const int kap = 16 * ROLE - 256 + 32 * (n + 2);
        U[n + 2] = lds16(((ROLE && ((n + 2) & 3) == 3) ? b2 : b1) + 2 * kap + 16 * (kap >> 7));
      }
#pragma unroll
      for (int cc = 0; cc < 4; ++cc)
        if (n - cc >= 0 && n - cc < 9) acc[cc] = mfma_h(T[n - cc], U[n], acc[cc]);
      SCHED_FENCE();
    }
    if (ROLE == 0 && t < 2) {
      *reinterpret_cast<f4*>(smem + 2 * CV_CH + 2048 + t * 32) = spec0;
      *reinterpret_cast<float*>(smem + 2 * CV_CH + 2048 + t * 32 + 16) = bf2f(dsk0);
    }
    lds_barrier();
#pragma unroll
    for (int cc = 0; cc < 4; ++cc)
      *reinterpret_cast<f4*>(cb + P0O + idx * 528 + (16 * (ROLE + 2 * cc) + 4 * g) * 4) = acc[cc];
  } else if (ROLE == 2) {
    f4 acc[7];
#pragma unroll
    for (int i = 0; i < 7; ++i) acc[i] = ZERO4;
    const char* vb = cb + (g >= 2 ? U20 : B30) + 8 * (112 * tp - 64 + 8 * (g & 1) + tq);
    u4 V[11];
    V[0] = lds_tr2(vb, 32); V[1] = lds_tr2(vb + 128, 32);
#pragma unroll
    for (int m = 0; m < 11; ++m) {
      if (m + 2 < 11) V[m + 2] = lds_tr2(vb + 128 * (m + 2), 32);
#pragma unroll
      for (int n = 0; n < 5; ++n)
        if (m - n >= 0 && m - n < 7) acc[m - n] = mfma_h(V[m], T[n], acc[m - n]);
      SCHED_FENCE();
    }
    lds_barrier();
#pragma unroll
    for (int c = 0; c < 7; ++c)
      *reinterpret_cast<f4*>(cb + P1O + 16 * (64 + 112 * g + 16 * c + idx)) = acc[c];
  } else {
    f4 acc[6], acc3[4];
    const char* v3 = cb + (g >= 2 ? B70 - 16 : B90 - 32) + 32 * (8 * (g & 1) + tq) + 8 * tp;
    const char* vb = cb + (g >= 2 ? B30 - 8 : B50) + 16 * (96 * (tp >> 1) - 64 + 8 * (g & 1) + tq) + 8 * (tp & 1);
#pragma unroll
    for (int i = 0; i < 4; ++i) acc3[i] = ZERO4;
    u4 V[14];
    V[0] = lds_tr2(v3, 128); V[1] = lds_tr2(v3 + 512, 128);
#pragma unroll
    for (int m = 0; m < 4; ++m) {
      V[m + 2] = (m < 2) ? lds_tr2(v3 + 512 * (m + 2), 128) : lds_tr2(vb + 256 * (m - 2), 64);
#pragma unroll
      for (int c = m; c < 4; ++c) acc3[c] = mfma_h(V[m], T[5 + c - m], acc3[c]);
      SCHED_FENCE();
    }
#pragma unroll
    for (int i = 0; i < 6; ++i) acc[i] = ZERO4;
#pragma unroll
    for (int m = 0; m < 10; ++m) {
      if (m + 2 < 10) V[4 + m + 2] = lds_tr2(vb + 256 * (m + 2), 64);
#pragma unroll
      for (int n = 0; n < 5; ++n)
        if (m - n >= 0 && m - n < 6) acc[m - n] = mfma_h(V[4 + m], T[n], acc[m - n]);
      SCHED_FENCE();
    }
    lds_barrier();
    int t3 = TID; asm volatile("" : "+v"(t3));
    const int idx = t3 & 15, g = (t3 >> 4) & 3;
#pragma unroll
    for (int c = 0; c < 6; ++c)
      *reinterpret_cast<f4*>(cb + P2O + 16 * p2sw(2 * (64 + 96 * (g >> 1) + 16 * (c & 1) + idx) + (g & 1)) + 1024 * (c >> 1)) = acc[c];
    __builtin_amdgcn_fence(__ATOMIC_RELEASE, "wavefront");
    __builtin_amdgcn_wave_barrier();
    __builtin_amdgcn_fence(__ATOMIC_ACQUIRE, "wavefront");
#pragma unroll
    for (int c = 0; c < 4; ++c) {
      f4* p = reinterpret_cast<f4*>(cb + P2O + 16 * p2sw(4 * (64 + idx) + g) + 1024 * c);
      *p = *p + acc3[c];
    }
  }
  int t2 = TID; asm volatile("" : "+v"(t2));
  const int lch2 = (t2 >> 3) & 1;
  const uint32_t uoff2 = ubase + (uint32_t)(t2 >> 4) * 131072 + (t2 & 15) * 16;
  const u4 uraw = (ROLE == 3) ? lds16(smem + 2 * CV_CH + (t2 - 384) * 16) : uin;
  const f4 spec = ldsf4(smem + 2 * CV_CH + 2048 + lch2 * 32);
  const float dsk = *reinterpret_cast<const float*>(smem + 2 * CV_CH + 2048 + lch2 * 32 + 16);
  lds_barrier();
  {
    const int lch = lch2, q = (t2 >> 4) * 8 + (t2 & 7);
    const uint32_t uoff = uoff2;
    const char* eb = smem + lch * CV_CH;
    f4 y0 = ldsf4(eb + P0O + 32 * q + 16 * (q >> 4)), y1 = ldsf4(eb + P0O + 32 * q + 16 * (q >> 4) + 16);
    if (ROLE > 0 || w >= 1) { y0 += ldsf4(eb + P1O + 32 * q); y1 += ldsf4(eb + P1O + 32 * q + 16); }
    if (ROLE > 0) { const int ps = p2sw(2 * q); y0 += ldsf4(eb + P2O + 16 * ps); y1 += ldsf4(eb + P2O + 16 * (ps ^ 1)); }
    if (ROLE == 0) {
      h2 uu = __builtin_bit_cast(h2, u01);
      if (q == 31) y1[3] += spec[0] * (float)uu.x;
      if (q == 63) { y1[2] += spec[1] * (float)uu.x; y1[3] += spec[1] * ((float)uu.x + (float)uu.y); }
    }
    if (ROLE >= 2 || (ROLE == 1 && q == 127)) {
      const char* bp = eb + B70 + 16 * (q - 128);
      h2 a = *reinterpret_cast<const h2*>(bp - 4);
      h8 bq = *reinterpret_cast<const h8*>(bp);
      y0[0] += spec[2] * (float)a.y; y0[1] += spec[2] * (float)bq[0]; y0[2] += spec[2] * (float)bq[1]; y0[3] += spec[2] * (float)bq[2];
      y1[0] += spec[2] * (float)bq[3]; y1[1] += spec[2] * (float)bq[4]; y1[2] += spec[2] * (float)bq[5]; y1[3] += spec[2] * (float)bq[6];
    }
    const float sc = exp2i(EU - 11);
    y0 = y0 * sc * spec[3]; y1 = y1 * sc * spec[3];
    u4 o;
#pragma unroll
    for (int i = 0; i < 4; ++i) {
      uint32_t yb = (i < 2) ? pk_bf(y0[2 * i], y0[2 * i + 1]) : pk_bf(y1[2 * i - 4], y1[2 * i - 3]);
      uint32_t sb = pk_bf(bf_lo(uraw[i]) * dsk, bf_hi(uraw[i]) * dsk);
      o[i] = pk_bf(bf_lo(yb) + bf_lo(sb), bf_hi(yb) + bf_hi(sb));
    }
    st16(mk_rsrc(ws), uoff + (OFF_OT - OFF_UT), o);
  }
}

DEV void cv_block(int cbi, char* smem, const uint16_t* __restrict__ dskip, char* ws) {
  const int w = __builtin_amdgcn_readfirstlane(TID >> 6);
  if (w < 2) cv_body<0>(cbi, w, smem, dskip, ws);
  else if (w < 4) cv_body<1>(cbi, w, smem, dskip, ws);
  else if (w < 6) cv_body<2>(cbi, w, smem, dskip, ws);
  else cv_body<3>(cbi, w, smem, dskip, ws);
}

DEV void k2_body(const uint16_t* __restrict__ dskip, char* ws) {
  __shared__ __attribute__((aligned(16))) char smem[SMEM2];
  const int b = BID;
  if (b >= 512) a2_block(b - 512, smem, ws);
  else cv_block(b, smem, dskip, ws);
}
extern "C" __global__ __launch_bounds__(512, 6) void gs_k2(const uint16_t* __restrict__ dskip, char* ws) { k2_body(dskip, ws); }

extern "C" __global__ __launch_bounds__(512) void gs_k3(char* ws, uint32_t* __restrict__ out) {
  __shared__ __attribute__((aligned(16))) char smem[SMEM3];
  const int b = BID, t = TID, w = __builtin_amdgcn_readfirstlane(t >> 6), lane = t & 63, idx = lane & 15, g = lane >> 4;
  char* tab = smem;
  char* T = smem + 10240;
  const rsrc_t ro = mk_rsrc(out);
  uint32_t er;
  if (w < 4) {
    const u4* Z2 = reinterpret_cast<const u4*>(ws + OFF_Z2) + ((b * 8 + 2 * g) * 512 + 64 * w + 4 * idx) / 4;
    u4 L00 = NTL(Z2), L01 = NTL(Z2 + 64), L10 = NTL(Z2 + 128), L11 = NTL(Z2 + 192);
    const u4 A0 = reinterpret_cast<const u4*>(TAB3)[lane], A1 = reinterpret_cast<const u4*>(TAB3)[64 + lane];
    const u4 exq = reinterpret_cast<const u4*>(ws + OFF_EX)[lane], ewq = reinterpret_cast<const u4*>(ws + OFF_EW)[lane];
    uint32_t* tp = reinterpret_cast<uint32_t*>(T) + g * 648 + (4 * (idx & 3)) * 20 + 4 * w + (idx >> 2);
#pragma unroll
    for (int tt = 0; tt < 4; ++tt) {
      u4 B = {L00[tt], L01[tt], L10[tt], L11[tt]};
      f4 d0 = mfma_h(A0, B, ZERO4), d1 = mfma_h(A1, B, ZERO4);
      tp[tt * 20] = pk_h(d0[0], d0[1]); tp[tt * 20 + 320] = pk_h(d0[2], d0[3]);
      tp[tt * 20 + 4 * 648] = pk_h(d1[0], d1[1]); tp[tt * 20 + 4 * 648 + 320] = pk_h(d1[2], d1[3]);
    }
    er = red_exps(exq, ewq);
  } else {
    const int tt = t - 256, r4 = tt & 15, cg = tt >> 4;
    const u2* OT = reinterpret_cast<const u2*>(ws + (OFF_OT + (uint32_t)(b >> 3) * 131072 + (uint32_t)(128 * (b & 7) + 8 * cg) * 128)) + r4;
    u2 o[8];
#pragma unroll
    for (int i = 0; i < 8; ++i) o[i] = NTL(OT + 16 * i);
    const u4 c0 = reinterpret_cast<const u4*>(TAB3)[128 + tt], c1 = reinterpret_cast<const u4*>(TAB3)[384 + tt];
    const u4 exq = reinterpret_cast<const u4*>(ws + OFF_EX)[lane], ewq = reinterpret_cast<const u4*>(ws + OFF_EW)[lane];
    reinterpret_cast<u4*>(tab)[128 + tt] = c0;
    reinterpret_cast<u4*>(tab)[384 + tt] = c1;
    char* bp = smem + 31744 + (4 * r4) * 256 + (cg ^ r4) * 16;
#pragma unroll
    for (int h = 0; h < 2; ++h) {
      u4 lo, hi;
#pragma unroll
      for (int i = 0; i < 4; ++i) {
        lo[i] = (o[2 * i][h] & 0xffffu) | (o[2 * i + 1][h] << 16);
        hi[i] = (o[2 * i][h] >> 16) | (o[2 * i + 1][h] & 0xffff0000u);
      }
      *reinterpret_cast<u4*>(bp + (2 * h) * 256) = lo; *reinterpret_cast<u4*>(bp + (2 * h + 1) * 256) = hi;
    }
    er = red_exps(exq, ewq);
  }
  lds_barrier();
#pragma unroll
  for (int i = 0; i < 2; ++i) {
    const int row = (t >> 4) + 32 * i;
    st16(ro, (uint32_t)(64 * (b >> 3) + row) * 4096 + 2048 + (b & 7) * 256 + (t & 15) * 16, lds16(smem + 31744 + row * 256 + ((t & 15) ^ (row >> 2)) * 16));
  }
  {
    const char* tl = tab + lane * 16;
    const char* tp = T + (w * 648 + idx * 20 + 4 * g) * 4;
    const u4 f2r = lds16(tl + 2048), f2i = lds16(tl + 3072);
    const int eo = 5 + (int)(er & 0xffffu) + (int)(er >> 16) - 1024;
    const float s1 = exp2i(eo >> 1), s2 = exp2i(eo - (eo >> 1));
    f4 yr[2], yi[2];
#pragma unroll
    for (int n3 = 0; n3 < 2; ++n3) {
      u4 d = lds16(tp + n3 * 1280);
      f4 re = mfma_h(d, n3 ? lds16(tl + 4096) : f2r, ZERO4), im = mfma_h(d, n3 ? lds16(tl + 5120) : f2i, ZERO4);
      u4 e = tw_pack(re, im, ldsf4(tl + (6 + 2 * n3) * 1024), ldsf4(tl + (7 + 2 * n3) * 1024));
      yr[n3] = mfma_h(e, f2r, ZERO4) * s1 * s2; yi[n3] = mfma_h(e, f2i, ZERO4) * s1 * (-s2);
    }
    u4 o0, o1;
#pragma unroll
    for (int r = 0; r < 2; ++r) {
      o0[2 * r] = pk_bf(yr[0][r], yi[0][r]);
      o0[2 * r + 1] = pk_bf(yr[1][r], yi[1][r]);
      o1[2 * r] = pk_bf(yr[0][r + 2], yi[0][r + 2]);
      o1[2 * r + 1] = pk_bf(yr[1][r + 2], yi[1][r + 2]);
    }
    char* rp = T + w * (648 * 4);
    *reinterpret_cast<u4*>(rp + idx * 144 + g * 32) = o0; *reinterpret_cast<u4*>(rp + idx * 144 + g * 32 + 16) = o1;
    __builtin_amdgcn_fence(__ATOMIC_RELEASE, "wavefront");
    __builtin_amdgcn_wave_barrier();
    __builtin_amdgcn_fence(__ATOMIC_ACQUIRE, "wavefront");
    const uint32_t oo = (uint32_t)(b + 256 * w) * 4096 + lane * 16;
    st16(ro, oo, lds16(rp + (lane >> 3) * 144 + (lane & 7) * 16));
    st16(ro, oo + 1024, lds16(rp + (8 + (lane >> 3)) * 144 + (lane & 7) * 16));
  }
}

#if defined(__HIP_DEVICE_COMPILE__)
#pragma clang attribute pop
#endif

#define GETPTRS \
  const uint32_t* x = (const uint32_t*)d_in[0]; \
  const uint16_t* wr = (const uint16_t*)d_in[1]; \
  const uint16_t* wi = (const uint16_t*)d_in[2]; \
  const uint32_t* u = (const uint32_t*)d_in[3]; \
  const uint16_t* lar = (const uint16_t*)d_in[4]; \
  const uint16_t* aim = (const uint16_t*)d_in[5]; \
  const uint16_t* cre = (const uint16_t*)d_in[6]; \
  const uint16_t* cim = (const uint16_t*)d_in[7]; \
  const uint16_t* logdt = (const uint16_t*)d_in[8]; \
  const uint16_t* dskip = (const uint16_t*)d_in[9]; \
  char* ws = (char*)d_ws;

extern "C" void kernel_launch(void* const* d_in, const int* in_sizes, int n_in, void* d_out, int out_size,
                              void* d_ws, size_t ws_size, hipStream_t stream) {
  GETPTRS
  gs_k1<<<768, 512, 0, stream>>>(x, wr, wi, u, lar, aim, cre, cim, logdt, ws);
  gs_k2<<<768, 512, 0, stream>>>(dskip, ws);
  gs_k3<<<256, 512, 0, stream>>>(ws, (uint32_t*)d_out);
}
```
